```python
import torch

def _softmax(x, axis=-1):
    return torch.softmax(x, dim=axis)
def _gelu(x):
    return torch.nn.functional.gelu(x, approximate="tanh")
def _silu(x):
    return torch.nn.functional.silu(x)
def _sigmoid(x):
    return torch.sigmoid(x)
def _layernorm(x, axis=-1, eps=1e-5):
    m = x.mean(dim=axis, keepdim=True)
    v = x.var(dim=axis, keepdim=True, unbiased=False)
    return (x - m) / torch.sqrt(v + eps)
def _scatter_add(tgt, idx, src, axis=0):
    return tgt.clone().index_add_(axis, idx, src)
def _segment_sum(src, seg, n):
    out = torch.zeros((n,) + src.shape[1:], dtype=src.dtype)
    return out.index_add_(0, seg, src)
def _pad(x, pw):
    flat = tuple(v for pair in reversed(pw) for v in pair)
    return torch.nn.functional.pad(x, flat)

kernel_name = 'gated_mix__gqa_block_cached__dense_activation_relu__decode'

def setup_inputs():

    _w = (lambda a: (lambda t: t.to(torch.bfloat16) if t.is_floating_point() else t)(torch.as_tensor(a)))
    def _A_setup():
        B, Sq, D = 4, 1, 448
        Hq, Hkv, Sk, Dh = 16, 8, 8192, 256
        assert Hq % Hkv == 0, (Hq, Hkv)
        return {
            "x": _w((lambda *s: torch.randn(*s, dtype=torch.bfloat16) * 0.05)(B, Sq, D)),
            "k_cache": _w((lambda *s: torch.randn(*s, dtype=torch.bfloat16) * 6.4)(B, Hkv, Sk, Dh)),
            "v_cache": _w((lambda *s: torch.randn(*s, dtype=torch.bfloat16) * 1.6)(B, Hkv, Sk, Dh)),
            "wq": _w((lambda *s: torch.randn(*s, dtype=torch.bfloat16) * 6.4)(D, Hq * Dh) * (D ** -0.5)),
            "wo": _w((lambda *s: torch.randn(*s, dtype=torch.bfloat16) * 1.6)(Hq * Dh, D) * ((Hq * Dh) ** -0.5)),
        }
    _A_in = _A_setup()

    def _B_setup():
        B, S, D, Dff = 4, 1, 448, 1792
        return {
            "x": _w((lambda *s: torch.randn(*s, dtype=torch.bfloat16) * 0.05)(B, S, D)),
            "w1": _w((lambda *s: torch.randn(*s, dtype=torch.bfloat16) * 0.05)(D, Dff)),
            "w2": _w((lambda *s: torch.randn(*s, dtype=torch.bfloat16) * 0.05)(Dff, D)),
        }
    _B_in = _B_setup()

    return {
        "A_x": _A_in["x"],
        "A_k_cache": _A_in["k_cache"],
        "A_v_cache": _A_in["v_cache"],
        "A_wq": _A_in["wq"],
        "A_wo": _A_in["wo"],
        "B_x": _B_in["x"],
        "B_w1": _B_in["w1"],
        "B_w2": _B_in["w2"],
        "gate_w": _w((lambda *s: torch.randn(*s, dtype=torch.bfloat16) * 0.05)(448) * 16.0),
    }

def reference(A_x, A_k_cache, A_v_cache, A_wq, A_wo, B_x, B_w1, B_w2, gate_w):
    def _A_ref(x, k_cache, v_cache, wq, wo):
        B, Sq, D = x.shape
        Hq, Dh = 16, 256
        Hkv = k_cache.shape[1]
        rep = Hq // Hkv
        q = (lambda x, *d: x.permute(*d))((x @ wq).reshape(B, Sq, Hq, Dh), 0, 2, 1, 3)
        k = (lambda x, n, axis: torch.repeat_interleave(x, n, dim=axis))(k_cache, rep, axis=1)
        v = (lambda x, n, axis: torch.repeat_interleave(x, n, dim=axis))(v_cache, rep, axis=1)
        scores = torch.einsum("bhqd,bhkd->bhqk", q, k) * (Dh ** -0.5)
        p = _softmax(scores, axis=-1)
        o = torch.einsum("bhqk,bhkd->bhqd", p, v)
        return (lambda x, *d: x.permute(*d))(o, 0, 2, 1, 3).reshape(B, Sq, Hq * Dh) @ wo

    def _B_ref(x, w1, w2):
        h = torch.einsum("bsd,df->bsf", x, w1)
        zero = torch.zeros_like(h)
        h = torch.maximum(h, zero)
        return torch.einsum("bsf,fd->bsd", h, w2)

    x = A_x
    g = _sigmoid((x * gate_w).sum(-1, keepdims=True))
    a = _A_ref(x, A_k_cache, A_v_cache, A_wq, A_wo)
    b = _B_ref(x, B_w1, B_w2)
    return g * a + (1 - g) * b
```

```cpp
#include <hip/hip_runtime.h>
#include <hip/hip_bf16.h>
#include <cstdint>

typedef unsigned v4u __attribute__((ext_vector_type(4)));
typedef float f32x4 __attribute__((ext_vector_type(4)));
typedef __bf16 bf16x2 __attribute__((ext_vector_type(2)));
typedef unsigned short u16;

constexpr int NB = 4, DM = 448, HQ = 16, HKV = 8, SK = 8192, DH = 256, DFF = 1792;
constexpr int NSPLIT = 8, KEYS_WG = SK / NSPLIT;
constexpr int NW = 8, KEYS_W = KEYS_WG / NW;

__device__ __forceinline__ float bf2f(u16 b) { return __builtin_bit_cast(float, (unsigned)b << 16); }
__device__ __forceinline__ float bflo(unsigned u) { return __builtin_bit_cast(float, u << 16); }
__device__ __forceinline__ float bfhi(unsigned u) { return __builtin_bit_cast(float, u & 0xffff0000u); }
__device__ __forceinline__ u16 f2bf(float f) { __hip_bfloat16 h = __float2bfloat16(f); return __builtin_bit_cast(u16, h); }
__device__ __forceinline__ float dot2(unsigned k, unsigned q, float acc) {
  return __builtin_amdgcn_fdot2_f32_bf16(__builtin_bit_cast(bf16x2, k), __builtin_bit_cast(bf16x2, q), acc, false);
}

constexpr size_t WS_Q = 0;
constexpr size_t WS_H = 65536;
constexpr size_t WS_PML = 131072;
constexpr size_t WS_PO = 196608;
constexpr size_t WS_Y = WS_PO + 4 * 16 * 8 * 256 * 4;

__global__ void __launch_bounds__(256) k_pre(const u16* x, const u16* wq, const u16* w1, unsigned char* ws) {
  __shared__ float xs[NB * DM];
  __shared__ float red[32][NB][64];
  const int t = threadIdx.x, blk = blockIdx.x;
  for (int i = t; i < NB * DM; i += 256) xs[i] = bf2f(x[i]);
  __syncthreads();
  const bool isq = blk < 64;
  const u16* W = isq ? wq : w1;
  const int ld = isq ? HQ * DH : DFF;
  const int c0 = (isq ? blk : blk - 64) * 64;
  const int cg = t & 7, rg = t >> 3;
  float acc[NB][8];
  for (int b = 0; b < NB; ++b) for (int j = 0; j < 8; ++j) acc[b][j] = 0.f;
  for (int r = rg * 14; r < rg * 14 + 14; ++r) {
    v4u w = *(const v4u*)(W + (size_t)r * ld + c0 + cg * 8);
    float wf[8];
    for (int j = 0; j < 4; ++j) { wf[2 * j] = bflo(w[j]); wf[2 * j + 1] = bfhi(w[j]); }
    for (int b = 0; b < NB; ++b) { float xv = xs[b * DM + r]; for (int j = 0; j < 8; ++j) acc[b][j] = fmaf(xv, wf[j], acc[b][j]); }
  }
  for (int b = 0; b < NB; ++b) for (int j = 0; j < 8; ++j) red[rg][b][cg * 8 + j] = acc[b][j];
  __syncthreads();
  const int b = t >> 6, c = t & 63;
  float s = 0.f;
  for (int r = 0; r < 32; ++r) s += red[r][b][c];
  if (isq) ((u16*)(ws + WS_Q))[b * 4096 + c0 + c] = f2bf(s);
  else ((float*)(ws + WS_H))[b * DFF + c0 + c] = fmaxf(bf2f(f2bf(s)), 0.f);
}

__global__ void __launch_bounds__(512) k_attn(const u16* K, const u16* V, unsigned char* ws) {
  __shared__ __attribute__((aligned(16))) float sc[NW][2][KEYS_W];
  __shared__ __attribute__((aligned(16))) float mrg[NW][2][DH];
  __shared__ float ml[NW][2][2];
  const int blk = blockIdx.x, kvh = blk & 7, s = (blk >> 3) & 7, b = blk >> 6;
  const int tid = threadIdx.x, lane = tid & 63, w = tid >> 6, h = lane >> 5, c = lane & 31;
  const u16* q = (const u16*)(ws + WS_Q) + b * 4096 + kvh * 512;
  const v4u q0 = *(const v4u*)(q + c * 8), q1 = *(const v4u*)(q + 256 + c * 8);
  const size_t base = ((size_t)(b * HKV + kvh) * SK + s * KEYS_WG + w * KEYS_W) * DH;
  const v4u* kp = (const v4u*)(K + base) + lane;
  const v4u* vp = (const v4u*)(V + base) + lane;
  for (int i = 0; i < KEYS_W / 2; ++i) {
    v4u k = __builtin_nontemporal_load(kp + i * 64);
    float a0 = 0.f, a1 = 0.f;
    for (int j = 0; j < 4; ++j) { a0 = dot2(k[j], q0[j], a0); a1 = dot2(k[j], q1[j], a1); }
    for (int m = 16; m >= 1; m >>= 1) { a0 += __shfl_xor(a0, m); a1 += __shfl_xor(a1, m); }
    if (c == 0) { sc[w][0][2 * i + h] = a0; sc[w][1][2 * i + h] = a1; }
  }
  __syncthreads();
  const float C2 = 0.0625f * 1.4426950408889634f;
  float m[2], l[2];
  for (int hh = 0; hh < 2; ++hh) {
    float s0 = sc[w][hh][lane] * C2, s1 = sc[w][hh][lane + 64] * C2;
    float mx = fmaxf(s0, s1);
    for (int mm = 32; mm >= 1; mm >>= 1) mx = fmaxf(mx, __shfl_xor(mx, mm));
    float p0 = exp2f(s0 - mx), p1 = exp2f(s1 - mx);
    float ls = p0 + p1;
    for (int mm = 32; mm >= 1; mm >>= 1) ls += __shfl_xor(ls, mm);
    sc[w][hh][lane] = p0; sc[w][hh][lane + 64] = p1;
    m[hh] = mx; l[hh] = ls;
  }
  __syncthreads();
  float acc[2][8];
  for (int j = 0; j < 8; ++j) { acc[0][j] = 0.f; acc[1][j] = 0.f; }
  for (int i = 0; i < KEYS_W / 2; ++i) {
    v4u v = __builtin_nontemporal_load(vp + i * 64);
    float p0 = sc[w][0][2 * i + h], p1 = sc[w][1][2 * i + h];
    for (int j = 0; j < 4; ++j) {
      float lo = bflo(v[j]), hi = bfhi(v[j]);
      acc[0][2 * j] = fmaf(p0, lo, acc[0][2 * j]); acc[0][2 * j + 1] = fmaf(p0, hi, acc[0][2 * j + 1]);
      acc[1][2 * j] = fmaf(p1, lo, acc[1][2 * j]); acc[1][2 * j + 1] = fmaf(p1, hi, acc[1][2 * j + 1]);
    }
  }
  for (int hh = 0; hh < 2; ++hh) for (int j = 0; j < 8; ++j) acc[hh][j] += __shfl_xor(acc[hh][j], 32);
  if (h == 0) for (int hh = 0; hh < 2; ++hh) for (int j = 0; j < 8; ++j) mrg[w][hh][c * 8 + j] = acc[hh][j];
  if (lane == 0) { ml[w][0][0] = m[0]; ml[w][0][1] = l[0]; ml[w][1][0] = m[1]; ml[w][1][1] = l[1]; }
  __syncthreads();
  {
    const int hh = tid >> 8, d = tid & 255;
    float M = -INFINITY;
    for (int ww = 0; ww < NW; ++ww) M = fmaxf(M, ml[ww][hh][0]);
    float L = 0.f, o = 0.f;
    for (int ww = 0; ww < NW; ++ww) { float wt = exp2f(ml[ww][hh][0] - M); L = fmaf(wt, ml[ww][hh][1], L); o = fmaf(wt, mrg[ww][hh][d], o); }
    const int row = b * HQ + kvh * 2 + hh;
    ((float*)(ws + WS_PO))[((size_t)row * NSPLIT + s) * DH + d] = o;
    if (d == 0) { float* p = (float*)(ws + WS_PML) + ((size_t)row * NSPLIT + s) * 2; p[0] = M; p[1] = L; }
  }
}

__global__ void __launch_bounds__(512) k_post1(const u16* wo, const u16* w2, unsigned char* ws) {
  __shared__ float in[NB][64];
  __shared__ float red[8][NB][DM];
  const int t = threadIdx.x, blk = blockIdx.x;
  const bool iso = blk < 64;
  if (t < 256) {
    const int b = t >> 6, d = t & 63;
    if (iso) {
      const int head = blk >> 2, dd = (blk & 3) * 64 + d, row = b * HQ + head;
      const float* pml = (const float*)(ws + WS_PML) + (size_t)row * NSPLIT * 2;
      const float* po = (const float*)(ws + WS_PO) + (size_t)row * NSPLIT * DH + dd;
      float M = -INFINITY;
      for (int s = 0; s < NSPLIT; ++s) M = fmaxf(M, pml[2 * s]);
      float L = 0.f, o = 0.f;
      for (int s = 0; s < NSPLIT; ++s) { float wt = exp2f(pml[2 * s] - M); L = fmaf(wt, pml[2 * s + 1], L); o = fmaf(wt, po[s * DH], o); }
      in[b][d] = bf2f(f2bf(o / L));
    } else in[b][d] = ((const float*)(ws + WS_H))[b * DFF + (blk - 64) * 64 + d];
  }
  __syncthreads();
  const u16* W = (iso ? wo + (size_t)blk * 64 * DM : w2 + (size_t)(blk - 64) * 64 * DM);
  if (t < 448) {
    const int cg = t % 56, rg = t / 56;
    float acc[NB][8];
    for (int b = 0; b < NB; ++b) for (int j = 0; j < 8; ++j) acc[b][j] = 0.f;
    for (int r = rg * 8; r < rg * 8 + 8; ++r) {
      v4u w = *(const v4u*)(W + (size_t)r * DM + cg * 8);
      float wf[8];
      for (int j = 0; j < 4; ++j) { wf[2 * j] = bflo(w[j]); wf[2 * j + 1] = bfhi(w[j]); }
      for (int b = 0; b < NB; ++b) { float xv = in[b][r]; for (int j = 0; j < 8; ++j) acc[b][j] = fmaf(xv, wf[j], acc[b][j]); }
    }
    for (int b = 0; b < NB; ++b) for (int j = 0; j < 8; ++j) red[rg][b][cg * 8 + j] = acc[b][j];
  }
  __syncthreads();
  for (int i = t; i < NB * DM; i += 512) {
    const int b = i / DM, c = i % DM;
    float s = 0.f;
    for (int r = 0; r < 8; ++r) s += red[r][b][c];
    ((float*)(ws + WS_Y))[(size_t)blk * NB * DM + i] = s;
  }
}

__global__ void __launch_bounds__(448) k_post2(const u16* x, const u16* gw, const unsigned char* ws, u16* out) {
  __shared__ float red[7];
  const int b = blockIdx.x, t = threadIdx.x;
  float gp = bf2f(f2bf(bf2f(x[b * DM + t]) * bf2f(gw[t])));
  for (int m = 32; m >= 1; m >>= 1) gp += __shfl_xor(gp, m);
  if ((t & 63) == 0) red[t >> 6] = gp;
  __syncthreads();
  float z = 0.f;
  for (int i = 0; i < 7; ++i) z += red[i];
  const float g = 1.f / (1.f + __expf(-z));
  const float* Y = (const float*)(ws + WS_Y) + b * DM + t;
  float a = 0.f, bb = 0.f;
  for (int i = 0; i < 64; ++i) a += Y[(size_t)i * NB * DM];
  for (int i = 64; i < 92; ++i) bb += Y[(size_t)i * NB * DM];
  out[b * DM + t] = f2bf(g * a + (1.f - g) * bb);
}

extern "C" void kernel_launch(void* const* d_in, const int* in_sizes, int n_in, void* d_out, int out_size, void* d_ws, size_t ws_size, hipStream_t stream) {
  const u16* x = (const u16*)d_in[0];
  const u16* K = (const u16*)d_in[1];
  const u16* V = (const u16*)d_in[2];
  const u16* wq = (const u16*)d_in[3];
  const u16* wo = (const u16*)d_in[4];
  const u16* w1 = (const u16*)d_in[6];
  const u16* w2 = (const u16*)d_in[7];
  const u16* gw = (const u16*)d_in[8];
  unsigned char* ws = (unsigned char*)d_ws;
  k_pre<<<92, 256, 0, stream>>>(x, wq, w1, ws);
  k_attn<<<256, 512, 0, stream>>>(K, V, ws);
  k_post1<<<92, 512, 0, stream>>>(wo, w2, ws);
  k_post2<<<4, 448, 0, stream>>>(x, gw, ws, (u16*)d_out);
}
```

```cpp
#include <hip/hip_runtime.h>
#include <hip/hip_bf16.h>
#include <cstdint>

#define KV_NT 1
#define W_NT 1
#define CL 16
#define PACE0 0
#define PACE 0
#define STAMPS 0

typedef unsigned v4u __attribute__((ext_vector_type(4)));
typedef unsigned v3u __attribute__((ext_vector_type(3)));
typedef unsigned v2u __attribute__((ext_vector_type(2)));
typedef float f32x4 __attribute__((ext_vector_type(4)));
typedef __bf16 bf16x2 __attribute__((ext_vector_type(2)));
typedef unsigned short u16;
typedef unsigned long long u64;
#define GAS __attribute__((address_space(1)))
#define LAS __attribute__((address_space(3)))
#define RLX_AGENT __ATOMIC_RELAXED, __HIP_MEMORY_SCOPE_AGENT

constexpr int DM = 448, SK = 8192, DH = 256, DFF = 1792, QLD = 4096;
constexpr unsigned SENT = 0xFFFFFFFFu;
constexpr unsigned SPIN_CAP = 1u << 18;
constexpr int NCH = 128 / CL;
constexpr int WS_EPOCH = 0;
constexpr int WS_ML = 4096;
constexpr int WS_QG = 8192;
constexpr int WS_PO = WS_QG + 256 * 64 * 8;
constexpr int WS_Y = WS_PO + 256 * 512 * 4;
constexpr int WS_END = WS_Y + 256 * DM * 4;
constexpr size_t WS_STAMP = 16u << 20;

__device__ __forceinline__ float bflo(unsigned u) { return __builtin_bit_cast(float, u << 16); }
__device__ __forceinline__ float bfhi(unsigned u) { return __builtin_bit_cast(float, u & 0xffff0000u); }
__device__ __forceinline__ unsigned pkbf(float lo, float hi) {
  bf16x2 r; r[0] = (__bf16)lo; r[1] = (__bf16)hi; return __builtin_bit_cast(unsigned, r);
}
__device__ __forceinline__ float rbf(float f) { return bflo(pkbf(f, 0.f)); }
__device__ __forceinline__ float dot2(unsigned a, unsigned b, float acc) {
  return __builtin_amdgcn_fdot2_f32_bf16(__builtin_bit_cast(bf16x2, a), __builtin_bit_cast(bf16x2, b), acc, false);
}
__device__ __forceinline__ unsigned perm_lo(unsigned a, unsigned b) { return __builtin_amdgcn_perm(b, a, 0x05040100u); }
__device__ __forceinline__ unsigned perm_hi(unsigned a, unsigned b) { return __builtin_amdgcn_perm(b, a, 0x07060302u); }
template <int CTRL> __device__ __forceinline__ float dpp(float x) {
  return __builtin_bit_cast(float, __builtin_amdgcn_mov_dpp(__builtin_bit_cast(int, x), CTRL, 0xf, 0xf, true));
}
constexpr int XOR1 = 0xB1, XOR2 = 0x4E, XOR7 = 0x141, XOR8 = 0x128, ROR4 = 0x124, SHR4 = 0x114;
__device__ __forceinline__ float swp16_add(float a, float b) {
  auto s = __builtin_amdgcn_permlane16_swap(__float_as_uint(a), __float_as_uint(b), false, false);
  return __uint_as_float(s[0]) + __uint_as_float(s[1]);
}
__device__ __forceinline__ float swp32_add(float a, float b) {
  auto s = __builtin_amdgcn_permlane32_swap(__float_as_uint(a), __float_as_uint(b), false, false);
  return __uint_as_float(s[0]) + __uint_as_float(s[1]);
}
__device__ __forceinline__ float swp16_max(float a) {
  auto s = __builtin_amdgcn_permlane16_swap(__float_as_uint(a), __float_as_uint(a), false, false);
  return fmaxf(__uint_as_float(s[0]), __uint_as_float(s[1]));
}
__device__ __forceinline__ float swp32_max(float a) {
  auto s = __builtin_amdgcn_permlane32_swap(__float_as_uint(a), __float_as_uint(a), false, false);
  return fmaxf(__uint_as_float(s[0]), __uint_as_float(s[1]));
}
template <int CTRL> __device__ __forceinline__ float tr_dpp(float lo, float hi, bool sel) {
  const float t1 = lo + dpp<CTRL>(lo), t2 = hi + dpp<CTRL>(hi);
  return sel ? t2 : t1;
}
__device__ __forceinline__ float tr8(float (&a)[8], int lane) {
#pragma unroll
  for (int i = 0; i < 4; ++i) a[i] = swp32_add(a[i], a[i + 4]);
#pragma unroll
  for (int i = 0; i < 2; ++i) a[i] = swp16_add(a[i], a[i + 2]);
  return tr_dpp<XOR8>(a[0], a[1], lane & 8);
}
__device__ __forceinline__ void pair_dot(const v4u& ra, const v4u& rb, unsigned xp, float (&a)[8]) {
#pragma unroll
  for (int c = 0; c < 4; ++c) {
    a[2 * c] = dot2(perm_lo(ra[c], rb[c]), xp, a[2 * c]);
    a[2 * c + 1] = dot2(perm_hi(ra[c], rb[c]), xp, a[2 * c + 1]);
  }
}
__device__ __forceinline__ unsigned san(float f) { const unsigned u = __float_as_uint(f); return u == SENT ? 0x7FC00000u : u; }
__device__ __forceinline__ v4u ldkv(const GAS char* p) {
  return KV_NT ? __builtin_nontemporal_load((const GAS v4u*)p) : *(const GAS v4u*)p;
}
__device__ __forceinline__ v4u ldw(const GAS void* p) {
  return W_NT ? __builtin_nontemporal_load((const GAS v4u*)p) : *(const GAS v4u*)p;
}
#define WG_BARRIER() do { asm volatile("s_waitcnt lgkmcnt(0)" ::: "memory"); __builtin_amdgcn_s_barrier(); asm volatile("" ::: "memory"); } while (0)
#define DRAIN() asm volatile("s_waitcnt vmcnt(0)" ::: "memory")
#define STAMP(i) do {} while (0)
#define STAMP_OUT() do {} while (0)

struct Lds {
  unsigned q[256];
  float p[4][64];
  float mrg[4][512];
  float ml[4][4];
  float yred[4][DM];
  float qpart[4][64];
  unsigned cnt[4];
};

#define PIN16(a) asm volatile("" : "+v"(a[0]), "+v"(a[1]), "+v"(a[2]), "+v"(a[3]), "+v"(a[4]), "+v"(a[5]), "+v"(a[6]), "+v"(a[7]), \
                                   "+v"(a[8]), "+v"(a[9]), "+v"(a[10]), "+v"(a[11]), "+v"(a[12]), "+v"(a[13]), "+v"(a[14]), "+v"(a[15]))
#define PIN8(a) asm volatile("" : "+v"(a[0]), "+v"(a[1]), "+v"(a[2]), "+v"(a[3]), "+v"(a[4]), "+v"(a[5]), "+v"(a[6]), "+v"(a[7]))

template <bool REFILL>
__device__ __forceinline__ void chunk(v4u (&kv)[2 * CL], const GAS char* kn, const GAS char* vn, const v4u& q0, const v4u& q1,
                                      float& m, float& l, float (&acc)[16], LAS float* pw, int lane) {
  float x[2 * CL];
#pragma unroll
  for (int j = 0; j < CL; ++j) {
    float a0 = 0.f, a1 = 0.f;
#pragma unroll
    for (int k = 0; k < 4; ++k) { a0 = dot2(kv[j][k], q0[k], a0); a1 = dot2(kv[j][k], q1[k], a1); }
    x[2 * j] = a0; x[2 * j + 1] = a1;
    if (REFILL) {
      asm volatile("" : "+v"(x[2 * j]), "+v"(x[2 * j + 1]));
      if (PACE) asm volatile("s_waitcnt vmcnt(%0)" ::"i"(PACE - 1));
      kv[j] = ldkv(kn + j * 1024);
      __builtin_amdgcn_sched_barrier(0);
    }
  }
#pragma unroll
  for (int i = 0; i < CL; ++i) x[i] = swp16_add(x[i], x[i + CL]);
#pragma unroll
  for (int i = 0; i < CL / 2; ++i) x[i] = tr_dpp<XOR8>(x[i], x[i + CL / 2], lane & 8);
#pragma unroll
  for (int i = 0; i < CL / 4; ++i) x[i] = tr_dpp<XOR7>(x[i], x[i + CL / 4], lane & 4);
#pragma unroll
  for (int i = 0; i < CL / 8; ++i) x[i] = tr_dpp<XOR2>(x[i], x[i + CL / 8], lane & 2);
  float sc;
  if (CL == 16) sc = tr_dpp<XOR1>(x[0], x[1], lane & 1); else sc = x[0] + dpp<XOR1>(x[0]);
  sc = rbf(sc) * (0.0625f * 1.4426950408889634f);
  float cm = sc;
  if (CL == 16) cm = fmaxf(cm, dpp<XOR2>(cm));
  cm = fmaxf(cm, dpp<ROR4>(cm)); cm = fmaxf(cm, dpp<XOR8>(cm)); cm = swp16_max(cm); cm = swp32_max(cm);
  const float mn = fmaxf(m, cm);
  const float alpha = __builtin_amdgcn_exp2f(m - mn), p = __builtin_amdgcn_exp2f(sc - mn);
  l = fmaf(l, alpha, p); m = mn;
  pw[CL == 16 ? lane : lane >> 1] = p;
  const float al0 = dpp<0x00>(alpha), al1 = dpp<CL == 16 ? 0x55 : 0xAA>(alpha);
#pragma unroll
  for (int k = 0; k < 8; ++k) { acc[k] *= al0; acc[8 + k] *= al1; }
  asm volatile("s_waitcnt lgkmcnt(0)" ::: "memory");
  f32x4 pr[CL / 2];
#pragma unroll
  for (int u = 0; u < CL / 2; ++u) pr[u] = *(const LAS f32x4*)(pw + (lane >> 5) * (2 * CL) + 4 * u);
  __builtin_amdgcn_sched_barrier(0);
#pragma unroll
  for (int j = 0; j < CL; ++j) {
    const float p0 = pr[j >> 1][(j & 1) * 2], p1 = pr[j >> 1][(j & 1) * 2 + 1];
#pragma unroll
    for (int k = 0; k < 4; ++k) {
      const float lo = bflo(kv[CL + j][k]), hi = bfhi(kv[CL + j][k]);
      acc[2 * k] = fmaf(p0, lo, acc[2 * k]); acc[2 * k + 1] = fmaf(p0, hi, acc[2 * k + 1]);
      acc[8 + 2 * k] = fmaf(p1, lo, acc[8 + 2 * k]); acc[8 + 2 * k + 1] = fmaf(p1, hi, acc[8 + 2 * k + 1]);
    }
    if (REFILL) {
      PIN16(acc);
      if (PACE) asm volatile("s_waitcnt vmcnt(%0)" ::"i"(PACE - 1));
      kv[CL + j] = ldkv(vn + j * 1024);
      __builtin_amdgcn_sched_barrier(0);
    }
  }
  asm volatile("" ::: "memory");
}

__global__ void __launch_bounds__(512, 2) fused_kernel(const u16* __restrict__ x, const u16* __restrict__ Kc, const u16* __restrict__ Vc,
    const u16* __restrict__ wq, const u16* __restrict__ wo, const u16* __restrict__ w1, const u16* __restrict__ w2,
    const u16* __restrict__ gw, unsigned char* ws, u16* out) {
  __shared__ __attribute__((aligned(16))) Lds sm;
  LAS Lds* L = (LAS Lds*)&sm;
  const int tid = threadIdx.x, lane = tid & 63, wave = __builtin_amdgcn_readfirstlane(tid >> 6);
  const int blk = blockIdx.x, kvh = blk & 7, s = (blk >> 3) & 7, b = blk >> 6;
  const int j = kvh * 8 + s;
  const int gid = b * 64 + j;
  const __amdgpu_buffer_rsrc_t rs = __builtin_amdgcn_make_buffer_rsrc((void*)ws, 0, WS_END, 0x00020000);
  STAMP(0);
  if (wave >= 4) {
    const int w = wave - 4;
    const size_t base = ((size_t)((b * 8 + kvh) * SK + s * 1024 + w * 2 * CL)) * (DH * 2) + lane * 16;
    const GAS char* kp = (const GAS char*)Kc + base;
    const GAS char* vp = (const GAS char*)Vc + base;
    if (tid == 256) L->cnt[0] = 0u;
    WG_BARRIER();
    v4u kv[2 * CL];
#pragma unroll
    for (int i = 0; i < 2 * CL; ++i) {
      if (PACE0 && i >= PACE0) asm volatile("s_waitcnt vmcnt(%0)" ::"i"(PACE0 - 1));
      kv[i] = ldkv((i < CL ? kp : vp) + (i & (CL - 1)) * 1024);
      __builtin_amdgcn_sched_barrier(0);
    }
    STAMP(1);
    WG_BARRIER();
    STAMP(2);
    const v4u q0 = *(const LAS v4u*)(L->q + (lane & 31) * 4), q1 = *(const LAS v4u*)(L->q + 128 + (lane & 31) * 4);
    float m = -INFINITY, l = 0.f, acc[16];
#pragma unroll
    for (int k = 0; k < 16; ++k) acc[k] = 0.f;
    LAS float* pw = L->p[w];
    for (int t = 1; t < NCH; ++t) {
      kp += CL * 4096; vp += CL * 4096;
      chunk<true>(kv, kp, vp, q0, q1, m, l, acc, pw, lane);
    }
    chunk<false>(kv, kp, vp, q0, q1, m, l, acc, pw, lane);
    STAMP(3);
    float o[8];
#pragma unroll
    for (int k = 0; k < 8; ++k) o[k] = swp32_add(acc[k], acc[8 + k]);
    *(LAS f32x4*)(L->mrg[w] + lane * 8) = f32x4{o[0], o[1], o[2], o[3]};
    *(LAS f32x4*)(L->mrg[w] + lane * 8 + 4) = f32x4{o[4], o[5], o[6], o[7]};
    if (CL == 16) l += dpp<XOR2>(l);
    l += dpp<ROR4>(l); l += dpp<XOR8>(l); l = swp16_add(l, l); l = swp32_add(l, l);
    if (CL == 16) { if (lane < 2) { L->ml[w][2 * lane] = m; L->ml[w][2 * lane + 1] = l; } }
    else if ((lane & 0x3d) == 0) { L->ml[w][lane] = m; L->ml[w][lane + 1] = l; }
    WG_BARRIER();
    STAMP(4);
    if (w < 2) {
      float M = -INFINITY;
#pragma unroll
      for (int ww = 0; ww < 4; ++ww) M = fmaxf(M, L->ml[ww][2 * w]);
      float Ls = 0.f; f32x4 os = {0.f, 0.f, 0.f, 0.f};
#pragma unroll
      for (int ww = 0; ww < 4; ++ww) {
        const float wt = __builtin_amdgcn_exp2f(L->ml[ww][2 * w] - M);
        Ls = fmaf(wt, L->ml[ww][2 * w + 1], Ls);
        os += wt * *(const LAS f32x4*)(L->mrg[ww] + w * 256 + lane * 4);
      }
      const v4u ov = {san(os[0]), san(os[1]), san(os[2]), san(os[3])};
      __builtin_amdgcn_raw_buffer_store_b128(ov, rs, WS_PO + gid * 2048 + w * 1024 + lane * 16, 0, 16);
      if (lane == 0) __builtin_amdgcn_raw_buffer_store_b64(v2u{san(M), san(Ls)}, rs, WS_ML + gid * 16 + w * 8, 0, 16);
      STAMP(5);
    }
    WG_BARRIER();
    if (w < 2 && j < 56) {
      const int cg = 2 * j + w;
      v4u y;
      for (unsigned spins = 0;; ++spins) {
        y = __builtin_amdgcn_raw_buffer_load_b128(rs, WS_Y + (b * 64 + lane) * (DM * 4) + cg * 16, 0, 16);
        const bool ok = y[0] != SENT && y[1] != SENT && y[2] != SENT && y[3] != SENT;
        if (__all(ok) || spins > SPIN_CAP) break;
        __builtin_amdgcn_s_sleep(1);
      }
      STAMP(6);
      float r0 = swp32_add(__uint_as_float(y[0]), __uint_as_float(y[2])), r1 = swp32_add(__uint_as_float(y[1]), __uint_as_float(y[3]));
      float r = swp16_add(r0, r1);
      r += dpp<XOR8>(r); r += dpp<XOR7>(r); r += dpp<XOR2>(r); r += dpp<XOR1>(r);
      if ((lane & 15) == 0) out[b * DM + cg * 4 + (lane >> 4)] = (u16)pkbf(r, 0.f);
      STAMP(7);
    }
  } else {
    const int hw = wave;
    const GAS unsigned* x32 = (const GAS unsigned*)(x + b * DM);
    const int r = lane >> 3;
    unsigned xp[7];
#pragma unroll
    for (int k = 0; k < 7; ++k) xp[k] = x32[56 * hw + 7 * r + k];
    const GAS char* wqp = (const GAS char*)wq + (size_t)(112 * hw + 14 * r) * (QLD * 2) + (j * 64 + (lane & 7) * 8) * 2;
    v4u wr[14];
#pragma unroll
    for (int k = 0; k < 14; ++k) wr[k] = ldw(wqp + (size_t)k * (QLD * 2));
    const u64 ep = __hip_atomic_load((GAS u64*)(ws + WS_EPOCH + b * 256), RLX_AGENT);
    {
      const v4u sv = {SENT, SENT, SENT, SENT};
      const int off = hw < 2 ? WS_PO + gid * 2048 + hw * 1024 + lane * 16
                    : (tid < 240 ? WS_Y + gid * (DM * 4) + (tid - 128) * 16 : (tid == 240 ? WS_ML + gid * 16 : WS_END));
      __builtin_amdgcn_raw_buffer_store_b128(sv, rs, off, 0, 16);
    }
    __builtin_amdgcn_sched_barrier(0);
    WG_BARRIER();
    STAMP(1);
    float qa[8];
#pragma unroll
    for (int c = 0; c < 8; ++c) qa[c] = 0.f;
#pragma unroll
    for (int k = 0; k < 7; ++k) pair_dot(wr[2 * k], wr[2 * k + 1], xp[k], qa);
    L->qpart[hw][(lane & 7) * 8 + r] = tr8(qa, lane);
    const u64 tag = (ep + 1) << 16;
    STAMP(2);
    asm volatile("s_waitcnt vmcnt(0) lgkmcnt(0)" ::: "memory");
    unsigned arr = 0;
    if (lane == 0) arr = __hip_atomic_fetch_add(&L->cnt[0], 1u, __ATOMIC_RELAXED, __HIP_MEMORY_SCOPE_WORKGROUP);
    if (__builtin_amdgcn_readfirstlane(arr) == 3) {
      asm volatile("" ::: "memory");
      const float qv = (L->qpart[0][lane] + L->qpart[1][lane]) + (L->qpart[2][lane] + L->qpart[3][lane]);
      __hip_atomic_store((GAS u64*)(ws + WS_QG) + gid * 64 + lane, tag | (pkbf(qv, 0.f) & 0xffffu), RLX_AGENT);
    }
    STAMP(3);
    {
      const unsigned thi = (unsigned)(tag >> 32), tlo = (unsigned)tag;
      v4u g;
      for (unsigned spins = 0;; ++spins) {
        g = __builtin_amdgcn_raw_buffer_load_b128(rs, WS_QG + ((gid & ~7) + 2 * hw) * 512 + lane * 16, 0, 16);
        const bool ok = g[1] == thi && g[3] == thi && (g[0] & 0xffff0000u) == tlo && (g[2] & 0xffff0000u) == tlo;
        if (__all(ok) || spins > SPIN_CAP) break;
        __builtin_amdgcn_s_sleep(1);
      }
      L->q[hw * 64 + lane] = perm_lo(g[0], g[2]);
    }
    STAMP(4);
    WG_BARRIER();
    float z = 0.f;
#pragma unroll
    for (int k = 0; k < 4; ++k) {
      const int idx = lane + 64 * k;
      const bool in = idx < DM / 2;
      const unsigned xv = x32[in ? idx : 0], gv = ((const GAS unsigned*)gw)[in ? idx : 0];
      const float t = rbf(bflo(xv) * bflo(gv)) + rbf(bfhi(xv) * bfhi(gv));
      z += in ? t : 0.f;
    }
    z = swp32_add(z, z); z = swp16_add(z, z); z += dpp<XOR8>(z); z += dpp<XOR7>(z); z += dpp<XOR2>(z); z += dpp<XOR1>(z);
    float g = rbf(1.f / (1.f + __expf(-rbf(z))));
    float ya[8], yb[8];
#pragma unroll
    for (int c = 0; c < 8; ++c) { ya[c] = 0.f; yb[c] = 0.f; }
    const int cl = lane < 56 ? lane : 55;
    if (hw == 3 && s < 7) {
      const int u0 = (kvh * 7 + s) * 32, rr = lane >> 2;
      float ha[8];
#pragma unroll
      for (int c = 0; c < 8; ++c) ha[c] = 0.f;
      const GAS u16* w1p = (const GAS u16*)w1 + (size_t)(2 * rr) * DFF + u0 + (lane & 3) * 8;
      for (int h = 0; h < 2; ++h) {
        v4u ma[14]; unsigned xq[7];
#pragma unroll
        for (int k = 0; k < 7; ++k) {
          ma[2 * k] = ldw(w1p + (size_t)((7 * h + k) * 32) * DFF);
          ma[2 * k + 1] = ldw(w1p + (size_t)((7 * h + k) * 32 + 1) * DFF);
          xq[k] = x32[(7 * h + k) * 16 + rr];
        }
#pragma unroll
        for (int k = 0; k < 7; ++k) pair_dot(ma[2 * k], ma[2 * k + 1], xq[k], ha);
      }
      float hv = tr8(ha, lane);
      hv += dpp<SHR4>(hv);
      const float omg = rbf(1.f - g);
      hv = fmaxf(rbf(hv), 0.f);
      for (int c4 = 0; c4 < 4; ++c4) {
        v4u w2r[8];
#pragma unroll
        for (int u = 0; u < 8; ++u) w2r[u] = ldw((const GAS u16*)w2 + (size_t)(u0 + c4 * 8 + u) * DM + cl * 8);
#pragma unroll
        for (int u = 0; u < 8; ++u) {
          const float hu = __builtin_bit_cast(float, __builtin_amdgcn_readlane(__builtin_bit_cast(int, hv), u * 8 + 4 + c4));
#pragma unroll
          for (int c = 0; c < 4; ++c) { yb[2 * c] = fmaf(hu, bflo(w2r[u][c]), yb[2 * c]); yb[2 * c + 1] = fmaf(hu, bfhi(w2r[u][c]), yb[2 * c + 1]); }
        }
      }
#pragma unroll
      for (int c = 0; c < 8; ++c) yb[c] *= omg;
    }
    PIN8(yb);
    unsigned wp[8][8];
    {
      const GAS u16* wop = (const GAS u16*)wo + (size_t)(j * 64 + hw * 16) * DM + cl * 8;
#pragma unroll
      for (int i = 0; i < 8; ++i) {
        const v4u ra = ldw(wop + (2 * i) * DM), rb = ldw(wop + (2 * i + 1) * DM);
#pragma unroll
        for (int c = 0; c < 4; ++c) { wp[i][2 * c] = perm_lo(ra[c], rb[c]); wp[i][2 * c + 1] = perm_hi(ra[c], rb[c]); }
      }
#pragma unroll
      for (int i = 0; i < 8; ++i) PIN8(wp[i]);
    }
    asm volatile("" : "+v"(g));
    if (hw == 0) {
      for (unsigned spins = 0;; ++spins) {
        const u64 gv = __hip_atomic_load((GAS u64*)(ws + WS_QG) + (b * 64 + lane) * 64, RLX_AGENT);
        if (__all((gv & ~0xffffull) == tag) || spins > SPIN_CAP) break;
        __builtin_amdgcn_s_sleep(8);
      }
      if (j == 0 && lane == 0) __hip_atomic_store((GAS u64*)(ws + WS_EPOCH + b * 256), ep + 1, RLX_AGENT);
    }
    STAMP(5);
    WG_BARRIER();
    {
      const int pr = (gid & ~7) + (lane >> 3), head = s >> 2;
      v2u ov, mlv;
      for (unsigned spins = 0;; ++spins) {
        ov = __builtin_amdgcn_raw_buffer_load_b64(rs, WS_PO + pr * 2048 + (s * 64 + hw * 16 + (lane & 7) * 2) * 4, 0, 16);
        mlv = __builtin_amdgcn_raw_buffer_load_b64(rs, WS_ML + pr * 16 + head * 8, 0, 16);
        const bool ok = ov[0] != SENT && ov[1] != SENT && mlv[0] != SENT && mlv[1] != SENT;
        if (__all(ok) || spins > SPIN_CAP) break;
        __builtin_amdgcn_s_sleep(1);
      }
      STAMP(6);
      const float mp = __uint_as_float(mlv[0]);
      float M = fmaxf(mp, dpp<XOR8>(mp)); M = swp16_max(M); M = swp32_max(M);
      const float wt = __builtin_amdgcn_exp2f(mp - M);
      float Ls = wt * __uint_as_float(mlv[1]), o0 = wt * __uint_as_float(ov[0]), o1 = wt * __uint_as_float(ov[1]);
      Ls += dpp<XOR8>(Ls); o0 += dpp<XOR8>(o0); o1 += dpp<XOR8>(o1);
      Ls = swp16_add(Ls, Ls); o0 = swp16_add(o0, o0); o1 = swp16_add(o1, o1);
      Ls = swp32_add(Ls, Ls); o0 = swp32_add(o0, o0); o1 = swp32_add(o1, o1);
      const float inv = 1.f / Ls;
      const unsigned op = pkbf(o0 * inv, o1 * inv);
#pragma unroll
      for (int i = 0; i < 8; ++i) {
        const unsigned oi = (unsigned)__builtin_amdgcn_readlane((int)op, i);
#pragma unroll
        for (int c = 0; c < 8; ++c) ya[c] = dot2(wp[i][c], oi, ya[c]);
      }
      if (lane < 56) {
        f32x4 y0, y1;
#pragma unroll
        for (int c = 0; c < 4; ++c) { y0[c] = fmaf(g, ya[c], yb[c]); y1[c] = fmaf(g, ya[4 + c], yb[4 + c]); }
        *(LAS f32x4*)(L->yred[hw] + lane * 8) = y0;
        *(LAS f32x4*)(L->yred[hw] + lane * 8 + 4) = y1;
      }
    }
    STAMP(7);
    WG_BARRIER();
    if (tid < 112) {
      f32x4 y = *(const LAS f32x4*)(L->yred[0] + tid * 4);
#pragma unroll
      for (int ww = 1; ww < 4; ++ww) y += *(const LAS f32x4*)(L->yred[ww] + tid * 4);
      const v4u yv = {san(y[0]), san(y[1]), san(y[2]), san(y[3])};
      __builtin_amdgcn_raw_buffer_store_b128(yv, rs, WS_Y + gid * (DM * 4) + tid * 16, 0, 16);
    }
    STAMP(8);
  }
  STAMP_OUT();
}

extern "C" void kernel_launch(void* const* d_in, const int* in_sizes, int n_in, void* d_out, int out_size, void* d_ws, size_t ws_size, hipStream_t stream) {
  fused_kernel<<<256, 512, 0, stream>>>((const u16*)d_in[0], (const u16*)d_in[1], (const u16*)d_in[2], (const u16*)d_in[3], (const u16*)d_in[4],
                                        (const u16*)d_in[6], (const u16*)d_in[7], (const u16*)d_in[8], (unsigned char*)d_ws, (u16*)d_out);
}
```

```cpp
#include <hip/hip_runtime.h>
#include <hip/hip_bf16.h>
#include <cstdint>

#define K_NT 1
#define V_NT 0
#define W_NT 0
#define CL 16
#define PACE0 0
#define PACE 0
#define STAMPS 0

typedef unsigned v4u __attribute__((ext_vector_type(4)));
typedef unsigned v3u __attribute__((ext_vector_type(3)));
typedef unsigned v2u __attribute__((ext_vector_type(2)));
typedef float f32x4 __attribute__((ext_vector_type(4)));
typedef __bf16 bf16x2 __attribute__((ext_vector_type(2)));
typedef unsigned short u16;
typedef unsigned long long u64;
#define GAS __attribute__((address_space(1)))
#define LAS __attribute__((address_space(3)))
#define RLX_AGENT __ATOMIC_RELAXED, __HIP_MEMORY_SCOPE_AGENT

constexpr int DM = 448, SK = 8192, DH = 256, DFF = 1792, QLD = 4096;
constexpr unsigned SENT = 0xFFFFFFFFu;
constexpr unsigned SPIN_CAP = 1u << 18;
constexpr int NCH = 128 / CL;
constexpr int WS_EPOCH = 0;
constexpr int WS_ML = 4096;
constexpr int WS_QG = 8192;
constexpr int WS_PO = WS_QG + 256 * 64 * 8;
constexpr int WS_Y = WS_PO + 256 * 512 * 4;
constexpr int WS_END = WS_Y + 256 * DM * 4;
constexpr size_t WS_STAMP = 16u << 20;

__device__ __forceinline__ float bflo(unsigned u) { return __builtin_bit_cast(float, u << 16); }
__device__ __forceinline__ float bfhi(unsigned u) { return __builtin_bit_cast(float, u & 0xffff0000u); }
__device__ __forceinline__ unsigned pkbf(float lo, float hi) {
  bf16x2 r; r[0] = (__bf16)lo; r[1] = (__bf16)hi; return __builtin_bit_cast(unsigned, r);
}
__device__ __forceinline__ float rbf(float f) { return bflo(pkbf(f, 0.f)); }
__device__ __forceinline__ float dot2(unsigned a, unsigned b, float acc) {
  return __builtin_amdgcn_fdot2_f32_bf16(__builtin_bit_cast(bf16x2, a), __builtin_bit_cast(bf16x2, b), acc, false);
}
__device__ __forceinline__ unsigned perm_lo(unsigned a, unsigned b) { return __builtin_amdgcn_perm(b, a, 0x05040100u); }
__device__ __forceinline__ unsigned perm_hi(unsigned a, unsigned b) { return __builtin_amdgcn_perm(b, a, 0x07060302u); }
template <int CTRL> __device__ __forceinline__ float dpp(float x) {
  return __builtin_bit_cast(float, __builtin_amdgcn_mov_dpp(__builtin_bit_cast(int, x), CTRL, 0xf, 0xf, true));
}
constexpr int XOR1 = 0xB1, XOR2 = 0x4E, XOR7 = 0x141, XOR8 = 0x128, ROR4 = 0x124, SHR4 = 0x114;
__device__ __forceinline__ float swp16_add(float a, float b) {
  auto s = __builtin_amdgcn_permlane16_swap(__float_as_uint(a), __float_as_uint(b), false, false);
  return __uint_as_float(s[0]) + __uint_as_float(s[1]);
}
__device__ __forceinline__ float swp32_add(float a, float b) {
  auto s = __builtin_amdgcn_permlane32_swap(__float_as_uint(a), __float_as_uint(b), false, false);
  return __uint_as_float(s[0]) + __uint_as_float(s[1]);
}
__device__ __forceinline__ float swp16_max(float a) {
  auto s = __builtin_amdgcn_permlane16_swap(__float_as_uint(a), __float_as_uint(a), false, false);
  return fmaxf(__uint_as_float(s[0]), __uint_as_float(s[1]));
}
__device__ __forceinline__ float swp32_max(float a) {
  auto s = __builtin_amdgcn_permlane32_swap(__float_as_uint(a), __float_as_uint(a), false, false);
  return fmaxf(__uint_as_float(s[0]), __uint_as_float(s[1]));
}
template <int CTRL> __device__ __forceinline__ float tr_dpp(float lo, float hi, bool sel) {
  const float t1 = lo + dpp<CTRL>(lo), t2 = hi + dpp<CTRL>(hi);
  return sel ? t2 : t1;
}
__device__ __forceinline__ float tr8(float (&a)[8], int lane) {
#pragma unroll
  for (int i = 0; i < 4; ++i) a[i] = swp32_add(a[i], a[i + 4]);
#pragma unroll
  for (int i = 0; i < 2; ++i) a[i] = swp16_add(a[i], a[i + 2]);
  return tr_dpp<XOR8>(a[0], a[1], lane & 8);
}
__device__ __forceinline__ void pair_dot(const v4u& ra, const v4u& rb, unsigned xp, float (&a)[8]) {
#pragma unroll
  for (int c = 0; c < 4; ++c) {
    a[2 * c] = dot2(perm_lo(ra[c], rb[c]), xp, a[2 * c]);
    a[2 * c + 1] = dot2(perm_hi(ra[c], rb[c]), xp, a[2 * c + 1]);
  }
}
__device__ __forceinline__ unsigned san(float f) { const unsigned u = __float_as_uint(f); return u == SENT ? 0x7FC00000u : u; }
template <bool NT> __device__ __forceinline__ v4u ldkv(const GAS char* p) {
  return NT ? __builtin_nontemporal_load((const GAS v4u*)p) : *(const GAS v4u*)p;
}
__device__ __forceinline__ v4u ldw(const GAS void* p) {
  return W_NT ? __builtin_nontemporal_load((const GAS v4u*)p) : *(const GAS v4u*)p;
}
#define WG_BARRIER() do { asm volatile("s_waitcnt lgkmcnt(0)" ::: "memory"); __builtin_amdgcn_s_barrier(); asm volatile("" ::: "memory"); } while (0)
#define DRAIN() asm volatile("s_waitcnt vmcnt(0)" ::: "memory")
#define STAMP(i) do {} while (0)
#define STAMP_OUT() do {} while (0)

struct Lds {
  unsigned q[256];
  float p[4][64];
  float mrg[4][512];
  float ml[4][4];
  float yred[4][DM];
  float qpart[4][64];
  unsigned cnt[4];
};

#define PIN16(a) asm volatile("" : "+v"(a[0]), "+v"(a[1]), "+v"(a[2]), "+v"(a[3]), "+v"(a[4]), "+v"(a[5]), "+v"(a[6]), "+v"(a[7]), \
                                   "+v"(a[8]), "+v"(a[9]), "+v"(a[10]), "+v"(a[11]), "+v"(a[12]), "+v"(a[13]), "+v"(a[14]), "+v"(a[15]))
#define PIN8(a) asm volatile("" : "+v"(a[0]), "+v"(a[1]), "+v"(a[2]), "+v"(a[3]), "+v"(a[4]), "+v"(a[5]), "+v"(a[6]), "+v"(a[7]))

template <bool REFILL>
__device__ __forceinline__ void chunk(v4u (&kv)[2 * CL], const GAS char* kn, const GAS char* vn, const v4u& q0, const v4u& q1,
                                      float& m, float& l, float (&acc)[16], LAS float* pw, int lane) {
  float x[2 * CL];
#pragma unroll
  for (int j = 0; j < CL; ++j) {
    float a0 = 0.f, a1 = 0.f;
#pragma unroll
    for (int k = 0; k < 4; ++k) { a0 = dot2(kv[j][k], q0[k], a0); a1 = dot2(kv[j][k], q1[k], a1); }
    x[2 * j] = a0; x[2 * j + 1] = a1;
    if (REFILL) {
      asm volatile("" : "+v"(x[2 * j]), "+v"(x[2 * j + 1]));
      if (PACE) asm volatile("s_waitcnt vmcnt(%0)" ::"i"(PACE - 1));
      kv[j] = ldkv<K_NT>(kn + j * 1024);
      __builtin_amdgcn_sched_barrier(0);
    }
  }
#pragma unroll
  for (int i = 0; i < CL; ++i) x[i] = swp16_add(x[i], x[i + CL]);
#pragma unroll
  for (int i = 0; i < CL / 2; ++i) x[i] = tr_dpp<XOR8>(x[i], x[i + CL / 2], lane & 8);
#pragma unroll
  for (int i = 0; i < CL / 4; ++i) x[i] = tr_dpp<XOR7>(x[i], x[i + CL / 4], lane & 4);
#pragma unroll
  for (int i = 0; i < CL / 8; ++i) x[i] = tr_dpp<XOR2>(x[i], x[i + CL / 8], lane & 2);
  float sc;
  if (CL == 16) sc = tr_dpp<XOR1>(x[0], x[1], lane & 1); else sc = x[0] + dpp<XOR1>(x[0]);
  sc = rbf(sc) * (0.0625f * 1.4426950408889634f);
  float cm = sc;
  if (CL == 16) cm = fmaxf(cm, dpp<XOR2>(cm));
  cm = fmaxf(cm, dpp<ROR4>(cm)); cm = fmaxf(cm, dpp<XOR8>(cm)); cm = swp16_max(cm); cm = swp32_max(cm);
  const float mn = fmaxf(m, cm);
  const float alpha = __builtin_amdgcn_exp2f(m - mn), p = __builtin_amdgcn_exp2f(sc - mn);
  l = fmaf(l, alpha, p); m = mn;
  pw[CL == 16 ? lane : lane >> 1] = p;
  const float al0 = dpp<0x00>(alpha), al1 = dpp<CL == 16 ? 0x55 : 0xAA>(alpha);
#pragma unroll
  for (int k = 0; k < 8; ++k) { acc[k] *= al0; acc[8 + k] *= al1; }
  asm volatile("s_waitcnt lgkmcnt(0)" ::: "memory");
  f32x4 pr[CL / 2];
#pragma unroll
  for (int u = 0; u < CL / 2; ++u) pr[u] = *(const LAS f32x4*)(pw + (lane >> 5) * (2 * CL) + 4 * u);
  __builtin_amdgcn_sched_barrier(0);
#pragma unroll
  for (int j = 0; j < CL; ++j) {
    const float p0 = pr[j >> 1][(j & 1) * 2], p1 = pr[j >> 1][(j & 1) * 2 + 1];
#pragma unroll
    for (int k = 0; k < 4; ++k) {
      const float lo = bflo(kv[CL + j][k]), hi = bfhi(kv[CL + j][k]);
      acc[2 * k] = fmaf(p0, lo, acc[2 * k]); acc[2 * k + 1] = fmaf(p0, hi, acc[2 * k + 1]);
      acc[8 + 2 * k] = fmaf(p1, lo, acc[8 + 2 * k]); acc[8 + 2 * k + 1] = fmaf(p1, hi, acc[8 + 2 * k + 1]);
    }
    if (REFILL) {
      PIN16(acc);
      if (PACE) asm volatile("s_waitcnt vmcnt(%0)" ::"i"(PACE - 1));
      kv[CL + j] = ldkv<V_NT>(vn + j * 1024);
      __builtin_amdgcn_sched_barrier(0);
    }
  }
  asm volatile("" ::: "memory");
}

__global__ void __launch_bounds__(512, 2) fused_kernel(const u16* __restrict__ x, const u16* __restrict__ Kc, const u16* __restrict__ Vc,
    const u16* __restrict__ wq, const u16* __restrict__ wo, const u16* __restrict__ w1, const u16* __restrict__ w2,
    const u16* __restrict__ gw, unsigned char* ws, u16* out) {
  __shared__ __attribute__((aligned(16))) Lds sm;
  LAS Lds* L = (LAS Lds*)&sm;
  const int tid = threadIdx.x, lane = tid & 63, wave = __builtin_amdgcn_readfirstlane(tid >> 6);
  const int blk = blockIdx.x, kvh = blk & 7, s = (blk >> 3) & 7, b = blk >> 6;
  const int j = kvh * 8 + s;
  const int gid = b * 64 + j;
  const __amdgpu_buffer_rsrc_t rs = __builtin_amdgcn_make_buffer_rsrc((void*)ws, 0, WS_END, 0x00020000);
  STAMP(0);
  if (wave >= 4) {
    const int w = wave - 4;
    const size_t base = ((size_t)((b * 8 + kvh) * SK + s * 1024 + w * 2 * CL)) * (DH * 2) + lane * 16;
    const GAS char* kp = (const GAS char*)Kc + base;
    const GAS char* vp = (const GAS char*)Vc + base;
    if (tid == 256) L->cnt[0] = 0u;
    WG_BARRIER();
    v4u kv[2 * CL];
#pragma unroll
    for (int i = 0; i < 2 * CL; ++i) {
      if (PACE0 && i >= PACE0) asm volatile("s_waitcnt vmcnt(%0)" ::"i"(PACE0 - 1));
      kv[i] = i < CL ? ldkv<K_NT>(kp + i * 1024) : ldkv<V_NT>(vp + (i - CL) * 1024);
      __builtin_amdgcn_sched_barrier(0);
    }
    STAMP(1);
    WG_BARRIER();
    STAMP(2);
    const v4u q0 = *(const LAS v4u*)(L->q + (lane & 31) * 4), q1 = *(const LAS v4u*)(L->q + 128 + (lane & 31) * 4);
    float m = -INFINITY, l = 0.f, acc[16];
#pragma unroll
    for (int k = 0; k < 16; ++k) acc[k] = 0.f;
    LAS float* pw = L->p[w];
    for (int t = 1; t < NCH; ++t) {
      kp += CL * 4096; vp += CL * 4096;
      chunk<true>(kv, kp, vp, q0, q1, m, l, acc, pw, lane);
    }
    chunk<false>(kv, kp, vp, q0, q1, m, l, acc, pw, lane);
    STAMP(3);
    float o[8];
#pragma unroll
    for (int k = 0; k < 8; ++k) o[k] = swp32_add(acc[k], acc[8 + k]);
    *(LAS f32x4*)(L->mrg[w] + lane * 8) = f32x4{o[0], o[1], o[2], o[3]};
    *(LAS f32x4*)(L->mrg[w] + lane * 8 + 4) = f32x4{o[4], o[5], o[6], o[7]};
    if (CL == 16) l += dpp<XOR2>(l);
    l += dpp<ROR4>(l); l += dpp<XOR8>(l); l = swp16_add(l, l); l = swp32_add(l, l);
    if (CL == 16) { if (lane < 2) { L->ml[w][2 * lane] = m; L->ml[w][2 * lane + 1] = l; } }
    else if ((lane & 0x3d) == 0) { L->ml[w][lane] = m; L->ml[w][lane + 1] = l; }
    WG_BARRIER();
    STAMP(4);
    if (w < 2) {
      float M = -INFINITY;
#pragma unroll
      for (int ww = 0; ww < 4; ++ww) M = fmaxf(M, L->ml[ww][2 * w]);
      float Ls = 0.f; f32x4 os = {0.f, 0.f, 0.f, 0.f};
#pragma unroll
      for (int ww = 0; ww < 4; ++ww) {
        const float wt = __builtin_amdgcn_exp2f(L->ml[ww][2 * w] - M);
        Ls = fmaf(wt, L->ml[ww][2 * w + 1], Ls);
        os += wt * *(const LAS f32x4*)(L->mrg[ww] + w * 256 + lane * 4);
      }
      const v4u ov = {san(os[0]), san(os[1]), san(os[2]), san(os[3])};
      __builtin_amdgcn_raw_buffer_store_b128(ov, rs, WS_PO + gid * 2048 + w * 1024 + lane * 16, 0, 16);
      if (lane == 0) __builtin_amdgcn_raw_buffer_store_b64(v2u{san(M), san(Ls)}, rs, WS_ML + gid * 16 + w * 8, 0, 16);
      STAMP(5);
    }
    WG_BARRIER();
    if (w < 2 && j < 56) {
      const int cg = 2 * j + w;
      v4u y;
      for (unsigned spins = 0;; ++spins) {
        y = __builtin_amdgcn_raw_buffer_load_b128(rs, WS_Y + (b * 64 + lane) * (DM * 4) + cg * 16, 0, 16);
        const bool ok = y[0] != SENT && y[1] != SENT && y[2] != SENT && y[3] != SENT;
        if (__all(ok) || spins > SPIN_CAP) break;
        __builtin_amdgcn_s_sleep(1);
      }
      STAMP(6);
      float r0 = swp32_add(__uint_as_float(y[0]), __uint_as_float(y[2])), r1 = swp32_add(__uint_as_float(y[1]), __uint_as_float(y[3]));
      float r = swp16_add(r0, r1);
      r += dpp<XOR8>(r); r += dpp<XOR7>(r); r += dpp<XOR2>(r); r += dpp<XOR1>(r);
      if ((lane & 15) == 0) out[b * DM + cg * 4 + (lane >> 4)] = (u16)pkbf(r, 0.f);
      STAMP(7);
    }
  } else {
    const int hw = wave;
    const GAS unsigned* x32 = (const GAS unsigned*)(x + b * DM);
    const int r = lane >> 3;
    unsigned xp[7];
#pragma unroll
    for (int k = 0; k < 7; ++k) xp[k] = x32[56 * hw + 7 * r + k];
    const GAS char* wqp = (const GAS char*)wq + (size_t)(112 * hw + 14 * r) * (QLD * 2) + (j * 64 + (lane & 7) * 8) * 2;
    v4u wr[14];
#pragma unroll
    for (int k = 0; k < 14; ++k) wr[k] = ldw(wqp + (size_t)k * (QLD * 2));
    const u64 ep = __hip_atomic_load((GAS u64*)(ws + WS_EPOCH + b * 256), RLX_AGENT);
    {
      const v4u sv = {SENT, SENT, SENT, SENT};
      const int off = hw < 2 ? WS_PO + gid * 2048 + hw * 1024 + lane * 16
                    : (tid < 240 ? WS_Y + gid * (DM * 4) + (tid - 128) * 16 : (tid == 240 ? WS_ML + gid * 16 : WS_END));
      __builtin_amdgcn_raw_buffer_store_b128(sv, rs, off, 0, 16);
    }
    __builtin_amdgcn_sched_barrier(0);
    WG_BARRIER();
    STAMP(1);
    float qa[8];
#pragma unroll
    for (int c = 0; c < 8; ++c) qa[c] = 0.f;
#pragma unroll
    for (int k = 0; k < 7; ++k) pair_dot(wr[2 * k], wr[2 * k + 1], xp[k], qa);
    L->qpart[hw][(lane & 7) * 8 + r] = tr8(qa, lane);
    const u64 tag = (ep + 1) << 16;
    STAMP(2);
    asm volatile("s_waitcnt vmcnt(0) lgkmcnt(0)" ::: "memory");
    unsigned arr = 0;
    if (lane == 0) arr = __hip_atomic_fetch_add(&L->cnt[0], 1u, __ATOMIC_RELAXED, __HIP_MEMORY_SCOPE_WORKGROUP);
    if (__builtin_amdgcn_readfirstlane(arr) == 3) {
      asm volatile("" ::: "memory");
      const float qv = (L->qpart[0][lane] + L->qpart[1][lane]) + (L->qpart[2][lane] + L->qpart[3][lane]);
      __hip_atomic_store((GAS u64*)(ws + WS_QG) + gid * 64 + lane, tag | (pkbf(qv, 0.f) & 0xffffu), RLX_AGENT);
    }
    STAMP(3);
    {
      const unsigned thi = (unsigned)(tag >> 32), tlo = (unsigned)tag;
      v4u g;
      for (unsigned spins = 0;; ++spins) {
        g = __builtin_amdgcn_raw_buffer_load_b128(rs, WS_QG + ((gid & ~7) + 2 * hw) * 512 + lane * 16, 0, 16);
        const bool ok = g[1] == thi && g[3] == thi && (g[0] & 0xffff0000u) == tlo && (g[2] & 0xffff0000u) == tlo;
        if (__all(ok) || spins > SPIN_CAP) break;
        __builtin_amdgcn_s_sleep(1);
      }
      L->q[hw * 64 + lane] = perm_lo(g[0], g[2]);
    }
    STAMP(4);
    WG_BARRIER();
    float z = 0.f;
#pragma unroll
    for (int k = 0; k < 4; ++k) {
      const int idx = lane + 64 * k;
      const bool in = idx < DM / 2;
      const unsigned xv = x32[in ? idx : 0], gv = ((const GAS unsigned*)gw)[in ? idx : 0];
      const float t = rbf(bflo(xv) * bflo(gv)) + rbf(bfhi(xv) * bfhi(gv));
      z += in ? t : 0.f;
    }
    z = swp32_add(z, z); z = swp16_add(z, z); z += dpp<XOR8>(z); z += dpp<XOR7>(z); z += dpp<XOR2>(z); z += dpp<XOR1>(z);
    float g = rbf(1.f / (1.f + __expf(-rbf(z))));
    float ya[8], yb[8];
#pragma unroll
    for (int c = 0; c < 8; ++c) { ya[c] = 0.f; yb[c] = 0.f; }
    const int cl = lane < 56 ? lane : 55;
    if (hw == 3 && s < 7) {
      const int u0 = (kvh * 7 + s) * 32, rr = lane >> 2;
      float ha[8];
#pragma unroll
      for (int c = 0; c < 8; ++c) ha[c] = 0.f;
      const GAS u16* w1p = (const GAS u16*)w1 + (size_t)(2 * rr) * DFF + u0 + (lane & 3) * 8;
      for (int h = 0; h < 2; ++h) {
        v4u ma[14]; unsigned xq[7];
#pragma unroll
        for (int k = 0; k < 7; ++k) {
          ma[2 * k] = ldw(w1p + (size_t)((7 * h + k) * 32) * DFF);
          ma[2 * k + 1] = ldw(w1p + (size_t)((7 * h + k) * 32 + 1) * DFF);
          xq[k] = x32[(7 * h + k) * 16 + rr];
        }
#pragma unroll
        for (int k = 0; k < 7; ++k) pair_dot(ma[2 * k], ma[2 * k + 1], xq[k], ha);
      }
      float hv = tr8(ha, lane);
      hv += dpp<SHR4>(hv);
      const float omg = rbf(1.f - g);
      hv = fmaxf(rbf(hv), 0.f);
      for (int c4 = 0; c4 < 4; ++c4) {
        v4u w2r[8];
#pragma unroll
        for (int u = 0; u < 8; ++u) w2r[u] = ldw((const GAS u16*)w2 + (size_t)(u0 + c4 * 8 + u) * DM + cl * 8);
#pragma unroll
        for (int u = 0; u < 8; ++u) {
          const float hu = __builtin_bit_cast(float, __builtin_amdgcn_readlane(__builtin_bit_cast(int, hv), u * 8 + 4 + c4));
#pragma unroll
          for (int c = 0; c < 4; ++c) { yb[2 * c] = fmaf(hu, bflo(w2r[u][c]), yb[2 * c]); yb[2 * c + 1] = fmaf(hu, bfhi(w2r[u][c]), yb[2 * c + 1]); }
        }
      }
#pragma unroll
      for (int c = 0; c < 8; ++c) yb[c] *= omg;
    }
    PIN8(yb);
    unsigned wp[8][8];
    {
      const GAS u16* wop = (const GAS u16*)wo + (size_t)(j * 64 + hw * 16) * DM + cl * 8;
#pragma unroll
      for (int i = 0; i < 8; ++i) {
        const v4u ra = ldw(wop + (2 * i) * DM), rb = ldw(wop + (2 * i + 1) * DM);
#pragma unroll
        for (int c = 0; c < 4; ++c) { wp[i][2 * c] = perm_lo(ra[c], rb[c]); wp[i][2 * c + 1] = perm_hi(ra[c], rb[c]); }
      }
#pragma unroll
      for (int i = 0; i < 8; ++i) PIN8(wp[i]);
    }
    asm volatile("" : "+v"(g));
    if (hw == 0) {
      for (unsigned spins = 0;; ++spins) {
        const u64 gv = __hip_atomic_load((GAS u64*)(ws + WS_QG) + (b * 64 + lane) * 64, RLX_AGENT);
        if (__all((gv & ~0xffffull) == tag) || spins > SPIN_CAP) break;
        __builtin_amdgcn_s_sleep(8);
      }
      if (j == 0 && lane == 0) __hip_atomic_store((GAS u64*)(ws + WS_EPOCH + b * 256), ep + 1, RLX_AGENT);
    }
    STAMP(5);
    WG_BARRIER();
    {
      const int pr = (gid & ~7) + (lane >> 3), head = s >> 2;
      v2u ov, mlv;
      for (unsigned spins = 0;; ++spins) {
        ov = __builtin_amdgcn_raw_buffer_load_b64(rs, WS_PO + pr * 2048 + (s * 64 + hw * 16 + (lane & 7) * 2) * 4, 0, 16);
        mlv = __builtin_amdgcn_raw_buffer_load_b64(rs, WS_ML + pr * 16 + head * 8, 0, 16);
        const bool ok = ov[0] != SENT && ov[1] != SENT && mlv[0] != SENT && mlv[1] != SENT;
        if (__all(ok) || spins > SPIN_CAP) break;
        __builtin_amdgcn_s_sleep(1);
      }
      STAMP(6);
      const float mp = __uint_as_float(mlv[0]);
      float M = fmaxf(mp, dpp<XOR8>(mp)); M = swp16_max(M); M = swp32_max(M);
      const float wt = __builtin_amdgcn_exp2f(mp - M);
      float Ls = wt * __uint_as_float(mlv[1]), o0 = wt * __uint_as_float(ov[0]), o1 = wt * __uint_as_float(ov[1]);
      Ls += dpp<XOR8>(Ls); o0 += dpp<XOR8>(o0); o1 += dpp<XOR8>(o1);
      Ls = swp16_add(Ls, Ls); o0 = swp16_add(o0, o0); o1 = swp16_add(o1, o1);
      Ls = swp32_add(Ls, Ls); o0 = swp32_add(o0, o0); o1 = swp32_add(o1, o1);
      const float inv = 1.f / Ls;
      const unsigned op = pkbf(o0 * inv, o1 * inv);
#pragma unroll
      for (int i = 0; i < 8; ++i) {
        const unsigned oi = (unsigned)__builtin_amdgcn_readlane((int)op, i);
#pragma unroll
        for (int c = 0; c < 8; ++c) ya[c] = dot2(wp[i][c], oi, ya[c]);
      }
      if (lane < 56) {
        f32x4 y0, y1;
#pragma unroll
        for (int c = 0; c < 4; ++c) { y0[c] = fmaf(g, ya[c], yb[c]); y1[c] = fmaf(g, ya[4 + c], yb[4 + c]); }
        *(LAS f32x4*)(L->yred[hw] + lane * 8) = y0;
        *(LAS f32x4*)(L->yred[hw] + lane * 8 + 4) = y1;
      }
    }
    STAMP(7);
    WG_BARRIER();
    if (tid < 112) {
      f32x4 y = *(const LAS f32x4*)(L->yred[0] + tid * 4);
#pragma unroll
      for (int ww = 1; ww < 4; ++ww) y += *(const LAS f32x4*)(L->yred[ww] + tid * 4);
      const v4u yv = {san(y[0]), san(y[1]), san(y[2]), san(y[3])};
      __builtin_amdgcn_raw_buffer_store_b128(yv, rs, WS_Y + gid * (DM * 4) + tid * 16, 0, 16);
    }
    STAMP(8);
  }
  STAMP_OUT();
}

extern "C" void kernel_launch(void* const* d_in, const int* in_sizes, int n_in, void* d_out, int out_size, void* d_ws, size_t ws_size, hipStream_t stream) {
  fused_kernel<<<256, 512, 0, stream>>>((const u16*)d_in[0], (const u16*)d_in[1], (const u16*)d_in[2], (const u16*)d_in[3], (const u16*)d_in[4],
                                        (const u16*)d_in[6], (const u16*)d_in[7], (const u16*)d_in[8], (unsigned char*)d_ws, (u16*)d_out);
}
```

```cpp
#include <hip/hip_runtime.h>
#include <hip/hip_bf16.h>
#include <cstdint>

#define K_NT 1
#define V_NT 1
#define W_NT 0
#define DEPTH 13
#define PREH 1
#define DEPTH2 20
#define HSW 54
#define ABL 0
#define GMAP 0
#define SKEW 0
#define SKEWS 0
#define STAMPS 0

typedef unsigned v4u __attribute__((ext_vector_type(4)));
typedef unsigned v3u __attribute__((ext_vector_type(3)));
typedef unsigned v2u __attribute__((ext_vector_type(2)));
typedef float f32x4 __attribute__((ext_vector_type(4)));
typedef __bf16 bf16x2 __attribute__((ext_vector_type(2)));
typedef __bf16 bf16x8 __attribute__((ext_vector_type(8)));
typedef short s16x4 __attribute__((ext_vector_type(4)));
typedef unsigned short u16;
typedef unsigned long long u64;
#define GAS __attribute__((address_space(1)))
#define LAS __attribute__((address_space(3)))
#define RLX_AGENT __ATOMIC_RELAXED, __HIP_MEMORY_SCOPE_AGENT

constexpr int DM = 448, SK = 8192, DH = 256, DFF = 1792, QLD = 4096;
constexpr unsigned SENT = 0xFFFFFFFFu;
constexpr unsigned SPIN_CAP = 1u << 18;
constexpr int WS_EPOCH = 0;
constexpr int WS_RDY = 4096;
constexpr int WS_QG = 8192;
constexpr int WS_PO = WS_QG + 256 * 64 * 8;
constexpr int WS_Y = WS_PO + 256 * 4096;
constexpr int WS_END = WS_Y + 256 * DM * 4;
constexpr size_t WS_STAMP = 16u << 20;

__device__ __forceinline__ float bflo(unsigned u) { return __builtin_bit_cast(float, u << 16); }
__device__ __forceinline__ float bfhi(unsigned u) { return __builtin_bit_cast(float, u & 0xffff0000u); }
__device__ __forceinline__ unsigned pkbf(float lo, float hi) {
  bf16x2 r; r[0] = (__bf16)lo; r[1] = (__bf16)hi; return __builtin_bit_cast(unsigned, r);
}
__device__ __forceinline__ float rbf(float f) { return bflo(pkbf(f, 0.f)); }
__device__ __forceinline__ float dot2(unsigned a, unsigned b, float acc) {
  return __builtin_amdgcn_fdot2_f32_bf16(__builtin_bit_cast(bf16x2, a), __builtin_bit_cast(bf16x2, b), acc, false);
}
__device__ __forceinline__ unsigned perm_lo(unsigned a, unsigned b) { return __builtin_amdgcn_perm(b, a, 0x05040100u); }
__device__ __forceinline__ unsigned perm_hi(unsigned a, unsigned b) { return __builtin_amdgcn_perm(b, a, 0x07060302u); }
template <int CTRL> __device__ __forceinline__ float dpp(float x) {
  return __builtin_bit_cast(float, __builtin_amdgcn_mov_dpp(__builtin_bit_cast(int, x), CTRL, 0xf, 0xf, true));
}
constexpr int XOR1 = 0xB1, XOR2 = 0x4E, XOR7 = 0x141, XOR8 = 0x128, ROR4 = 0x124, SHR4 = 0x114;
__device__ __forceinline__ float swp16_add(float a, float b) {
  auto s = __builtin_amdgcn_permlane16_swap(__float_as_uint(a), __float_as_uint(b), false, false);
  return __uint_as_float(s[0]) + __uint_as_float(s[1]);
}
__device__ __forceinline__ float swp32_add(float a, float b) {
  auto s = __builtin_amdgcn_permlane32_swap(__float_as_uint(a), __float_as_uint(b), false, false);
  return __uint_as_float(s[0]) + __uint_as_float(s[1]);
}
__device__ __forceinline__ float swp16_max(float a) {
  auto s = __builtin_amdgcn_permlane16_swap(__float_as_uint(a), __float_as_uint(a), false, false);
  return fmaxf(__uint_as_float(s[0]), __uint_as_float(s[1]));
}
__device__ __forceinline__ float swp32_max(float a) {
  auto s = __builtin_amdgcn_permlane32_swap(__float_as_uint(a), __float_as_uint(a), false, false);
  return fmaxf(__uint_as_float(s[0]), __uint_as_float(s[1]));
}
template <int CTRL> __device__ __forceinline__ float tr_dpp(float lo, float hi, bool sel) {
  const float t1 = lo + dpp<CTRL>(lo), t2 = hi + dpp<CTRL>(hi);
  return sel ? t2 : t1;
}
__device__ __forceinline__ float tr8(float (&a)[8], int lane) {
#pragma unroll
  for (int i = 0; i < 4; ++i) a[i] = swp32_add(a[i], a[i + 4]);
#pragma unroll
  for (int i = 0; i < 2; ++i) a[i] = swp16_add(a[i], a[i + 2]);
  return tr_dpp<XOR8>(a[0], a[1], lane & 8);
}
__device__ __forceinline__ void pair_dot(const v4u& ra, const v4u& rb, unsigned xp, float (&a)[8]) {
#pragma unroll
  for (int c = 0; c < 4; ++c) {
    a[2 * c] = dot2(perm_lo(ra[c], rb[c]), xp, a[2 * c]);
    a[2 * c + 1] = dot2(perm_hi(ra[c], rb[c]), xp, a[2 * c + 1]);
  }
}
__device__ __forceinline__ unsigned san(float f) { const unsigned u = __float_as_uint(f); return u == SENT ? 0x7FC00000u : u; }
template <bool NT> __device__ __forceinline__ v4u ldkv(const GAS char* p) {
  return NT ? __builtin_nontemporal_load((const GAS v4u*)p) : *(const GAS v4u*)p;
}
__device__ __forceinline__ v4u ldw(const GAS void* p) {
  return W_NT ? __builtin_nontemporal_load((const GAS v4u*)p) : *(const GAS v4u*)p;
}
#define WG_BARRIER() do { asm volatile("s_waitcnt lgkmcnt(0)" ::: "memory"); __builtin_amdgcn_s_barrier(); asm volatile("" ::: "memory"); __builtin_amdgcn_sched_barrier(0); } while (0)
#define DRAIN() asm volatile("s_waitcnt vmcnt(0)" ::: "memory")
#define STAMP(i) do {} while (0)
#define STAMP_OUT() do {} while (0)

struct Lds {
  unsigned char ring[8 * 16384];
  unsigned q[256];
  float fin[8][32];
  float mrg[4][512];
  float ml[4][4];
  float yred[5][DM];
  float qpart[6][64];
  unsigned done[4];
  unsigned full[2];
  unsigned cnt;
  unsigned qready;
  float gate;
};

#define PIN16(a) asm volatile("" : "+v"(a[0]), "+v"(a[1]), "+v"(a[2]), "+v"(a[3]), "+v"(a[4]), "+v"(a[5]), "+v"(a[6]), "+v"(a[7]), \
                                   "+v"(a[8]), "+v"(a[9]), "+v"(a[10]), "+v"(a[11]), "+v"(a[12]), "+v"(a[13]), "+v"(a[14]), "+v"(a[15]))
#define PIN8(a) asm volatile("" : "+v"(a[0]), "+v"(a[1]), "+v"(a[2]), "+v"(a[3]), "+v"(a[4]), "+v"(a[5]), "+v"(a[6]), "+v"(a[7]))
#define LGKM0() asm volatile("s_waitcnt lgkmcnt(0)" ::: "memory")

__device__ __forceinline__ void wait_ge(volatile LAS unsigned* w, unsigned need) {
#pragma unroll 1
  for (unsigned spins = 0; spins < (1u << 22); ++spins) {
    if ((unsigned)__builtin_amdgcn_readfirstlane(*w) >= need) break;
    __builtin_amdgcn_s_sleep(1);
  }
  asm volatile("" ::: "memory");
}

#define PTHR 16
#define PSLOW 10
#define PSLEEP 2
__device__ __forceinline__ v4u poll16(const v4u& srd, int off) {
  unsigned c0, c1, c2, c3, mx, cnt;
  asm volatile(
      "s_waitcnt vmcnt(0)\n\t"
      "v_mov_b32 v252, -1\n\tv_mov_b32 v253, -1\n\tv_mov_b32 v254, -1\n\tv_mov_b32 v255, -1\n\t"
      "s_mov_b32 %5, 0x200000\n\t"
      "s_nop 1\n"
      "1:\n\t"
      "buffer_load_dwordx4 v[252:255], %6, %7, 0 offen sc1\n\t"
      "s_sleep %8\n\t"
      "s_waitcnt vmcnt(12)\n\t"
      "v_mov_b32 %0, v252\n\tv_mov_b32 %1, v253\n\tv_mov_b32 %2, v254\n\tv_mov_b32 %3, v255\n\t"
      "v_max3_u32 %4, %0, %1, %2\n\t"
      "v_max_u32 %4, %4, %3\n\t"
      "v_cmp_eq_u32 vcc, -1, %4\n\t"
      "s_and_b64 exec, exec, vcc\n\t"
      "s_cbranch_scc0 2f\n\t"
      "s_bcnt1_i32_b64 vcc_lo, exec\n\t"
      "s_cmp_le_u32 vcc_lo, %9\n\t"
      "s_cbranch_scc1 3f\n\t"
      "s_sleep %10\n"
      "3:\n\t"
      "s_sub_u32 %5, %5, 1\n\t"
      "s_cbranch_scc0 1b\n"
      "2:\n\t"
      "s_mov_b64 exec, -1\n\t"
      "s_nop 4"
      : "=&v"(c0), "=&v"(c1), "=&v"(c2), "=&v"(c3), "=&v"(mx), "=&s"(cnt)
      : "v"(off), "s"(srd), "i"(PSLEEP), "i"(PTHR), "i"(PSLOW)
      : "vcc", "scc", "v252", "v253", "v254", "v255", "memory");
  return v4u{c0, c1, c2, c3};
}

template <int D>
__device__ __forceinline__ void issue8(const char* src, unsigned char* dst, const unsigned (&voff)[4], int h, int& pub, volatile LAS unsigned* full) {
#pragma unroll
  for (int g = 0; g < 8; ++g) {
    asm volatile("s_waitcnt vmcnt(%0)" ::"i"(D - 1) : "memory");
    if (g == (D - 1) % 8) { pub = max(pub, h - (D - 1) / 8); *full = pub; }
    if ((g & 3) == 0) __builtin_amdgcn_global_load_lds((const unsigned*)(src + (g & 4) * 1024 + voff[0]), (unsigned*)(dst + (g & 4) * 1024), 16, 0, 2);
    if ((g & 3) == 1) __builtin_amdgcn_global_load_lds((const unsigned*)(src + (g & 4) * 1024 + voff[1]), (unsigned*)(dst + (g & 4) * 1024), 16, 1024, 2);
    if ((g & 3) == 2) __builtin_amdgcn_global_load_lds((const unsigned*)(src + (g & 4) * 1024 + voff[2]), (unsigned*)(dst + (g & 4) * 1024), 16, 2048, 2);
    if ((g & 3) == 3) __builtin_amdgcn_global_load_lds((const unsigned*)(src + (g & 4) * 1024 + voff[3]), (unsigned*)(dst + (g & 4) * 1024), 16, 3072, 2);
  }
}

__device__ __forceinline__ void merge_publish(LAS Lds* L, __amdgpu_buffer_rsrc_t rs, int gid, int u, int lane) {
  typedef float f32x2 __attribute__((ext_vector_type(2)));
  const int hh = u >> 1;
  float M = -INFINITY;
#pragma unroll
  for (int ww = 0; ww < 4; ++ww) M = fmaxf(M, L->ml[ww][2 * hh]);
  float Ls = 0.f; f32x2 os = {0.f, 0.f};
#pragma unroll
  for (int ww = 0; ww < 4; ++ww) {
    const float wt = __builtin_amdgcn_exp2f(L->ml[ww][2 * hh] - M);
    Ls = fmaf(wt, L->ml[ww][2 * hh + 1], Ls);
    os += wt * *(const LAS f32x2*)(L->mrg[ww] + u * 128 + lane * 2);
  }
  const v4u ov = {san(os[0]), san(os[1]), san(M), san(Ls)};
  __builtin_amdgcn_raw_buffer_store_b128(ov, rs, WS_PO + gid * 4096 + u * 1024 + lane * 16, 0, 16);
}

__global__ void __launch_bounds__(512, 2) fused_kernel(const u16* __restrict__ x, const u16* __restrict__ Kc, const u16* __restrict__ Vc,
    const u16* __restrict__ wq, const u16* __restrict__ wo, const u16* __restrict__ w1, const u16* __restrict__ w2,
    const u16* __restrict__ gw, unsigned char* ws, u16* out) {
  __shared__ __attribute__((aligned(16))) Lds sm;
  LAS Lds* L = (LAS Lds*)&sm;
  asm volatile("" ::"s"(x), "s"(Kc), "s"(Vc), "s"(wq), "s"(wo), "s"(w1), "s"(w2), "s"(gw), "s"(ws), "s"(out));
  const int tid = threadIdx.x, lane = tid & 63, wave = __builtin_amdgcn_readfirstlane(tid >> 6);
  const int blk = blockIdx.x, b = blk >> 6;
  const int kvh = GMAP ? (blk & 7) : ((blk >> 3) & 7);
  const int s = GMAP ? ((blk >> 3) & 7) : ((blk + (kvh & 6)) & 7);
  const int j = kvh * 8 + s;
  const int gid = b * 64 + j;
  const __amdgpu_buffer_rsrc_t rs = __builtin_amdgcn_make_buffer_rsrc((void*)ws, 0, WS_END, 0x00020000);
  const v4u srd = {(unsigned)(size_t)ws, (unsigned)((size_t)ws >> 32) & 0xffffu, (unsigned)WS_END, 0x00020000u};
  STAMP(0);
  if (tid < 8) L->done[tid] = 0u;
  if (wave < 2) {
    __builtin_amdgcn_s_setprio(3);
    volatile LAS unsigned* full = &L->full[wave];
    const char* kb = (const char*)Kc + ((size_t)((b * 8 + kvh) * SK + s * 1024)) * (DH * 2);
    const long vdelta = (const char*)Vc - (const char*)Kc;
    const int rot = (b * 8 + kvh) * SKEW + s * SKEWS;
    unsigned voff[4];
#pragma unroll
    for (int g = 0; g < 4; ++g) voff[g] = (lane >> 5) * 512 + (((lane & 31) ^ (((2 * g + (lane >> 5)) & 7) << 1)) << 4);
    int pub = 0;
#pragma unroll 1
    for (int h = 0; h < 64; ++h) {
      const int u = (h & ~1) + wave;
      if (h == PREH) WG_BARRIER();
      if (!(h & 1) && h >= 8) {
        const unsigned need = (unsigned)(u >> 2) - 1u;
        volatile LAS unsigned* dn = &L->done[u & 3];
#pragma unroll 1
        for (unsigned spins = 0; spins < (1u << 22); ++spins) {
          if ((unsigned)__builtin_amdgcn_readfirstlane(*dn) >= need) break;
          DRAIN();
          pub = h; *full = pub;
          __builtin_amdgcn_s_sleep(1);
        }
        asm volatile("" ::: "memory");
      }
      const char* src = kb + (size_t)((u + rot) & 63) * 8192 + (vdelta & -(long)(h & 1));
      unsigned char* dst = sm.ring + (u & 7) * 16384 + (h & 1) * 8192;
      if (h < HSW) issue8<DEPTH>(src, dst, voff, h, pub, full); else issue8<DEPTH2>(src, dst, voff, h, pub, full);
    }
    STAMP(1);
#pragma unroll
    for (int k = (DEPTH2 - 1) / 8; k >= 0; --k) {
      asm volatile("s_waitcnt vmcnt(%0)" ::"i"(8 * k) : "memory");
      *full = max(pub, 64 - k);
    }
    STAMP(2);
    WG_BARRIER();
    merge_publish(L, rs, gid, wave, lane);
    STAMP(5);
    WG_BARRIER();
  } else {
    const GAS unsigned* x32 = (const GAS unsigned*)(x + b * DM);
    const int wv = wave - 2;
    const u64 ep = *(const u64*)(ws + WS_EPOCH + b * 256);
    {
      const int r = lane >> 3;
      const GAS char* wqp = (const GAS char*)wq + (size_t)(2 * r) * (QLD * 2) + (j * 64 + (lane & 7) * 8) * 2;
      v4u wr[10]; unsigned xp[5];
#pragma unroll
      for (int k = 0; k < 5; ++k) {
        const int pg = min(wv + 6 * k, 27);
        xp[k] = x32[8 * pg + r];
        wr[2 * k] = (ABL & 4) ? v4u{1u, 2u, 3u, 4u} : ldw(wqp + (size_t)(16 * pg) * (QLD * 2));
        wr[2 * k + 1] = (ABL & 4) ? v4u{1u, 2u, 3u, 4u} : ldw(wqp + (size_t)(16 * pg + 1) * (QLD * 2));
      }
      __builtin_amdgcn_sched_barrier(0);
      __builtin_amdgcn_s_barrier();
      asm volatile("" ::: "memory");
      __builtin_amdgcn_sched_barrier(0);
      if (wv >= 4) xp[4] = 0u;
      STAMP(1);
      float qa[8];
#pragma unroll
      for (int c = 0; c < 8; ++c) qa[c] = 0.f;
#pragma unroll
      for (int k = 0; k < 5; ++k) pair_dot(wr[2 * k], wr[2 * k + 1], xp[k], qa);
      L->qpart[wv][(lane & 7) * 8 + r] = tr8(qa, lane);
    }
    const u64 tag = (ep + 1) << 16;
    LGKM0();
    unsigned arr = 0;
    if (lane == 0) arr = __hip_atomic_fetch_add(&L->cnt, 1u, __ATOMIC_RELAXED, __HIP_MEMORY_SCOPE_WORKGROUP);
    if (__builtin_amdgcn_readfirstlane(arr) == 5) {
      asm volatile("" ::: "memory");
      float qv = L->qpart[0][lane];
#pragma unroll
      for (int k = 1; k < 6; ++k) qv += L->qpart[k][lane];
      __hip_atomic_store((GAS u64*)(ws + WS_QG) + gid * 64 + lane, tag | (pkbf(qv, 0.f) & 0xffffu), RLX_AGENT);
    }
    STAMP(2);
    const int cl = lane < 56 ? lane : 55;
    if (wave >= 4) {
      const int w = wave - 4;
      volatile LAS unsigned* dn = &L->done[w];
      volatile LAS unsigned* full = &L->full[w & 1];
      const int g4 = lane >> 4, c16 = lane & 15;
      const unsigned ka0 = c16 * 512 + ((g4 ^ ((c16 & 7) << 1)) << 4);
      const int vrow = 4 * g4 + (c16 >> 2);
      const unsigned va0 = 8192 + vrow * 512 + ((vrow & 7) << 5) + (lane & 3) * 8;
      bf16x8 qf[8];
      v4u wrow[16];
      float m = -INFINITY, l = 0.f;
      f32x4 acc0 = {0.f, 0.f, 0.f, 0.f}, acc1 = {0.f, 0.f, 0.f, 0.f};
#pragma unroll 1
      for (int n = 0; n < 16; ++n) {
        const int u = 4 * n + w;
        const LAS unsigned char* sl = L->ring + (u & 7) * 16384;
        bf16x8 kf[8];
        s16x4 vf[16];
        wait_ge(full, (u | 1));
#pragma unroll
        for (int t = 0; t < 8; ++t) kf[t] = *(const LAS bf16x8*)(sl + ((ka0 ^ ((t & 3) << 6)) + (t >> 2) * 256));
        if (n == 0) {
          wait_ge(full, (u | 1) + 1);
#pragma unroll
          for (int t = 0; t < 16; ++t) vf[t] = __builtin_amdgcn_ds_read_tr16_b64_v4i16((LAS s16x4*)(sl + ((va0 ^ ((t & 7) << 5)) + (t >> 3) * 256)));
          LGKM0(); *dn = 1u; PIN16(vf); PIN8(kf);
          wait_ge(&L->qready, 1);
          STAMP(3);
#pragma unroll
          for (int t = 0; t < 8; ++t) qf[t] = *(const LAS bf16x8*)(L->q + (lane & 1) * 128 + (4 * t + g4) * 4);
          const GAS u16* wop = (const GAS u16*)wo + (size_t)(j * 64 + w * 16) * DM + cl * 8;
#pragma unroll
          for (int i = 0; i < 16; ++i) wrow[i] = (ABL & 2) ? v4u{1u, 2u, 3u, (unsigned)(size_t)wop} : ldw(wop + i * DM);
        }
        f32x4 sa = {0.f, 0.f, 0.f, 0.f}, sb = {0.f, 0.f, 0.f, 0.f};
#pragma unroll
        for (int t = 0; t < 8; t += 2) {
          sa = __builtin_amdgcn_mfma_f32_16x16x32_bf16(kf[t], qf[t], sa, 0, 0, 0);
          sb = __builtin_amdgcn_mfma_f32_16x16x32_bf16(kf[t + 1], qf[t + 1], sb, 0, 0, 0);
        }
        float sc[4];
#pragma unroll
        for (int r = 0; r < 4; ++r) sc[r] = rbf(sa[r] + sb[r]) * (0.0625f * 1.4426950408889634f);
        float cm = fmaxf(fmaxf(sc[0], sc[1]), fmaxf(sc[2], sc[3]));
        cm = swp16_max(cm); cm = swp32_max(cm);
        const float mn = fmaxf(m, cm);
        const float alpha = __builtin_amdgcn_exp2f(m - mn);
        m = mn;
#pragma unroll
        for (int r = 0; r < 4; ++r) sc[r] = __builtin_amdgcn_exp2f(sc[r] - mn);
        l = fmaf(l, alpha, (sc[0] + sc[1]) + (sc[2] + sc[3]));
        const unsigned p01 = pkbf(sc[0], sc[1]), p23 = pkbf(sc[2], sc[3]);
        const float al0 = dpp<0x00>(alpha), al1 = dpp<0x55>(alpha);
        acc0 *= f32x4{al0, al1, al0, al1}; acc1 *= f32x4{al0, al1, al0, al1};
        s16x4 pa[8];
#pragma unroll
        for (int v = 0; v < 8; ++v) { const bool on = (c16 >> 1) == v; pa[v] = __builtin_bit_cast(s16x4, v2u{on ? p01 : 0u, on ? p23 : 0u}); }
        PIN8(pa);
        if (n != 0) {
          wait_ge(full, (u | 1) + 1);
#pragma unroll
          for (int t = 0; t < 16; ++t) vf[t] = __builtin_amdgcn_ds_read_tr16_b64_v4i16((LAS s16x4*)(sl + ((va0 ^ ((t & 7) << 5)) + (t >> 3) * 256)));
        }
#pragma unroll
        for (int v = 0; v < 8; ++v) {
          acc0 = __builtin_amdgcn_mfma_f32_16x16x16bf16_1k(pa[v], vf[v], acc0, 0, 0, 0);
          acc1 = __builtin_amdgcn_mfma_f32_16x16x16bf16_1k(pa[v], vf[8 + v], acc1, 0, 0, 0);
        }
        asm volatile("" : "+v"(acc0), "+v"(acc1));
        LGKM0(); *dn = n + 1;
      }
      STAMP(4);
#pragma unroll
      for (int r = 0; r < 4; ++r) {
        L->mrg[w][(r & 1) * 256 + (2 * g4 + (r >> 1)) * 16 + c16] = acc0[r];
        L->mrg[w][(r & 1) * 256 + 128 + (2 * g4 + (r >> 1)) * 16 + c16] = acc1[r];
      }
      l = swp16_add(l, l); l = swp32_add(l, l);
      if (lane < 2) { L->ml[w][2 * lane] = m; L->ml[w][2 * lane + 1] = l; }
      WG_BARRIER();
      STAMP(5);
#pragma unroll
      for (int i = 0; i < 8; ++i) {
        const v4u ra = wrow[2 * i], rb = wrow[2 * i + 1];
        wrow[2 * i] = v4u{perm_lo(ra[0], rb[0]), perm_hi(ra[0], rb[0]), perm_lo(ra[1], rb[1]), perm_hi(ra[1], rb[1])};
        wrow[2 * i + 1] = v4u{perm_lo(ra[2], rb[2]), perm_hi(ra[2], rb[2]), perm_lo(ra[3], rb[3]), perm_hi(ra[3], rb[3])};
      }
      PIN16(wrow);
      const float g = L->gate;
      const v4u pv = poll16(srd, WS_PO + ((gid & ~7) + (lane >> 3)) * 4096 + (s * 32 + w * 8 + (lane & 7)) * 16);
      STAMP(6);
      const float mp = __uint_as_float(pv[2]);
      float M = fmaxf(mp, dpp<XOR8>(mp)); M = swp16_max(M); M = swp32_max(M);
      const float wt = __builtin_amdgcn_exp2f(mp - M);
      float Ls = wt * __uint_as_float(pv[3]), o0 = wt * __uint_as_float(pv[0]), o1 = wt * __uint_as_float(pv[1]);
      Ls += dpp<XOR8>(Ls); o0 += dpp<XOR8>(o0); o1 += dpp<XOR8>(o1);
      Ls = swp16_add(Ls, Ls); o0 = swp16_add(o0, o0); o1 = swp16_add(o1, o1);
      Ls = swp32_add(Ls, Ls); o0 = swp32_add(o0, o0); o1 = swp32_add(o1, o1);
      const float inv = 1.f / Ls;
      const unsigned op = pkbf(o0 * inv, o1 * inv);
      float ya[8];
#pragma unroll
      for (int c = 0; c < 8; ++c) ya[c] = 0.f;
#pragma unroll
      for (int i = 0; i < 8; ++i) {
        const unsigned oi = (unsigned)__builtin_amdgcn_readlane((int)op, i);
#pragma unroll
        for (int c = 0; c < 8; ++c) ya[c] = dot2(wrow[2 * i + (c >> 2)][c & 3], oi, ya[c]);
      }
      if (lane < 56) {
        *(LAS f32x4*)(L->yred[w] + lane * 8) = f32x4{g * ya[0], g * ya[1], g * ya[2], g * ya[3]};
        *(LAS f32x4*)(L->yred[w] + lane * 8 + 4) = f32x4{g * ya[4], g * ya[5], g * ya[6], g * ya[7]};
      }
      STAMP(7);
      WG_BARRIER();
      const int t = tid - 256;
      if (t < 112) {
        f32x4 y = *(const LAS f32x4*)(L->yred[0] + t * 4);
#pragma unroll
        for (int ww = 1; ww < 5; ++ww) y += *(const LAS f32x4*)(L->yred[ww] + t * 4);
        const v4u yv = {san(y[0]), san(y[1]), san(y[2]), san(y[3])};
        __builtin_amdgcn_raw_buffer_store_b128(yv, rs, WS_Y + gid * (DM * 4) + t * 16, 0, 16);
      }
      STAMP(8);
    } else {
      if (wave == 2) {
        const unsigned thi = (unsigned)(tag >> 32), tlo = (unsigned)tag;
        v4u g[4];
        for (unsigned spins = 0;; ++spins) {
          bool ok = true;
#pragma unroll
          for (int k = 0; k < 4; ++k) g[k] = __builtin_amdgcn_raw_buffer_load_b128(rs, WS_QG + (gid & ~7) * 512 + k * 1024 + lane * 16, 0, 16);
#pragma unroll
          for (int k = 0; k < 4; ++k) ok &= g[k][1] == thi && g[k][3] == thi && (g[k][0] & 0xffff0000u) == tlo && (g[k][2] & 0xffff0000u) == tlo;
          if (__all(ok) || spins > SPIN_CAP) break;
          __builtin_amdgcn_s_sleep(1);
        }
#pragma unroll
        for (int k = 0; k < 4; ++k) L->q[k * 64 + lane] = perm_lo(g[k][0], g[k][2]);
        LGKM0();
        *(volatile LAS unsigned*)&L->qready = 1u;
        STAMP(3);
        const v4u sv = {SENT, SENT, SENT, SENT};
#pragma unroll
        for (int k = 0; k < 4; ++k) __builtin_amdgcn_raw_buffer_store_b128(sv, rs, WS_PO + gid * 4096 + k * 1024 + lane * 16, 0, 16);
        __builtin_amdgcn_raw_buffer_store_b128(sv, rs, WS_Y + gid * (DM * 4) + lane * 16, 0, 16);
        __builtin_amdgcn_raw_buffer_store_b128(sv, rs, lane < 48 ? WS_Y + gid * (DM * 4) + 1024 + lane * 16 : WS_END, 0, 16);
        DRAIN();
        if (lane == 0) __hip_atomic_store((GAS u64*)(ws + WS_RDY) + gid, ep + 1, RLX_AGENT);
#pragma unroll 1
        for (unsigned spins = 0; spins < SPIN_CAP; ++spins) {
          __builtin_amdgcn_s_sleep(127);
          const u64 gv = __hip_atomic_load((GAS u64*)(ws + WS_RDY) + b * 64 + lane, RLX_AGENT);
          if (__all(gv == ep + 1)) break;
        }
        if (j == 0 && lane == 0) __hip_atomic_store((GAS u64*)(ws + WS_EPOCH + b * 256), ep + 1, RLX_AGENT);
      } else {
        wait_ge(&L->qready, 1);
        float z = 0.f;
#pragma unroll
        for (int k = 0; k < 4; ++k) {
          const int idx = lane + 64 * k;
          const bool in = idx < DM / 2;
          const unsigned xv = x32[in ? idx : 0], gv = ((const GAS unsigned*)gw)[in ? idx : 0];
          const float t = rbf(bflo(xv) * bflo(gv)) + rbf(bfhi(xv) * bfhi(gv));
          z += in ? t : 0.f;
        }
        z = swp32_add(z, z); z = swp16_add(z, z); z += dpp<XOR8>(z); z += dpp<XOR7>(z); z += dpp<XOR2>(z); z += dpp<XOR1>(z);
        const float g = rbf(1.f / (1.f + __expf(-rbf(z))));
        if (lane == 0) L->gate = g;
        float yb[8];
#pragma unroll
        for (int c = 0; c < 8; ++c) yb[c] = 0.f;
        const int ms = GMAP ? s : kvh, mx = GMAP ? kvh : (blk & 7);
        if (ms < 7 && !(ABL & 1)) {
          const int u0 = (mx * 7 + ms) * 32, rr = lane >> 2;
          float ha[8];
#pragma unroll
          for (int c = 0; c < 8; ++c) ha[c] = 0.f;
          const GAS u16* w1p = (const GAS u16*)w1 + (size_t)(2 * rr) * DFF + u0 + (lane & 3) * 8;
#pragma unroll 1
          for (int h = 0; h < 2; ++h) {
            v4u ma[14]; unsigned xq[7];
#pragma unroll
            for (int k = 0; k < 7; ++k) {
              ma[2 * k] = ldw(w1p + (size_t)((7 * h + k) * 32) * DFF);
              ma[2 * k + 1] = ldw(w1p + (size_t)((7 * h + k) * 32 + 1) * DFF);
              xq[k] = x32[(7 * h + k) * 16 + rr];
            }
#pragma unroll
            for (int k = 0; k < 7; ++k) pair_dot(ma[2 * k], ma[2 * k + 1], xq[k], ha);
          }
          float hv = tr8(ha, lane);
          hv += dpp<SHR4>(hv);
          hv = fmaxf(rbf(hv), 0.f);
#pragma unroll 1
          for (int c4 = 0; c4 < 4; ++c4) {
            v4u w2r[8];
#pragma unroll
            for (int u = 0; u < 8; ++u) w2r[u] = ldw((const GAS u16*)w2 + (size_t)(u0 + c4 * 8 + u) * DM + cl * 8);
#pragma unroll
            for (int u = 0; u < 8; ++u) {
              const float hu = __builtin_bit_cast(float, __builtin_amdgcn_readlane(__builtin_bit_cast(int, hv), u * 8 + 4 + c4));
#pragma unroll
              for (int c = 0; c < 4; ++c) { yb[2 * c] = fmaf(hu, bflo(w2r[u][c]), yb[2 * c]); yb[2 * c + 1] = fmaf(hu, bfhi(w2r[u][c]), yb[2 * c + 1]); }
            }
          }
        }
        const float omg = rbf(1.f - g);
        if (lane < 56) {
          *(LAS f32x4*)(L->yred[4] + lane * 8) = f32x4{omg * yb[0], omg * yb[1], omg * yb[2], omg * yb[3]};
          *(LAS f32x4*)(L->yred[4] + lane * 8 + 4) = f32x4{omg * yb[4], omg * yb[5], omg * yb[6], omg * yb[7]};
        }
      }
      STAMP(4);
      WG_BARRIER();
      merge_publish(L, rs, gid, wave, lane);
      STAMP(5);
      WG_BARRIER();
    }
  }
  if (j < 14) {
    const v4u y = poll16(srd, WS_Y + (b * 64 + wave * 8 + (lane >> 3)) * (DM * 4) + j * 128 + (lane & 7) * 16);
    STAMP(9);
    const float r0 = swp32_add(__uint_as_float(y[0]), __uint_as_float(y[2])), r1 = swp32_add(__uint_as_float(y[1]), __uint_as_float(y[3]));
    float r = swp16_add(r0, r1);
    r += dpp<XOR8>(r);
    if (!(lane & 8)) L->fin[wave][(lane & 7) * 4 + (lane >> 4)] = r;
    WG_BARRIER();
    if (tid < 32) {
      float t = L->fin[0][tid];
#pragma unroll
      for (int k = 1; k < 8; ++k) t += L->fin[k][tid];
      out[b * DM + j * 32 + tid] = (u16)pkbf(t, 0.f);
    }
  }
  STAMP_OUT();
  DRAIN();
}

extern "C" void kernel_launch(void* const* d_in, const int* in_sizes, int n_in, void* d_out, int out_size, void* d_ws, size_t ws_size, hipStream_t stream) {
  fused_kernel<<<256, 512, 0, stream>>>((const u16*)d_in[0], (const u16*)d_in[1], (const u16*)d_in[2], (const u16*)d_in[3], (const u16*)d_in[4],
                                        (const u16*)d_in[6], (const u16*)d_in[7], (const u16*)d_in[8], (unsigned char*)d_ws, (u16*)d_out);
}
```

```cpp
#include <hip/hip_runtime.h>
#include <hip/hip_bf16.h>
#include <cstdint>

#define W_NT 0
#define DEPTH 13
#define PREH 1
#define DEPTH2 20
#define HSW 24
#define SHARE 2
#define MLPMAP 1
#define WOB 8
#define WSP 12
#define DEPTHV DEPTH
#define GMAP 0
#define STAMPS 0

typedef unsigned v4u __attribute__((ext_vector_type(4)));
typedef unsigned v3u __attribute__((ext_vector_type(3)));
typedef unsigned v2u __attribute__((ext_vector_type(2)));
typedef float f32x4 __attribute__((ext_vector_type(4)));
typedef __bf16 bf16x2 __attribute__((ext_vector_type(2)));
typedef __bf16 bf16x8 __attribute__((ext_vector_type(8)));
typedef short s16x4 __attribute__((ext_vector_type(4)));
typedef unsigned short u16;
typedef unsigned long long u64;
#define GAS __attribute__((address_space(1)))
#define LAS __attribute__((address_space(3)))
#define RLX_AGENT __ATOMIC_RELAXED, __HIP_MEMORY_SCOPE_AGENT

constexpr int DM = 448, SK = 8192, DH = 256, DFF = 1792, QLD = 4096;
constexpr unsigned SENT = 0xFFFFFFFFu;
constexpr unsigned SPIN_CAP = 1u << 18;
constexpr int WS_EPOCH = 0;
constexpr int WS_RDY = 4096;
constexpr int WS_QG = 8192;
constexpr int WS_ST = WS_QG + 256 * 64 * 8;
constexpr int WS_S = WS_ST + 256 * 128;
constexpr int WS_Y = WS_S + 256 * 4096;
constexpr int WS_END = WS_Y + 256 * DM * 4;
constexpr size_t WS_STAMP = 16u << 20;

__device__ __forceinline__ float bflo(unsigned u) { return __builtin_bit_cast(float, u << 16); }
__device__ __forceinline__ float bfhi(unsigned u) { return __builtin_bit_cast(float, u & 0xffff0000u); }
__device__ __forceinline__ unsigned pkbf(float lo, float hi) {
  bf16x2 r; r[0] = (__bf16)lo; r[1] = (__bf16)hi; return __builtin_bit_cast(unsigned, r);
}
__device__ __forceinline__ float rbf(float f) { return bflo(pkbf(f, 0.f)); }
__device__ __forceinline__ float dot2(unsigned a, unsigned b, float acc) {
  return __builtin_amdgcn_fdot2_f32_bf16(__builtin_bit_cast(bf16x2, a), __builtin_bit_cast(bf16x2, b), acc, false);
}
__device__ __forceinline__ unsigned perm_lo(unsigned a, unsigned b) { return __builtin_amdgcn_perm(b, a, 0x05040100u); }
__device__ __forceinline__ unsigned perm_hi(unsigned a, unsigned b) { return __builtin_amdgcn_perm(b, a, 0x07060302u); }
template <int CTRL> __device__ __forceinline__ float dpp(float x) {
  return __builtin_bit_cast(float, __builtin_amdgcn_mov_dpp(__builtin_bit_cast(int, x), CTRL, 0xf, 0xf, true));
}
constexpr int XOR1 = 0xB1, XOR2 = 0x4E, XOR7 = 0x141, XOR8 = 0x128, ROR4 = 0x124, SHR4 = 0x114;
__device__ __forceinline__ float swp16_add(float a, float b) {
  auto s = __builtin_amdgcn_permlane16_swap(__float_as_uint(a), __float_as_uint(b), false, false);
  return __uint_as_float(s[0]) + __uint_as_float(s[1]);
}
__device__ __forceinline__ float swp32_add(float a, float b) {
  auto s = __builtin_amdgcn_permlane32_swap(__float_as_uint(a), __float_as_uint(b), false, false);
  return __uint_as_float(s[0]) + __uint_as_float(s[1]);
}
__device__ __forceinline__ float swp16_max(float a) {
  auto s = __builtin_amdgcn_permlane16_swap(__float_as_uint(a), __float_as_uint(a), false, false);
  return fmaxf(__uint_as_float(s[0]), __uint_as_float(s[1]));
}
__device__ __forceinline__ float swp32_max(float a) {
  auto s = __builtin_amdgcn_permlane32_swap(__float_as_uint(a), __float_as_uint(a), false, false);
  return fmaxf(__uint_as_float(s[0]), __uint_as_float(s[1]));
}
template <int CTRL> __device__ __forceinline__ float tr_dpp(float lo, float hi, bool sel) {
  const float t1 = lo + dpp<CTRL>(lo), t2 = hi + dpp<CTRL>(hi);
  return sel ? t2 : t1;
}
__device__ __forceinline__ float tr8(float (&a)[8], int lane) {
#pragma unroll
  for (int i = 0; i < 4; ++i) a[i] = swp32_add(a[i], a[i + 4]);
#pragma unroll
  for (int i = 0; i < 2; ++i) a[i] = swp16_add(a[i], a[i + 2]);
  return tr_dpp<XOR8>(a[0], a[1], lane & 8);
}
__device__ __forceinline__ void pair_dot(const v4u& ra, const v4u& rb, unsigned xp, float (&a)[8]) {
#pragma unroll
  for (int c = 0; c < 4; ++c) {
    a[2 * c] = dot2(perm_lo(ra[c], rb[c]), xp, a[2 * c]);
    a[2 * c + 1] = dot2(perm_hi(ra[c], rb[c]), xp, a[2 * c + 1]);
  }
}
__device__ __forceinline__ unsigned san(float f) { const unsigned u = __float_as_uint(f); return u == SENT ? 0x7FC00000u : u; }
__device__ __forceinline__ v4u ldw(const GAS void* p) {
  return W_NT ? __builtin_nontemporal_load((const GAS v4u*)p) : *(const GAS v4u*)p;
}
#define WG_BARRIER() do { asm volatile("s_waitcnt lgkmcnt(0)" ::: "memory"); __builtin_amdgcn_s_barrier(); asm volatile("" ::: "memory"); __builtin_amdgcn_sched_barrier(0); } while (0)
#define DRAIN() asm volatile("s_waitcnt vmcnt(0)" ::: "memory")
#define STAMP(i) do {} while (0)
#define STAMP_OUT() do {} while (0)

struct Lds {
  unsigned char ring[2][8][8192];
  unsigned P[4][4][2][128];
  unsigned stage[4][2][128];
  unsigned xstage[2][32];
  unsigned q[256];
  float yb[DM];
  float qpart[6][64];
  float fin[8][32];
  unsigned ob[4][68];
  float mref[4][2];
  float fscale[2];
  float gate, pad0;
  unsigned freed[2][8];
  unsigned full[2];
  unsigned cnt, qready;
  unsigned kdone[4];
  unsigned pready, pad1[3];
};

#define PIN16(a) asm volatile("" : "+v"(a[0]), "+v"(a[1]), "+v"(a[2]), "+v"(a[3]), "+v"(a[4]), "+v"(a[5]), "+v"(a[6]), "+v"(a[7]), \
                                   "+v"(a[8]), "+v"(a[9]), "+v"(a[10]), "+v"(a[11]), "+v"(a[12]), "+v"(a[13]), "+v"(a[14]), "+v"(a[15]))
#define PIN8(a) asm volatile("" : "+v"(a[0]), "+v"(a[1]), "+v"(a[2]), "+v"(a[3]), "+v"(a[4]), "+v"(a[5]), "+v"(a[6]), "+v"(a[7]))
#define LGKM0() asm volatile("s_waitcnt lgkmcnt(0)" ::: "memory")

__device__ __forceinline__ void wait_ge(volatile LAS unsigned* w, unsigned need) {
#pragma unroll 1
  for (unsigned spins = 0; spins < (1u << 22); ++spins) {
    if ((unsigned)__builtin_amdgcn_readfirstlane(*w) >= need) break;
    __builtin_amdgcn_s_sleep(1);
  }
  asm volatile("" ::: "memory");
}

#define PTHR 16
#define PSLOW 10
#define PSLEEP 2
__device__ __forceinline__ v4u poll16(const v4u& srd, int off) {
  unsigned c0, c1, c2, c3, mx, cnt;
  asm volatile(
      "s_waitcnt vmcnt(0)\n\t"
      "v_mov_b32 v252, -1\n\tv_mov_b32 v253, -1\n\tv_mov_b32 v254, -1\n\tv_mov_b32 v255, -1\n\t"
      "s_mov_b32 %5, 0x200000\n\t"
      "s_nop 1\n"
      "1:\n\t"
      "buffer_load_dwordx4 v[252:255], %6, %7, 0 offen sc1\n\t"
      "s_sleep %8\n\t"
      "s_waitcnt vmcnt(12)\n\t"
      "v_mov_b32 %0, v252\n\tv_mov_b32 %1, v253\n\tv_mov_b32 %2, v254\n\tv_mov_b32 %3, v255\n\t"
      "v_max3_u32 %4, %0, %1, %2\n\t"
      "v_max_u32 %4, %4, %3\n\t"
      "v_cmp_eq_u32 vcc, -1, %4\n\t"
      "s_and_b64 exec, exec, vcc\n\t"
      "s_cbranch_scc0 2f\n\t"
      "s_bcnt1_i32_b64 vcc_lo, exec\n\t"
      "s_cmp_le_u32 vcc_lo, %9\n\t"
      "s_cbranch_scc1 3f\n\t"
      "s_sleep %10\n"
      "3:\n\t"
      "s_sub_u32 %5, %5, 1\n\t"
      "s_cbranch_scc0 1b\n"
      "2:\n\t"
      "s_mov_b64 exec, -1\n\t"
      "s_nop 4"
      : "=&v"(c0), "=&v"(c1), "=&v"(c2), "=&v"(c3), "=&v"(mx), "=&s"(cnt)
      : "v"(off), "s"(srd), "i"(PSLEEP), "i"(PTHR), "i"(PSLOW)
      : "vcc", "scc", "v252", "v253", "v254", "v255", "memory");
  return v4u{c0, c1, c2, c3};
}

template <int D, int AUX>
__device__ __forceinline__ void issue8(const char* src, int half, unsigned char* dst, const unsigned (&voff)[4], int n, int& pub,
                                       volatile LAS unsigned* full, volatile LAS unsigned* fr) {
  if (n >= 8) {
#pragma unroll 1
    for (unsigned spins = 0; spins < (1u << 22); ++spins) {
      if ((int)__builtin_amdgcn_readfirstlane(*fr) >= n - 7) break;
      DRAIN();
      pub = n; *full = pub;
      __builtin_amdgcn_s_sleep(1);
    }
    asm volatile("" ::: "memory");
  }
#pragma unroll
  for (int g = 0; g < 8; ++g) {
    asm volatile("s_waitcnt vmcnt(%0)" ::"i"(D - 1) : "memory");
    if (g == (D - 1) % 8) { pub = max(pub, n - (D - 1) / 8); *full = pub; }
    const char* sp = src + (g >> 2) * half;
    if ((g & 3) == 0) __builtin_amdgcn_global_load_lds((const unsigned*)(sp + voff[0]), (unsigned*)(dst + (g & 4) * 1024), 16, 0, AUX);
    if ((g & 3) == 1) __builtin_amdgcn_global_load_lds((const unsigned*)(sp + voff[1]), (unsigned*)(dst + (g & 4) * 1024), 16, 1024, AUX);
    if ((g & 3) == 2) __builtin_amdgcn_global_load_lds((const unsigned*)(sp + voff[2]), (unsigned*)(dst + (g & 4) * 1024), 16, 2048, AUX);
    if ((g & 3) == 3) __builtin_amdgcn_global_load_lds((const unsigned*)(sp + voff[3]), (unsigned*)(dst + (g & 4) * 1024), 16, 3072, AUX);
  }
}
__device__ __forceinline__ void read_tr(const LAS unsigned char* cell, unsigned a0, bf16x8* f) {
#pragma unroll
  for (int kb = 0; kb < 2; ++kb)
#pragma unroll
    for (int t = 0; t < 4; ++t) {
      const s16x4 lo = __builtin_amdgcn_ds_read_tr16_b64_v4i16((LAS s16x4*)(cell + ((a0 ^ (t << 5)) + kb * 4096)));
      const s16x4 hi = __builtin_amdgcn_ds_read_tr16_b64_v4i16((LAS s16x4*)(cell + ((a0 ^ (t << 5)) + kb * 4096 + 512)));
      f[kb * 4 + t] = __builtin_bit_cast(bf16x8, __builtin_shufflevector(lo, hi, 0, 1, 2, 3, 4, 5, 6, 7));
    }
}
__device__ __forceinline__ f32x4 sel4(f32x4 a0, f32x4 a1, f32x4 a2, f32x4 a3, bool b0, bool b1) {
  f32x4 r;
#pragma unroll
  for (int e = 0; e < 4; ++e) { const float lo = b0 ? a1[e] : a0[e], hi = b0 ? a3[e] : a2[e]; r[e] = b1 ? hi : lo; }
  return r;
}

__global__ void __launch_bounds__(512, 2) fused_kernel(const u16* __restrict__ x, const u16* __restrict__ Kc, const u16* __restrict__ Vc,
    const u16* __restrict__ wq, const u16* __restrict__ wo, const u16* __restrict__ w1, const u16* __restrict__ w2,
    const u16* __restrict__ gw, unsigned char* ws, u16* out) {
  __shared__ __attribute__((aligned(16))) Lds sm;
  LAS Lds* L = (LAS Lds*)&sm;
  asm volatile("" ::"s"(x), "s"(Kc), "s"(Vc), "s"(wq), "s"(wo), "s"(w1), "s"(w2), "s"(gw), "s"(ws), "s"(out));
  const int tid = threadIdx.x, lane = tid & 63, wave = __builtin_amdgcn_readfirstlane(tid >> 6);
  const int blk = blockIdx.x, b = blk >> 6;
  const int kvh = ((blk >> 3) & 6) | ((blk >> 2) & 1), kh = (blk >> 3) & 1, qd = (blk + (blk >> 4)) & 3;
  const int s = kh * 4 + qd;
  const int j = kvh * 8 + s;
  const int gid = b * 64 + j;
  const __amdgpu_buffer_rsrc_t rs = __builtin_amdgcn_make_buffer_rsrc((void*)ws, 0, WS_END, 0x00020000);
  const v4u srd = {(unsigned)(size_t)ws, (unsigned)((size_t)ws >> 32) & 0xffffu, (unsigned)WS_END, 0x00020000u};
  const int nb = qd == 3 ? 8 - SHARE : 8, nk = 4 * nb + SHARE, xq = (qd + 1) & 3;
  const char* vbase = (const char*)Vc + ((size_t)((b * 8 + kvh) * SK + kh * 4096)) * (DH * 2) + qd * 128;
  STAMP(0);
  if (wave < 2) {
    __builtin_amdgcn_s_setprio(3);
    if (tid < 28) L->freed[0][tid] = 0u;
    volatile LAS unsigned* full = &L->full[wave];
    const char* kb = (const char*)Kc + ((size_t)((b * 8 + kvh) * SK + s * 1024 + wave * 16)) * (DH * 2);
    unsigned vk[4], vv[4];
#pragma unroll
    for (int g = 0; g < 4; ++g) {
      vk[g] = (lane >> 5) * 512 + (((lane & 31) ^ (((2 * g + (lane >> 5)) & 7) << 1)) << 4);
      const unsigned ch = ((lane & 7) ^ (((lane >> 3) & 2) | ((g & 1) << 2))) << 4;
      vv[g] = (8 * g + (lane >> 3)) * 512 + ch - g * 1024;
    }
    int pub = 0;
#pragma unroll 1
    for (int n = 0; n < nk; ++n) {
      if (n == PREH) WG_BARRIER();
      const int cell = n >= 4 * nb ? (3 - qd) * 32 + 32 - 4 * SHARE + SHARE * xq + (n - 4 * nb) : n;
      issue8<DEPTH, 2>(kb + (size_t)cell * 16384, 4096, sm.ring[wave][n & 7], vk, n, pub, full, &L->freed[wave][n & 7]);
    }
    STAMP(1);
#pragma unroll 1
    for (int m = 0; m < 32; ++m) {
      const int n = nk + m;
      const char* src = vbase + (size_t)(((m >> 1) & 3) * 1024 + (m >> 3) * 256 + (2 * (m & 1) + wave) * 64) * 512;
      if (m < HSW) issue8<DEPTHV, 2>(src, 16384, sm.ring[wave][n & 7], vv, n, pub, full, &L->freed[wave][n & 7]);
      else issue8<DEPTH2, 2>(src, 16384, sm.ring[wave][n & 7], vv, n, pub, full, &L->freed[wave][n & 7]);
    }
#pragma unroll
    for (int k = (DEPTH2 - 1) / 8; k >= 0; --k) {
      asm volatile("s_waitcnt vmcnt(%0)" ::"i"(8 * k) : "memory");
      *full = max(pub, nk + 32 - k);
    }
    STAMP(2);
    WG_BARRIER();
  } else {
    const GAS unsigned* x32 = (const GAS unsigned*)(x + b * DM);
    const int wv = wave - 2;
    const u64 ep = *(const u64*)(ws + WS_EPOCH + b * 256);
    {
      const int r = lane >> 3;
      const GAS char* wqp = (const GAS char*)wq + (size_t)(2 * r) * (QLD * 2) + (j * 64 + (lane & 7) * 8) * 2;
      v4u wr[10]; unsigned xp[5];
#pragma unroll
      for (int k = 0; k < 5; ++k) {
        const int pg = min(wv + 6 * k, 27);
        xp[k] = x32[8 * pg + r];
        wr[2 * k] = ldw(wqp + (size_t)(16 * pg) * (QLD * 2));
        wr[2 * k + 1] = ldw(wqp + (size_t)(16 * pg + 1) * (QLD * 2));
      }
      __builtin_amdgcn_sched_barrier(0);
      __builtin_amdgcn_s_barrier();
      asm volatile("" ::: "memory");
      __builtin_amdgcn_sched_barrier(0);
      if (wv >= 4) xp[4] = 0u;
      STAMP(1);
      float qa[8];
#pragma unroll
      for (int c = 0; c < 8; ++c) qa[c] = 0.f;
#pragma unroll
      for (int k = 0; k < 5; ++k) pair_dot(wr[2 * k], wr[2 * k + 1], xp[k], qa);
      L->qpart[wv][(lane & 7) * 8 + r] = tr8(qa, lane);
    }
    const u64 tag = (ep + 1) << 16;
    LGKM0();
    unsigned arr = 0;
    if (lane == 0) arr = __hip_atomic_fetch_add(&L->cnt, 1u, __ATOMIC_RELAXED, __HIP_MEMORY_SCOPE_WORKGROUP);
    if (__builtin_amdgcn_readfirstlane(arr) == 5) {
      asm volatile("" ::: "memory");
      float qv = L->qpart[0][lane];
#pragma unroll
      for (int k = 1; k < 6; ++k) qv += L->qpart[k][lane];
      __hip_atomic_store((GAS u64*)(ws + WS_QG) + gid * 64 + lane, tag | (pkbf(qv, 0.f) & 0xffffu), RLX_AGENT);
    }
    STAMP(2);
    if (wave >= 4) {
      const int cw = wave - 4, w = cw & 1, par = cw >> 1;
      volatile LAS unsigned* full = &L->full[w];
      volatile LAS unsigned* fr = L->freed[w];
      const int g4 = lane >> 4, c16 = lane & 15;
      const unsigned ka0 = c16 * 512 + ((g4 ^ ((c16 & 7) << 1)) << 4);
      const unsigned ta0 = (8 * g4 + (c16 >> 2)) * 128 + ((((c16 >> 3) & 1) | ((g4 & 1) << 1)) << 5) + (lane & 3) * 8;
      {
        bf16x8 qf[8];
#pragma unroll 1
        for (int bk = 0; bk < nb; ++bk) {
          const int n0 = 4 * bk + par;
          bf16x8 kf[16];
#pragma unroll
          for (int h = 0; h < 2; ++h) {
            const int n = n0 + 2 * h;
            wait_ge(full, n + 1);
            const LAS unsigned char* cell = L->ring[w][n & 7];
#pragma unroll
            for (int t = 0; t < 8; ++t) kf[8 * h + t] = *(const LAS bf16x8*)(cell + ((ka0 ^ ((t & 3) << 6)) + (t >> 2) * 256));
            LGKM0(); fr[n & 7] = n + 1;
          }
          PIN16(kf);
          if (bk == 0) {
            wait_ge(&L->qready, 1);
            STAMP(3);
#pragma unroll
            for (int t = 0; t < 8; ++t) qf[t] = *(const LAS bf16x8*)(L->q + (lane & 1) * 128 + (4 * t + g4) * 4);
          }
          f32x4 sa = {0.f, 0.f, 0.f, 0.f}, sb = {0.f, 0.f, 0.f, 0.f};
#pragma unroll
          for (int t = 0; t < 8; ++t) {
            sa = __builtin_amdgcn_mfma_f32_16x16x32_bf16(kf[t], qf[t], sa, 0, 0, 0);
            sb = __builtin_amdgcn_mfma_f32_16x16x32_bf16(kf[8 + t], qf[t], sb, 0, 0, 0);
          }
          if (c16 < 4) {
            f32x4 sv;
#pragma unroll
            for (int e = 0; e < 4; ++e) sv[e] = (c16 & 2) ? sb[e] : sa[e];
            const int kc = 8 * (bk & 1) + 2 * par + w + 2 * (c16 & 2);
            *(LAS v2u*)&L->stage[bk >> 1][c16 & 1][kc * 8 + 2 * g4] = v2u{pkbf(sv[0], sv[1]), pkbf(sv[2], sv[3])};
          }
          LGKM0(); *(volatile LAS unsigned*)&L->kdone[cw] = bk + 1;
        }
        if (SHARE) {
          if (par < SHARE) {
            const int n = 4 * nb + par;
            wait_ge(full, n + 1);
            const LAS unsigned char* cell = L->ring[w][n & 7];
            f32x4 sa = {0.f, 0.f, 0.f, 0.f};
#pragma unroll
            for (int t = 0; t < 8; ++t)
              sa = __builtin_amdgcn_mfma_f32_16x16x32_bf16(*(const LAS bf16x8*)(cell + ((ka0 ^ ((t & 3) << 6)) + (t >> 2) * 256)), qf[t], sa, 0, 0, 0);
            if (c16 < 2) *(LAS v2u*)&L->xstage[c16][par * 16 + w * 8 + 2 * g4] = v2u{pkbf(sa[0], sa[1]), pkbf(sa[2], sa[3])};
            LGKM0(); fr[n & 7] = n + 1;
          }
          *(volatile LAS unsigned*)&L->kdone[cw] = 9u;
        }
      }
      STAMP(4);
      v4u wf[32];
      const GAS u16* wop = (const GAS u16*)wo + (size_t)(2 * kvh * 256 + 64 * qd + 8 * g4) * DM + min(128 * cw + 8 * c16, DM - 8);
      f32x4 acc[4];
#pragma unroll
      for (int t = 0; t < 4; ++t) acc[t] = f32x4{0.f, 0.f, 0.f, 0.f};
      float mprev = -INFINITY, fs = 0.f;
      auto vstep = [&](int mm) {
        const int n = nk + 2 * mm + par;
        if ((mm & 3) == 0) {
          wait_ge(&L->pready, (mm >> 2) + 1);
          const float mr = L->mref[mm >> 2][lane & 1], al = __builtin_amdgcn_exp2f(mprev - mr);
          mprev = mr;
#pragma unroll
          for (int t = 0; t < 4; ++t) acc[t] *= al;
        }
        if (mm == 15) { wait_ge(&L->pready, 5); fs = L->fscale[lane & 1]; }
        const LAS unsigned* pp = &L->P[mm >> 2][mm & 3][lane & 1][(2 * par + w) * 32 + 4 * g4];
        const bf16x8 p0 = *(const LAS bf16x8*)pp, p1 = *(const LAS bf16x8*)(pp + 16);
        wait_ge(full, n + 1);
        bf16x8 vf[8];
        read_tr(L->ring[w][n & 7], ta0, vf);
#pragma unroll
        for (int t = 0; t < 4; ++t) acc[t] = __builtin_amdgcn_mfma_f32_16x16x32_bf16(vf[t], p0, acc[t], 0, 0, 0);
#pragma unroll
        for (int t = 0; t < 4; ++t) acc[t] = __builtin_amdgcn_mfma_f32_16x16x32_bf16(vf[4 + t], p1, acc[t], 0, 0, 0);
        asm volatile("" : "+v"(acc[0]), "+v"(acc[1]), "+v"(acc[2]), "+v"(acc[3]));
        LGKM0(); fr[n & 7] = n + 1;
      };
#pragma unroll
      for (int hb = 0; hb < 32 / WOB; ++hb) {
#pragma unroll
        for (int i = WOB * hb; i < WOB * hb + WOB; ++i) wf[i] = ldw(wop + (size_t)((i >> 4) * 256 + ((i >> 3) & 1) * 32 + (i & 7)) * DM);
        __builtin_amdgcn_sched_barrier(0);
#pragma unroll 1
        for (int mm = hb * WOB * WSP / 32; mm < (hb + 1) * WOB * WSP / 32; ++mm) vstep(mm);
      }
#pragma unroll
      for (int kb = 0; kb < 4; ++kb) {
        v4u tf[8];
#pragma unroll
        for (int e = 0; e < 8; ++e)
#pragma unroll
          for (int d = 0; d < 4; ++d)
            tf[e][d] = (e & 1) ? perm_hi(wf[8 * kb + 2 * d][e >> 1], wf[8 * kb + 2 * d + 1][e >> 1]) : perm_lo(wf[8 * kb + 2 * d][e >> 1], wf[8 * kb + 2 * d + 1][e >> 1]);
        PIN8(tf);
#pragma unroll
        for (int e = 0; e < 8; ++e) wf[8 * kb + e] = tf[e];
      }
#pragma unroll 1
      for (int mm = WSP; mm < 16; ++mm) vstep(mm);
      STAMP(5);
      {
        const f32x4 o = sel4(acc[0], acc[1], acc[2], acc[3], c16 & 2, c16 & 4) * fs;
        if (c16 < 8) *(LAS v2u*)&L->ob[cw][(c16 & 1) * 32 + (c16 >> 1) * 8 + 2 * g4] = v2u{pkbf(o[0], o[1]), pkbf(o[2], o[3])};
      }
      WG_BARRIER();
      STAMP(6);
      const int col = 128 * cw + 8 * (4 * g4 + (c16 >> 2)) + 4 * (c16 & 1);
      const bool st_on = (c16 & 3) < 2 && col < DM;
      bf16x8 of[4];
#pragma unroll
      for (int kb = 0; kb < 4; ++kb) of[kb] = *(const LAS bf16x8*)&L->ob[c16 & 3][16 * kb + 4 * g4];
      const f32x4 ybv = *(const LAS f32x4*)&L->yb[st_on ? col : 0];
      const float g = L->gate;
      f32x4 ya[8];
#pragma unroll
      for (int e = 0; e < 8; ++e) ya[e] = f32x4{0.f, 0.f, 0.f, 0.f};
#pragma unroll
      for (int kb = 0; kb < 4; ++kb)
#pragma unroll
        for (int e = 0; e < 8; ++e) ya[e] = __builtin_amdgcn_mfma_f32_16x16x32_bf16(__builtin_bit_cast(bf16x8, wf[8 * kb + e]), of[kb], ya[e], 0, 0, 0);
      float yr[8];
#pragma unroll
      for (int e = 0; e < 8; ++e) { const float lo = (c16 & 4) ? ya[e][1] : ya[e][0], hi = (c16 & 4) ? ya[e][3] : ya[e][2]; yr[e] = (c16 & 8) ? hi : lo; }
      v4u yv;
#pragma unroll
      for (int r = 0; r < 4; ++r) {
        float v = tr_dpp<XOR1>(yr[r], yr[4 + r], c16 & 1);
        v += dpp<XOR2>(v);
        yv[r] = san(fmaf(g, v, ybv[r]));
      }
      __builtin_amdgcn_raw_buffer_store_b128(yv, rs, st_on ? WS_Y + gid * (DM * 4) + col * 4 : WS_END, 0, 16);
      STAMP(8);
    } else {
      if (wave == 2) {
        {
          const unsigned thi = (unsigned)(tag >> 32), tlo = (unsigned)tag;
          v4u g[4];
#pragma unroll 1
          for (unsigned spins = 0; spins < SPIN_CAP; ++spins) {
            bool ok = true;
#pragma unroll
            for (int k = 0; k < 4; ++k) g[k] = __builtin_amdgcn_raw_buffer_load_b128(rs, WS_QG + (gid & ~7) * 512 + k * 1024 + lane * 16, 0, 16);
#pragma unroll
            for (int k = 0; k < 4; ++k) ok &= g[k][1] == thi && g[k][3] == thi && (g[k][0] & 0xffff0000u) == tlo && (g[k][2] & 0xffff0000u) == tlo;
            if (__all(ok)) break;
            __builtin_amdgcn_s_sleep(1);
          }
#pragma unroll
          for (int k = 0; k < 4; ++k) L->q[k * 64 + lane] = perm_lo(g[k][0], g[k][2]);
          LGKM0();
          *(volatile LAS unsigned*)&L->qready = 1u;
        }
        STAMP(3);
        {
          const v4u sv = {SENT, SENT, SENT, SENT};
#pragma unroll
          for (int k = 0; k < 4; ++k) __builtin_amdgcn_raw_buffer_store_b128(sv, rs, WS_S + gid * 4096 + k * 1024 + lane * 16, 0, 16);
          __builtin_amdgcn_raw_buffer_store_b128(sv, rs, WS_Y + gid * (DM * 4) + lane * 16, 0, 16);
          __builtin_amdgcn_raw_buffer_store_b128(sv, rs, lane < 48 ? WS_Y + gid * (DM * 4) + 1024 + lane * 16 : (lane == 48 ? WS_ST + gid * 128 : WS_END), 0, 16);
          DRAIN();
          if (lane == 0) __hip_atomic_store((GAS u64*)(ws + WS_RDY) + gid, ep + 1, RLX_AGENT);
        }
        float mrun = -INFINITY, lrun = 0.f;
#pragma unroll 1
        for (int it = 0; it < 5; ++it) {
          if (it < 4) {
#pragma unroll
            for (int c = 0; c < 4; ++c) wait_ge(&L->kdone[c], (SHARE && it == 3) ? 9 : 2 * it + 2);
            v4u sv = *(const LAS v4u*)&L->stage[it][0][lane * 4];
#pragma unroll
            for (int k = 0; k < 4; ++k) sv[k] = sv[k] == SENT ? 0x7FC07FC0u : sv[k];
            const bool own = !(it == 3 && nb < 8 && (lane & 31) >= 32 - 16 * SHARE);
            __builtin_amdgcn_raw_buffer_store_b128(sv, rs, own ? WS_S + gid * 4096 + it * 1024 + lane * 16 : WS_END, 0, 16);
            if (SHARE && it == 3) {
              sv = *(const LAS v4u*)&L->xstage[0][(lane & 15) * 4];
#pragma unroll
              for (int k = 0; k < 4; ++k) sv[k] = sv[k] == SENT ? 0x7FC07FC0u : sv[k];
              __builtin_amdgcn_raw_buffer_store_b128(sv, rs, lane < 16 && (lane & 7) < 4 * SHARE ? WS_S + (gid | 3) * 4096 + 3072 + (lane >> 3) * 512 + 512 - 256 * SHARE + xq * 64 * SHARE + (lane & 7) * 16 : WS_END, 0, 16);
            }
          }
          if (it == 0) continue;
          if (it == 1) {
#pragma unroll 1
            for (unsigned spins = 0; spins < SPIN_CAP; ++spins) {
              const u64 gv = __hip_atomic_load((GAS u64*)(ws + WS_RDY) + b * 64 + lane, RLX_AGENT);
              if (__all(gv == ep + 1)) break;
              __builtin_amdgcn_s_sleep(32);
            }
            if (j == 0 && lane == 0) __hip_atomic_store((GAS u64*)(ws + WS_EPOCH + b * 256), ep + 1, RLX_AGENT);
          }
          const int gen = it - 1;
          v4u g[4];
#pragma unroll 1
          for (unsigned spins = 0; spins < SPIN_CAP; ++spins) {
            unsigned mx = 0u;
#pragma unroll
            for (int k = 0; k < 4; ++k) g[k] = __builtin_amdgcn_raw_buffer_load_b128(rs, WS_S + ((gid & ~3) + k) * 4096 + gen * 1024 + lane * 16, 0, 16);
#pragma unroll
            for (int k = 0; k < 4; ++k) mx = max(max(mx, max(g[k][0], g[k][1])), max(g[k][2], g[k][3]));
            if (__all(mx != SENT)) break;
            __builtin_amdgcn_s_sleep(8);
          }
          float cm = -INFINITY;
#pragma unroll
          for (int k = 0; k < 4; ++k)
#pragma unroll
            for (int e = 0; e < 4; ++e) cm = fmaxf(cm, fmaxf(bflo(g[k][e]), bfhi(g[k][e])));
          cm = fmaxf(cm, dpp<XOR1>(cm)); cm = fmaxf(cm, dpp<XOR2>(cm)); cm = fmaxf(cm, dpp<ROR4>(cm)); cm = fmaxf(cm, dpp<XOR8>(cm)); cm = swp16_max(cm);
          constexpr float C2 = 0.0625f * 1.4426950408889634f;
          const float mn = fmaxf(mrun, cm * C2);
          float ls = 0.f;
#pragma unroll
          for (int k = 0; k < 4; ++k) {
#pragma unroll
            for (int e = 0; e < 4; ++e) {
              const float pl = __builtin_amdgcn_exp2f(fmaf(bflo(g[k][e]), C2, -mn)), ph = __builtin_amdgcn_exp2f(fmaf(bfhi(g[k][e]), C2, -mn));
              ls += pl + ph;
              g[k][e] = pkbf(pl, ph);
            }
            *(LAS v4u*)&L->P[gen][k][0][lane * 4] = g[k];
          }
          lrun = fmaf(lrun, __builtin_amdgcn_exp2f(mrun - mn), ls);
          mrun = mn;
          if ((lane & 31) == 0) L->mref[gen][lane >> 5] = mn;
          LGKM0();
          *(volatile LAS unsigned*)&L->pready = gen + 1;
        }
        lrun += dpp<XOR1>(lrun); lrun += dpp<XOR2>(lrun); lrun += dpp<ROR4>(lrun); lrun += dpp<XOR8>(lrun); lrun = swp16_add(lrun, lrun);
        if ((lane & 31) == 0) __builtin_amdgcn_raw_buffer_store_b64(v2u{san(mrun), san(lrun)}, rs, WS_ST + gid * 128 + (lane >> 5) * 8, 0, 16);
        v2u o;
#pragma unroll 1
        for (unsigned spins = 0; spins < SPIN_CAP; ++spins) {
          o = __builtin_amdgcn_raw_buffer_load_b64(rs, WS_ST + (gid ^ 4) * 128 + (lane >> 5) * 8, 0, 16);
          if (__all(o[0] != SENT && o[1] != SENT)) break;
          __builtin_amdgcn_s_sleep(8);
        }
        const float mo = __uint_as_float(o[0]), M = fmaxf(mrun, mo), e0 = __builtin_amdgcn_exp2f(mrun - M);
        const float la = lrun * e0, lb = __uint_as_float(o[1]) * __builtin_amdgcn_exp2f(mo - M);
        if ((lane & 31) == 0) L->fscale[lane >> 5] = e0 / (la + lb);
        LGKM0();
        *(volatile LAS unsigned*)&L->pready = 5u;
      } else {
        wait_ge(&L->qready, 1);
        const int cl = lane < 56 ? lane : 55;
        {
          const unsigned t0 = *(const GAS unsigned*)vbase, t1 = *(const GAS u16*)out;
          asm volatile("" ::"v"(t0), "v"(t1));
        }
        float z = 0.f;
#pragma unroll
        for (int k = 0; k < 4; ++k) {
          const int idx = lane + 64 * k;
          const bool in = idx < DM / 2;
          const unsigned xv = x32[in ? idx : 0], gv = ((const GAS unsigned*)gw)[in ? idx : 0];
          const float t = rbf(bflo(xv) * bflo(gv)) + rbf(bfhi(xv) * bfhi(gv));
          z += in ? t : 0.f;
        }
        z = swp32_add(z, z); z = swp16_add(z, z); z += dpp<XOR8>(z); z += dpp<XOR7>(z); z += dpp<XOR2>(z); z += dpp<XOR1>(z);
        const float g = rbf(1.f / (1.f + __expf(-rbf(z))));
        if (lane == 0) L->gate = g;
        float yb[8];
#pragma unroll
        for (int c = 0; c < 8; ++c) yb[c] = 0.f;
        if (s != 3) {
          const int mx = blk & 7, y0 = (3 - mx) & 3;
          const int u0 = (GMAP == 1 ? (mx < 4 ? 6 * mx + kvh - (kvh > y0) - (kvh > y0 + 4) : 8 * mx - 8 + kvh) : (s == 7 ? 48 + kvh : kvh * 6 + 2 * qd + kh)) * 32, rr = lane >> 2;
          float ha[8];
#pragma unroll
          for (int c = 0; c < 8; ++c) ha[c] = 0.f;
          const GAS u16* w1p = (const GAS u16*)w1 + (size_t)(2 * rr) * DFF + u0 + (lane & 3) * 8;
#pragma unroll 1
          for (int h = 0; h < 2; ++h) {
            v4u ma[14]; unsigned xq[7];
#pragma unroll
            for (int k = 0; k < 7; ++k) {
              ma[2 * k] = ldw(w1p + (size_t)((7 * h + k) * 32) * DFF);
              ma[2 * k + 1] = ldw(w1p + (size_t)((7 * h + k) * 32 + 1) * DFF);
              xq[k] = x32[(7 * h + k) * 16 + rr];
            }
#pragma unroll
            for (int k = 0; k < 7; ++k) pair_dot(ma[2 * k], ma[2 * k + 1], xq[k], ha);
          }
          float hv = tr8(ha, lane);
          hv += dpp<SHR4>(hv);
          hv = fmaxf(rbf(hv), 0.f);
#pragma unroll 1
          for (int c4 = 0; c4 < 4; ++c4) {
            v4u w2r[8];
#pragma unroll
            for (int u = 0; u < 8; ++u) w2r[u] = ldw((const GAS u16*)w2 + (size_t)(u0 + c4 * 8 + u) * DM + cl * 8);
#pragma unroll
            for (int u = 0; u < 8; ++u) {
              const float hu = __builtin_bit_cast(float, __builtin_amdgcn_readlane(__builtin_bit_cast(int, hv), u * 8 + 4 + c4));
#pragma unroll
              for (int c = 0; c < 4; ++c) { yb[2 * c] = fmaf(hu, bflo(w2r[u][c]), yb[2 * c]); yb[2 * c + 1] = fmaf(hu, bfhi(w2r[u][c]), yb[2 * c + 1]); }
            }
          }
        }
        const float omg = rbf(1.f - g);
        if (lane < 56) {
          *(LAS f32x4*)(L->yb + lane * 8) = f32x4{omg * yb[0], omg * yb[1], omg * yb[2], omg * yb[3]};
          *(LAS f32x4*)(L->yb + lane * 8 + 4) = f32x4{omg * yb[4], omg * yb[5], omg * yb[6], omg * yb[7]};
        }
      }
      STAMP(4);
      WG_BARRIER();
    }
  }
  if (j < 14) {
    const v4u y = poll16(srd, WS_Y + (b * 64 + wave * 8 + (lane >> 3)) * (DM * 4) + j * 128 + (lane & 7) * 16);
    STAMP(9);
    const float r0 = swp32_add(__uint_as_float(y[0]), __uint_as_float(y[2])), r1 = swp32_add(__uint_as_float(y[1]), __uint_as_float(y[3]));
    float r = swp16_add(r0, r1);
    r += dpp<XOR8>(r);
    if (!(lane & 8)) L->fin[wave][(lane & 7) * 4 + (lane >> 4)] = r;
    WG_BARRIER();
    if (tid < 32) {
      float t = L->fin[0][tid];
#pragma unroll
      for (int k = 1; k < 8; ++k) t += L->fin[k][tid];
      out[b * DM + j * 32 + tid] = (u16)pkbf(t, 0.f);
    }
  }
  STAMP_OUT();
  DRAIN();
}

extern "C" void kernel_launch(void* const* d_in, const int* in_sizes, int n_in, void* d_out, int out_size, void* d_ws, size_t ws_size, hipStream_t stream) {
  fused_kernel<<<256, 512, 0, stream>>>((const u16*)d_in[0], (const u16*)d_in[1], (const u16*)d_in[2], (const u16*)d_in[3], (const u16*)d_in[4],
                                        (const u16*)d_in[6], (const u16*)d_in[7], (const u16*)d_in[8], (unsigned char*)d_ws, (u16*)d_out);
}
```

```cpp
#include <hip/hip_runtime.h>
#include <hip/hip_bf16.h>
#include <cstdint>

#define W_NT 0
#define DEPTH 13
#define PREH 1
#define DEPTH2 20
#define HSW 54
#define NB 2
#define HSLEEP 2
#define MLPMAP 1
#define PACER 1
#define DLO 10
#define QSLEEP 1
#define QWAIT 0
#define GMAP 0
#define STAMPS 0

typedef unsigned v4u __attribute__((ext_vector_type(4)));
typedef unsigned v3u __attribute__((ext_vector_type(3)));
typedef unsigned v2u __attribute__((ext_vector_type(2)));
typedef float f32x4 __attribute__((ext_vector_type(4)));
typedef __bf16 bf16x2 __attribute__((ext_vector_type(2)));
typedef __bf16 bf16x8 __attribute__((ext_vector_type(8)));
typedef short s16x4 __attribute__((ext_vector_type(4)));
typedef unsigned short u16;
typedef unsigned long long u64;
#define GAS __attribute__((address_space(1)))
#define LAS __attribute__((address_space(3)))
#define RLX_AGENT __ATOMIC_RELAXED, __HIP_MEMORY_SCOPE_AGENT

constexpr int DM = 448, SK = 8192, DH = 256, DFF = 1792, QLD = 4096;
constexpr unsigned SENT = 0xFFFFFFFFu;
constexpr unsigned SPIN_CAP = 1u << 18;
constexpr int WS_EPOCH = 0;
constexpr int WS_RDY = 4096;
constexpr int WS_QG = 8192;
constexpr int WS_S = WS_QG + 256 * 64 * 8;
constexpr int WS_PO = WS_S + 256 * 1024;
constexpr int WS_Y = WS_PO + 256 * 4096;
constexpr int WS_END = WS_Y + 256 * DM * 4;
constexpr size_t WS_STAMP = 16u << 20;

__device__ __forceinline__ float bflo(unsigned u) { return __builtin_bit_cast(float, u << 16); }
__device__ __forceinline__ float bfhi(unsigned u) { return __builtin_bit_cast(float, u & 0xffff0000u); }
__device__ __forceinline__ unsigned pkbf(float lo, float hi) {
  bf16x2 r; r[0] = (__bf16)lo; r[1] = (__bf16)hi; return __builtin_bit_cast(unsigned, r);
}
__device__ __forceinline__ float rbf(float f) { return bflo(pkbf(f, 0.f)); }
__device__ __forceinline__ float dot2(unsigned a, unsigned b, float acc) {
  return __builtin_amdgcn_fdot2_f32_bf16(__builtin_bit_cast(bf16x2, a), __builtin_bit_cast(bf16x2, b), acc, false);
}
__device__ __forceinline__ unsigned perm_lo(unsigned a, unsigned b) { return __builtin_amdgcn_perm(b, a, 0x05040100u); }
__device__ __forceinline__ unsigned perm_hi(unsigned a, unsigned b) { return __builtin_amdgcn_perm(b, a, 0x07060302u); }
template <int CTRL> __device__ __forceinline__ float dpp(float x) {
  return __builtin_bit_cast(float, __builtin_amdgcn_mov_dpp(__builtin_bit_cast(int, x), CTRL, 0xf, 0xf, true));
}
constexpr int XOR1 = 0xB1, XOR2 = 0x4E, XOR7 = 0x141, XOR8 = 0x128, ROR4 = 0x124, SHR4 = 0x114;
__device__ __forceinline__ float swp16_add(float a, float b) {
  auto s = __builtin_amdgcn_permlane16_swap(__float_as_uint(a), __float_as_uint(b), false, false);
  return __uint_as_float(s[0]) + __uint_as_float(s[1]);
}
__device__ __forceinline__ float swp32_add(float a, float b) {
  auto s = __builtin_amdgcn_permlane32_swap(__float_as_uint(a), __float_as_uint(b), false, false);
  return __uint_as_float(s[0]) + __uint_as_float(s[1]);
}
__device__ __forceinline__ float swp16_max(float a) {
  auto s = __builtin_amdgcn_permlane16_swap(__float_as_uint(a), __float_as_uint(a), false, false);
  return fmaxf(__uint_as_float(s[0]), __uint_as_float(s[1]));
}
__device__ __forceinline__ float swp32_max(float a) {
  auto s = __builtin_amdgcn_permlane32_swap(__float_as_uint(a), __float_as_uint(a), false, false);
  return fmaxf(__uint_as_float(s[0]), __uint_as_float(s[1]));
}
template <int CTRL> __device__ __forceinline__ float tr_dpp(float lo, float hi, bool sel) {
  const float t1 = lo + dpp<CTRL>(lo), t2 = hi + dpp<CTRL>(hi);
  return sel ? t2 : t1;
}
__device__ __forceinline__ float tr8(float (&a)[8], int lane) {
#pragma unroll
  for (int i = 0; i < 4; ++i) a[i] = swp32_add(a[i], a[i + 4]);
#pragma unroll
  for (int i = 0; i < 2; ++i) a[i] = swp16_add(a[i], a[i + 2]);
  return tr_dpp<XOR8>(a[0], a[1], lane & 8);
}
__device__ __forceinline__ float max32(float v) {
  v = fmaxf(v, dpp<XOR1>(v)); v = fmaxf(v, dpp<XOR2>(v)); v = fmaxf(v, dpp<ROR4>(v)); v = fmaxf(v, dpp<XOR8>(v));
  return swp16_max(v);
}
__device__ __forceinline__ void pair_dot(const v4u& ra, const v4u& rb, unsigned xp, float (&a)[8]) {
#pragma unroll
  for (int c = 0; c < 4; ++c) {
    a[2 * c] = dot2(perm_lo(ra[c], rb[c]), xp, a[2 * c]);
    a[2 * c + 1] = dot2(perm_hi(ra[c], rb[c]), xp, a[2 * c + 1]);
  }
}
__device__ __forceinline__ unsigned san(float f) { const unsigned u = __float_as_uint(f); return u == SENT ? 0x7FC00000u : u; }
__device__ __forceinline__ v4u ldw(const GAS void* p) {
  return W_NT ? __builtin_nontemporal_load((const GAS v4u*)p) : *(const GAS v4u*)p;
}
#define WG_BARRIER() do { asm volatile("s_waitcnt lgkmcnt(0)" ::: "memory"); __builtin_amdgcn_s_barrier(); asm volatile("" ::: "memory"); __builtin_amdgcn_sched_barrier(0); } while (0)
#define DRAIN() asm volatile("s_waitcnt vmcnt(0)" ::: "memory")
#define STAMP(i) do {} while (0)
#define STAMP_OUT() do {} while (0)

struct Lds {
  unsigned char ring[2][8][8192];
  unsigned P[4][2][128];
  unsigned PB[4][2][128];
  float mrg[4][512];
  float oa[128];
  unsigned q[256];
  float yb[DM];
  float qpart[6][64];
  float fin[8][32];
  unsigned ob[4][68];
  float mref[4][2];
  float fscale[2];
  float gate, pad0;
  float mlown[4];
  unsigned freed[2][8];
  unsigned full[2];
  unsigned cnt, qready;
  unsigned kdone[4];
  unsigned pready, acnt, pad1[2];
};

#define PIN16(a) asm volatile("" : "+v"(a[0]), "+v"(a[1]), "+v"(a[2]), "+v"(a[3]), "+v"(a[4]), "+v"(a[5]), "+v"(a[6]), "+v"(a[7]), \
                                   "+v"(a[8]), "+v"(a[9]), "+v"(a[10]), "+v"(a[11]), "+v"(a[12]), "+v"(a[13]), "+v"(a[14]), "+v"(a[15]))
#define PIN8(a) asm volatile("" : "+v"(a[0]), "+v"(a[1]), "+v"(a[2]), "+v"(a[3]), "+v"(a[4]), "+v"(a[5]), "+v"(a[6]), "+v"(a[7]))
#define LGKM0() asm volatile("s_waitcnt lgkmcnt(0)" ::: "memory")

__device__ __forceinline__ void wait_ge(volatile LAS unsigned* w, unsigned need) {
#pragma unroll 1
  for (unsigned spins = 0; spins < (1u << 22); ++spins) {
    if ((unsigned)__builtin_amdgcn_readfirstlane(*w) >= need) break;
    __builtin_amdgcn_s_sleep(1);
  }
  asm volatile("" ::: "memory");
}

#define PTHR 16
#define PSLOW 10
#define PSLEEP 2
__device__ __forceinline__ v4u poll16(const v4u& srd, int off) {
  unsigned c0, c1, c2, c3, mx, cnt;
  asm volatile(
      "s_waitcnt vmcnt(0)\n\t"
      "v_mov_b32 v252, -1\n\tv_mov_b32 v253, -1\n\tv_mov_b32 v254, -1\n\tv_mov_b32 v255, -1\n\t"
      "s_mov_b32 %5, 0x200000\n\t"
      "s_nop 1\n"
      "1:\n\t"
      "buffer_load_dwordx4 v[252:255], %6, %7, 0 offen sc1\n\t"
      "s_sleep %8\n\t"
      "s_waitcnt vmcnt(12)\n\t"
      "v_mov_b32 %0, v252\n\tv_mov_b32 %1, v253\n\tv_mov_b32 %2, v254\n\tv_mov_b32 %3, v255\n\t"
      "v_max3_u32 %4, %0, %1, %2\n\t"
      "v_max_u32 %4, %4, %3\n\t"
      "v_cmp_eq_u32 vcc, -1, %4\n\t"
      "s_and_b64 exec, exec, vcc\n\t"
      "s_cbranch_scc0 2f\n\t"
      "s_bcnt1_i32_b64 vcc_lo, exec\n\t"
      "s_cmp_le_u32 vcc_lo, %9\n\t"
      "s_cbranch_scc1 3f\n\t"
      "s_sleep %10\n"
      "3:\n\t"
      "s_sub_u32 %5, %5, 1\n\t"
      "s_cbranch_scc0 1b\n"
      "2:\n\t"
      "s_mov_b64 exec, -1\n\t"
      "s_nop 4"
      : "=&v"(c0), "=&v"(c1), "=&v"(c2), "=&v"(c3), "=&v"(mx), "=&s"(cnt)
      : "v"(off), "s"(srd), "i"(PSLEEP), "i"(PTHR), "i"(PSLOW)
      : "vcc", "scc", "v252", "v253", "v254", "v255", "memory");
  return v4u{c0, c1, c2, c3};
}

template <int D, int AUX>
__device__ __forceinline__ void issue8(const char* src, int half, unsigned char* dst, const unsigned (&voff)[4], int n, int& pub,
                                       volatile LAS unsigned* full, volatile LAS unsigned* fr) {
  if (n >= 8) {
#pragma unroll 1
    for (unsigned spins = 0; spins < (1u << 22); ++spins) {
      if ((int)__builtin_amdgcn_readfirstlane(*fr) >= n - 7) break;
      DRAIN();
      pub = n; *full = pub;
      __builtin_amdgcn_s_sleep(1);
    }
    asm volatile("" ::: "memory");
  }
#pragma unroll
  for (int g = 0; g < 8; ++g) {
    asm volatile("s_waitcnt vmcnt(%0)" ::"i"(D - 1) : "memory");
    if (g == (D - 1) % 8) { pub = max(pub, n - (D - 1) / 8); *full = pub; }
    const char* sp = src + (g >> 2) * half;
    if ((g & 3) == 0) __builtin_amdgcn_global_load_lds((const unsigned*)(sp + voff[0]), (unsigned*)(dst + (g & 4) * 1024), 16, 0, AUX);
    if ((g & 3) == 1) __builtin_amdgcn_global_load_lds((const unsigned*)(sp + voff[1]), (unsigned*)(dst + (g & 4) * 1024), 16, 1024, AUX);
    if ((g & 3) == 2) __builtin_amdgcn_global_load_lds((const unsigned*)(sp + voff[2]), (unsigned*)(dst + (g & 4) * 1024), 16, 2048, AUX);
    if ((g & 3) == 3) __builtin_amdgcn_global_load_lds((const unsigned*)(sp + voff[3]), (unsigned*)(dst + (g & 4) * 1024), 16, 3072, AUX);
  }
}
#define TAU 600
#define TOFF 0
#define TLAG 100000
__device__ __forceinline__ int pace(unsigned t0, int n) {
  if (!PACER) return 0;
  const int due = n < 8 ? 0 : TOFF + n * TAU / 10;
  int el = (int)((unsigned)__builtin_amdgcn_s_memrealtime() - t0);
  if (PACER == 2) { while (el < due) { __builtin_amdgcn_s_sleep(1); el = (int)((unsigned)__builtin_amdgcn_s_memrealtime() - t0); } }
  return el < due ? 1 : (el > due + TLAG ? 2 : 0);
}
__device__ __forceinline__ void read_tr(const LAS unsigned char* cell, unsigned a0, bf16x8* f) {
#pragma unroll
  for (int kb = 0; kb < 2; ++kb)
#pragma unroll
    for (int t = 0; t < 4; ++t) {
      const s16x4 lo = __builtin_amdgcn_ds_read_tr16_b64_v4i16((LAS s16x4*)(cell + ((a0 ^ (t << 5)) + kb * 4096)));
      const s16x4 hi = __builtin_amdgcn_ds_read_tr16_b64_v4i16((LAS s16x4*)(cell + ((a0 ^ (t << 5)) + kb * 4096 + 512)));
      f[kb * 4 + t] = __builtin_bit_cast(bf16x8, __builtin_shufflevector(lo, hi, 0, 1, 2, 3, 4, 5, 6, 7));
    }
}
__device__ __forceinline__ f32x4 sel4(f32x4 a0, f32x4 a1, f32x4 a2, f32x4 a3, bool b0, bool b1) {
  f32x4 r;
#pragma unroll
  for (int e = 0; e < 4; ++e) { const float lo = b0 ? a1[e] : a0[e], hi = b0 ? a3[e] : a2[e]; r[e] = b1 ? hi : lo; }
  return r;
}

__global__ void __launch_bounds__(512, 2) fused_kernel(const u16* __restrict__ x, const u16* __restrict__ Kc, const u16* __restrict__ Vc,
    const u16* __restrict__ wq, const u16* __restrict__ wo, const u16* __restrict__ w1, const u16* __restrict__ w2,
    const u16* __restrict__ gw, unsigned char* ws, u16* out) {
  __shared__ __attribute__((aligned(16))) Lds sm;
  LAS Lds* L = (LAS Lds*)&sm;
  asm volatile("" ::"s"(x), "s"(Kc), "s"(Vc), "s"(wq), "s"(wo), "s"(w1), "s"(w2), "s"(gw), "s"(ws), "s"(out));
  const int tid = threadIdx.x, lane = tid & 63, wave = __builtin_amdgcn_readfirstlane(tid >> 6);
  const int blk = blockIdx.x, b = blk >> 6;
  const int kvh = ((blk >> 3) & 6) | ((blk >> 2) & 1), kh = (blk >> 3) & 1, qd = (blk + (blk >> 4)) & 3;
  const int s = kh * 4 + qd;
  const int j = kvh * 8 + s;
  const int gid = b * 64 + j;
  const __amdgpu_buffer_rsrc_t rs = __builtin_amdgcn_make_buffer_rsrc((void*)ws, 0, WS_END, 0x00020000);
  const v4u srd = {(unsigned)(size_t)ws, (unsigned)((size_t)ws >> 32) & 0xffffu, (unsigned)WS_END, 0x00020000u};
  constexpr int NA = 64 - 4 * NB;
  const char* vbase = (const char*)Vc + ((size_t)((b * 8 + kvh) * SK + kh * 4096)) * (DH * 2) + qd * 128;
  STAMP(0);
  if (wave < 2) {
    __builtin_amdgcn_s_setprio(3);
    if (tid < 28) L->freed[0][tid] = 0u;
    volatile LAS unsigned* full = &L->full[wave];
    const char* kb = (const char*)Kc + ((size_t)((b * 8 + kvh) * SK + s * 1024 + wave * 16)) * (DH * 2);
    unsigned vk[4], vv[4];
#pragma unroll
    for (int g = 0; g < 4; ++g) {
      vk[g] = (lane >> 5) * 512 + (((lane & 31) ^ (((2 * g + (lane >> 5)) & 7) << 1)) << 4);
      const unsigned ch = ((lane & 7) ^ (((lane >> 3) & 2) | ((g & 1) << 2))) << 4;
      vv[g] = (8 * g + (lane >> 3)) * 512 + ch - g * 1024;
    }
    int pub = 0;
    const unsigned t0 = PACER ? (unsigned)__builtin_amdgcn_s_memrealtime() : 0u;
    const long vdelta = (const char*)Vc - (const char*)Kc - (32 - 4 * NB) * 16384;
#pragma unroll 1
    for (int n = 0; n < NA; ++n) {
      if (n == PREH) WG_BARRIER();
      const char* src = kb + (size_t)n * 16384 + (n < 32 ? 0 : vdelta);
      const int md = pace(t0, n);
      if (md == 1) issue8<DLO, 2>(src, 4096, sm.ring[wave][n & 7], vk, n, pub, full, &L->freed[wave][n & 7]);
      else if (md == 0 && n < HSW) issue8<DEPTH, 2>(src, 4096, sm.ring[wave][n & 7], vk, n, pub, full, &L->freed[wave][n & 7]);
      else issue8<DEPTH2, 2>(src, 4096, sm.ring[wave][n & 7], vk, n, pub, full, &L->freed[wave][n & 7]);
    }
    STAMP(1);
#pragma unroll 1
    for (int n = NA; n < 64; ++n) {
      const int m = n - NA;
      const char* src = vbase + (size_t)((m / NB) * 1024 + (m % NB) * 128 + wave * 64) * 512;
      const int md = pace(t0, n);
      if (md == 1) issue8<DLO, 2>(src, 16384, sm.ring[wave][n & 7], vv, n, pub, full, &L->freed[wave][n & 7]);
      else issue8<DEPTH2, 2>(src, 16384, sm.ring[wave][n & 7], vv, n, pub, full, &L->freed[wave][n & 7]);
    }
#pragma unroll
    for (int k = (DEPTH2 - 1) / 8; k >= 0; --k) {
      asm volatile("s_waitcnt vmcnt(%0)" ::"i"(8 * k) : "memory");
      *full = max(pub, 64 - k);
    }
    STAMP(2);
    WG_BARRIER();
  } else {
    const GAS unsigned* x32 = (const GAS unsigned*)(x + b * DM);
    const int wv = wave - 2;
    const u64 ep = *(const u64*)(ws + WS_EPOCH + b * 256);
    {
      const int r = lane >> 3;
      const GAS char* wqp = (const GAS char*)wq + (size_t)(2 * r) * (QLD * 2) + (j * 64 + (lane & 7) * 8) * 2;
      v4u wr[10]; unsigned xp[5];
#pragma unroll
      for (int k = 0; k < 5; ++k) {
        const int pg = min(wv + 6 * k, 27);
        xp[k] = x32[8 * pg + r];
        wr[2 * k] = ldw(wqp + (size_t)(16 * pg) * (QLD * 2));
        wr[2 * k + 1] = ldw(wqp + (size_t)(16 * pg + 1) * (QLD * 2));
      }
      __builtin_amdgcn_sched_barrier(0);
      __builtin_amdgcn_s_barrier();
      asm volatile("" ::: "memory");
      __builtin_amdgcn_sched_barrier(0);
      if (wv >= 4) xp[4] = 0u;
      STAMP(1);
      float qa[8];
#pragma unroll
      for (int c = 0; c < 8; ++c) qa[c] = 0.f;
#pragma unroll
      for (int k = 0; k < 5; ++k) pair_dot(wr[2 * k], wr[2 * k + 1], xp[k], qa);
      L->qpart[wv][(lane & 7) * 8 + r] = tr8(qa, lane);
    }
    const u64 tag = (ep + 1) << 16;
    LGKM0();
    unsigned arr = 0;
    if (lane == 0) arr = __hip_atomic_fetch_add(&L->cnt, 1u, __ATOMIC_RELAXED, __HIP_MEMORY_SCOPE_WORKGROUP);
    if (__builtin_amdgcn_readfirstlane(arr) == 5) {
      asm volatile("" ::: "memory");
      float qv = L->qpart[0][lane];
#pragma unroll
      for (int k = 1; k < 6; ++k) qv += L->qpart[k][lane];
      __hip_atomic_store((GAS u64*)(ws + WS_QG) + gid * 64 + lane, tag | (pkbf(qv, 0.f) & 0xffffu), RLX_AGENT);
    }
    STAMP(2);
    if (wave >= 4) {
      const int cw = wave - 4, w = cw & 1, par = cw >> 1;
      volatile LAS unsigned* full = &L->full[w];
      volatile LAS unsigned* fr = L->freed[w];
      const int g4 = lane >> 4, c16 = lane & 15;
      const unsigned ka0 = c16 * 512 + ((g4 ^ ((c16 & 7) << 1)) << 4);
      const unsigned ta0 = (8 * g4 + (c16 >> 2)) * 128 + ((((c16 >> 3) & 1) | ((g4 & 1) << 1)) << 5) + (lane & 3) * 8;
      {
        bf16x8 qf[8];
#pragma unroll 1
        for (int bk = 0; bk < 8; ++bk) {
          const int n0 = 4 * bk + par;
          bf16x8 kf[16];
#pragma unroll
          for (int h = 0; h < 2; ++h) {
            const int n = n0 + 2 * h;
            wait_ge(full, n + 1);
            const LAS unsigned char* cell = L->ring[w][n & 7];
#pragma unroll
            for (int t = 0; t < 8; ++t) kf[8 * h + t] = *(const LAS bf16x8*)(cell + ((ka0 ^ ((t & 3) << 6)) + (t >> 2) * 256));
            LGKM0(); fr[n & 7] = n + 1;
          }
          PIN16(kf);
          if (bk == 0) {
            wait_ge(&L->qready, 1);
            STAMP(3);
#pragma unroll
            for (int t = 0; t < 8; ++t) qf[t] = *(const LAS bf16x8*)(L->q + (lane & 1) * 128 + (4 * t + g4) * 4);
          }
          f32x4 sa = {0.f, 0.f, 0.f, 0.f}, sb = {0.f, 0.f, 0.f, 0.f};
#pragma unroll
          for (int t = 0; t < 8; ++t) {
            sa = __builtin_amdgcn_mfma_f32_16x16x32_bf16(kf[t], qf[t], sa, 0, 0, 0);
            sb = __builtin_amdgcn_mfma_f32_16x16x32_bf16(kf[8 + t], qf[t], sb, 0, 0, 0);
          }
          if (c16 < 4) {
            f32x4 sv;
#pragma unroll
            for (int e = 0; e < 4; ++e) sv[e] = (c16 & 2) ? sb[e] : sa[e];
            const int kc = 8 * (bk & 1) + 2 * par + w + 2 * (c16 & 2);
            *(LAS v2u*)&L->P[bk >> 1][c16 & 1][kc * 8 + 2 * g4] = v2u{pkbf(sv[0], sv[1]), pkbf(sv[2], sv[3])};
          }
          LGKM0(); *(volatile LAS unsigned*)&L->kdone[cw] = bk + 1;
        }
      }
      STAMP(4);
      v4u wf[32];
      const GAS u16* wop = (const GAS u16*)wo + (size_t)(2 * kvh * 256 + 64 * qd + 8 * g4) * DM + min(128 * cw + 8 * c16, DM - 8);
      f32x4 acc0 = {0.f, 0.f, 0.f, 0.f}, acc1 = {0.f, 0.f, 0.f, 0.f};
      float mprev0 = -INFINITY, mprev1 = -INFINITY;
      const int vrow = 4 * g4 + (c16 >> 2);
      const unsigned va0 = vrow * 512 + ((vrow & 7) << 5) + (lane & 3) * 8;
      auto astep = [&](int aa) {
        const int n = 32 - 4 * NB + 2 * aa + par, gen = aa >> 2;
        if ((aa & 3) == 0 || aa == 2 * NB) {
          wait_ge(&L->pready, gen + 1);
          const float m0 = L->mref[gen][0], m1 = L->mref[gen][1];
          const float a0 = __builtin_amdgcn_exp2f(mprev0 - m0), a1 = __builtin_amdgcn_exp2f(mprev1 - m1);
          mprev0 = m0; mprev1 = m1;
          acc0 *= f32x4{a0, a1, a0, a1}; acc1 *= f32x4{a0, a1, a0, a1};
        }
        const v2u pv = *(const LAS v2u*)&L->P[gen][lane & 1][(4 * (aa & 3) + 2 * par + w) * 8 + 2 * g4];
        s16x4 pa[8];
#pragma unroll
        for (int v = 0; v < 8; ++v) { const bool on = (c16 >> 1) == v; pa[v] = __builtin_bit_cast(s16x4, v2u{on ? pv[0] : 0u, on ? pv[1] : 0u}); }
        PIN8(pa);
        wait_ge(full, n + 1);
        const LAS unsigned char* cell = L->ring[w][n & 7];
#pragma unroll
        for (int v = 0; v < 8; ++v) {
          acc0 = __builtin_amdgcn_mfma_f32_16x16x16bf16_1k(pa[v], __builtin_amdgcn_ds_read_tr16_b64_v4i16((LAS s16x4*)(cell + (va0 ^ (v << 5)))), acc0, 0, 0, 0);
          acc1 = __builtin_amdgcn_mfma_f32_16x16x16bf16_1k(pa[v], __builtin_amdgcn_ds_read_tr16_b64_v4i16((LAS s16x4*)(cell + ((va0 ^ (v << 5)) + 256))), acc1, 0, 0, 0);
        }
        asm volatile("" : "+v"(acc0), "+v"(acc1));
        LGKM0(); fr[n & 7] = n + 1;
      };
#pragma unroll
      for (int hb = 0; hb < 4; ++hb) {
#pragma unroll
        for (int i = 8 * hb; i < 8 * hb + 8; ++i) wf[i] = ldw(wop + (size_t)((i >> 4) * 256 + ((i >> 3) & 1) * 32 + (i & 7)) * DM);
        __builtin_amdgcn_sched_barrier(0);
#pragma unroll 1
        for (int aa = 2 * NB + 2 * hb; aa < 2 * NB + 2 * hb + 2; ++aa) astep(aa);
      }
#pragma unroll
      for (int kb = 0; kb < 4; ++kb) {
        v4u tf[8];
#pragma unroll
        for (int e = 0; e < 8; ++e)
#pragma unroll
          for (int d = 0; d < 4; ++d)
            tf[e][d] = (e & 1) ? perm_hi(wf[8 * kb + 2 * d][e >> 1], wf[8 * kb + 2 * d + 1][e >> 1]) : perm_lo(wf[8 * kb + 2 * d][e >> 1], wf[8 * kb + 2 * d + 1][e >> 1]);
        PIN8(tf);
#pragma unroll
        for (int e = 0; e < 8; ++e) wf[8 * kb + e] = tf[e];
      }
#pragma unroll 1
      for (int aa = 2 * NB + 8; aa < 16; ++aa) astep(aa);
#pragma unroll
      for (int r = 0; r < 4; ++r) {
        L->mrg[cw][(r & 1) * 256 + (2 * g4 + (r >> 1)) * 16 + c16] = acc0[r];
        L->mrg[cw][(r & 1) * 256 + 128 + (2 * g4 + (r >> 1)) * 16 + c16] = acc1[r];
      }
      LGKM0();
      if (lane == 0) __hip_atomic_fetch_add(&L->acnt, 1u, __ATOMIC_RELAXED, __HIP_MEMORY_SCOPE_WORKGROUP);
      STAMP(7);
      f32x4 acc[4];
#pragma unroll
      for (int t = 0; t < 4; ++t) acc[t] = f32x4{0.f, 0.f, 0.f, 0.f};
#pragma unroll 1
      for (int mm = 0; mm < 2 * NB; ++mm) {
        const int m = 2 * mm + par, n = NA + m;
        const LAS unsigned* pp = &L->PB[m / NB][lane & 1][(m % NB) * 64 + w * 32 + 4 * g4];
        const bf16x8 p0 = *(const LAS bf16x8*)pp, p1 = *(const LAS bf16x8*)(pp + 16);
        wait_ge(full, n + 1);
        bf16x8 vf[8];
        read_tr(L->ring[w][n & 7], ta0, vf);
#pragma unroll
        for (int t = 0; t < 4; ++t) acc[t] = __builtin_amdgcn_mfma_f32_16x16x32_bf16(vf[t], p0, acc[t], 0, 0, 0);
#pragma unroll
        for (int t = 0; t < 4; ++t) acc[t] = __builtin_amdgcn_mfma_f32_16x16x32_bf16(vf[4 + t], p1, acc[t], 0, 0, 0);
        asm volatile("" : "+v"(acc[0]), "+v"(acc[1]), "+v"(acc[2]), "+v"(acc[3]));
      }
      STAMP(5);
      {
        wait_ge(&L->pready, 5);
        const float fs = L->fscale[lane & 1];
        f32x4 o = *(const LAS f32x4*)&L->oa[(c16 & 1) * 64 + ((c16 >> 1) & 3) * 16 + 4 * g4];
        if (cw != 0) o = f32x4{0.f, 0.f, 0.f, 0.f};
        o += sel4(acc[0], acc[1], acc[2], acc[3], c16 & 2, c16 & 4) * fs;
        if (c16 < 8) *(LAS v2u*)&L->ob[cw][(c16 & 1) * 32 + (c16 >> 1) * 8 + 2 * g4] = v2u{pkbf(o[0], o[1]), pkbf(o[2], o[3])};
      }
      WG_BARRIER();
      STAMP(6);
      const int col = 128 * cw + 8 * (4 * g4 + (c16 >> 2)) + 4 * (c16 & 1);
      const bool st_on = (c16 & 3) < 2 && col < DM;
      bf16x8 of[4];
#pragma unroll
      for (int kb = 0; kb < 4; ++kb) of[kb] = *(const LAS bf16x8*)&L->ob[c16 & 3][16 * kb + 4 * g4];
      const f32x4 ybv = *(const LAS f32x4*)&L->yb[st_on ? col : 0];
      const float g = L->gate;
      f32x4 ya[8];
#pragma unroll
      for (int e = 0; e < 8; ++e) ya[e] = f32x4{0.f, 0.f, 0.f, 0.f};
#pragma unroll
      for (int kb = 0; kb < 4; ++kb)
#pragma unroll
        for (int e = 0; e < 8; ++e) ya[e] = __builtin_amdgcn_mfma_f32_16x16x32_bf16(__builtin_bit_cast(bf16x8, wf[8 * kb + e]), of[kb], ya[e], 0, 0, 0);
      float yr[8];
#pragma unroll
      for (int e = 0; e < 8; ++e) { const float lo = (c16 & 4) ? ya[e][1] : ya[e][0], hi = (c16 & 4) ? ya[e][3] : ya[e][2]; yr[e] = (c16 & 8) ? hi : lo; }
      v4u yv;
#pragma unroll
      for (int r = 0; r < 4; ++r) {
        float v = tr_dpp<XOR1>(yr[r], yr[4 + r], c16 & 1);
        v += dpp<XOR2>(v);
        yv[r] = san(fmaf(g, v, ybv[r]));
      }
      __builtin_amdgcn_raw_buffer_store_b128(yv, rs, st_on ? WS_Y + gid * (DM * 4) + col * 4 : WS_END, 0, 16);
      STAMP(8);
    } else {
      float mb = 0.f;
      if (wave == 2) {
        {
          const unsigned thi = (unsigned)(tag >> 32), tlo = (unsigned)tag;
          v4u g[4];
          if (QWAIT) __builtin_amdgcn_s_sleep(QWAIT);
#pragma unroll 1
          for (unsigned spins = 0; spins < SPIN_CAP; ++spins) {
            bool ok = true;
#pragma unroll
            for (int k = 0; k < 4; ++k) g[k] = __builtin_amdgcn_raw_buffer_load_b128(rs, WS_QG + (gid & ~7) * 512 + k * 1024 + lane * 16, 0, 16);
#pragma unroll
            for (int k = 0; k < 4; ++k) ok &= g[k][1] == thi && g[k][3] == thi && (g[k][0] & 0xffff0000u) == tlo && (g[k][2] & 0xffff0000u) == tlo;
            if (__all(ok)) break;
            __builtin_amdgcn_s_sleep(QSLEEP);
          }
#pragma unroll
          for (int k = 0; k < 4; ++k) L->q[k * 64 + lane] = perm_lo(g[k][0], g[k][2]);
          LGKM0();
          *(volatile LAS unsigned*)&L->qready = 1u;
        }
        STAMP(3);
        {
          const v4u sv = {SENT, SENT, SENT, SENT};
#pragma unroll
          for (int k = 0; k < 4; ++k) __builtin_amdgcn_raw_buffer_store_b128(sv, rs, WS_PO + gid * 4096 + k * 1024 + lane * 16, 0, 16);
          __builtin_amdgcn_raw_buffer_store_b128(sv, rs, WS_S + gid * 1024 + lane * 16, 0, 16);
          __builtin_amdgcn_raw_buffer_store_b128(sv, rs, WS_Y + gid * (DM * 4) + lane * 16, 0, 16);
          __builtin_amdgcn_raw_buffer_store_b128(sv, rs, lane < 48 ? WS_Y + gid * (DM * 4) + 1024 + lane * 16 : WS_END, 0, 16);
          DRAIN();
          if (lane == 0) __hip_atomic_store((GAS u64*)(ws + WS_RDY) + gid, ep + 1, RLX_AGENT);
        }
        constexpr float C2 = 0.0625f * 1.4426950408889634f;
        float mrun = -INFINITY, lrun = 0.f;
#pragma unroll 1
        for (int gen = 0; gen < 4; ++gen) {
#pragma unroll
          for (int c = 0; c < 4; ++c) wait_ge(&L->kdone[c], 2 * gen + 2);
          v4u g = *(const LAS v4u*)&L->P[gen][0][lane * 4];
          if (gen == 0) {
            v4u sv;
#pragma unroll
            for (int k = 0; k < 4; ++k) sv[k] = g[k] == SENT ? 0x7FC07FC0u : g[k];
            __builtin_amdgcn_raw_buffer_store_b128(sv, rs, WS_S + gid * 1024 + lane * 16, 0, 16);
          }
          float cm = -INFINITY;
#pragma unroll
          for (int e = 0; e < 4; ++e) cm = fmaxf(cm, fmaxf(bflo(g[e]), bfhi(g[e])));
          const float mn = fmaxf(mrun, max32(cm) * C2);
          float ls = 0.f;
#pragma unroll
          for (int e = 0; e < 4; ++e) {
            const float pl = __builtin_amdgcn_exp2f(fmaf(bflo(g[e]), C2, -mn)), ph = __builtin_amdgcn_exp2f(fmaf(bfhi(g[e]), C2, -mn));
            ls += pl + ph;
            g[e] = pkbf(pl, ph);
          }
          *(LAS v4u*)&L->P[gen][0][lane * 4] = g;
          lrun = fmaf(lrun, __builtin_amdgcn_exp2f(mrun - mn), ls);
          mrun = mn;
          if ((lane & 31) == 0) L->mref[gen][lane >> 5] = mn;
          if (gen == 3) {
            lrun += dpp<XOR1>(lrun); lrun += dpp<XOR2>(lrun); lrun += dpp<ROR4>(lrun); lrun += dpp<XOR8>(lrun); lrun = swp16_add(lrun, lrun);
            if ((lane & 31) == 0) { L->mlown[lane >> 4] = mrun; L->mlown[(lane >> 4) + 1] = lrun; }
#pragma unroll 1
            for (unsigned spins = 0; spins < SPIN_CAP; ++spins) {
              const u64 gv = __hip_atomic_load((GAS u64*)(ws + WS_RDY) + b * 64 + lane, RLX_AGENT);
              if (__all(gv == ep + 1)) break;
              __builtin_amdgcn_s_sleep(32);
            }
            if (j == 0 && lane == 0) __hip_atomic_store((GAS u64*)(ws + WS_EPOCH + b * 256), ep + 1, RLX_AGENT);
            v4u h[4];
#pragma unroll 1
            for (unsigned spins = 0; spins < SPIN_CAP; ++spins) {
              unsigned mx = 0u;
#pragma unroll
              for (int k = 0; k < 4; ++k) h[k] = __builtin_amdgcn_raw_buffer_load_b128(rs, WS_S + ((gid & ~3) + k) * 1024 + lane * 16, 0, 16);
#pragma unroll
              for (int k = 0; k < 4; ++k) mx = max(max(mx, max(h[k][0], h[k][1])), max(h[k][2], h[k][3]));
              if (__all(mx != SENT)) break;
              __builtin_amdgcn_s_sleep(8);
            }
            float bm = -INFINITY;
#pragma unroll
            for (int k = 0; k < 4; ++k)
#pragma unroll
              for (int e = 0; e < 4; ++e) bm = fmaxf(bm, fmaxf(bflo(h[k][e]), bfhi(h[k][e])));
            mrun = max32(bm) * C2;
#pragma unroll
            for (int k = 0; k < 4; ++k) {
#pragma unroll
              for (int e = 0; e < 4; ++e)
                h[k][e] = pkbf(__builtin_amdgcn_exp2f(fmaf(bflo(h[k][e]), C2, -mrun)), __builtin_amdgcn_exp2f(fmaf(bfhi(h[k][e]), C2, -mrun)));
              *(LAS v4u*)&L->PB[k][0][lane * 4] = h[k];
            }
          }
          LGKM0();
          *(volatile LAS unsigned*)&L->pready = gen + 1;
        }
        mb = mrun;
      } else {
        wait_ge(&L->qready, 1);
        const int cl = lane < 56 ? lane : 55;
        {
          const unsigned t0 = *(const GAS unsigned*)vbase, t1 = *(const GAS u16*)out;
          asm volatile("" ::"v"(t0), "v"(t1));
        }
        float z = 0.f;
#pragma unroll
        for (int k = 0; k < 4; ++k) {
          const int idx = lane + 64 * k;
          const bool in = idx < DM / 2;
          const unsigned xv = x32[in ? idx : 0], gv = ((const GAS unsigned*)gw)[in ? idx : 0];
          const float t = rbf(bflo(xv) * bflo(gv)) + rbf(bfhi(xv) * bfhi(gv));
          z += in ? t : 0.f;
        }
        z = swp32_add(z, z); z = swp16_add(z, z); z += dpp<XOR8>(z); z += dpp<XOR7>(z); z += dpp<XOR2>(z); z += dpp<XOR1>(z);
        const float g = rbf(1.f / (1.f + __expf(-rbf(z))));
        if (lane == 0) L->gate = g;
        float yb[8];
#pragma unroll
        for (int c = 0; c < 8; ++c) yb[c] = 0.f;
        if (s != 3) {
          const int mx = blk & 7, y0 = (3 - mx) & 3;
          const int u0 = (GMAP == 1 ? (mx < 4 ? 6 * mx + kvh - (kvh > y0) - (kvh > y0 + 4) : 8 * mx - 8 + kvh) : (s == 7 ? 48 + kvh : kvh * 6 + 2 * qd + kh)) * 32, rr = lane >> 2;
          float ha[8];
#pragma unroll
          for (int c = 0; c < 8; ++c) ha[c] = 0.f;
          const GAS u16* w1p = (const GAS u16*)w1 + (size_t)(2 * rr) * DFF + u0 + (lane & 3) * 8;
#pragma unroll 1
          for (int h = 0; h < 2; ++h) {
            v4u ma[14]; unsigned xq[7];
#pragma unroll
            for (int k = 0; k < 7; ++k) {
              ma[2 * k] = ldw(w1p + (size_t)((7 * h + k) * 32) * DFF);
              ma[2 * k + 1] = ldw(w1p + (size_t)((7 * h + k) * 32 + 1) * DFF);
              xq[k] = x32[(7 * h + k) * 16 + rr];
            }
#pragma unroll
            for (int k = 0; k < 7; ++k) pair_dot(ma[2 * k], ma[2 * k + 1], xq[k], ha);
          }
          float hv = tr8(ha, lane);
          hv += dpp<SHR4>(hv);
          hv = fmaxf(rbf(hv), 0.f);
#pragma unroll 1
          for (int c4 = 0; c4 < 4; ++c4) {
            v4u w2r[8];
#pragma unroll
            for (int u = 0; u < 8; ++u) w2r[u] = ldw((const GAS u16*)w2 + (size_t)(u0 + c4 * 8 + u) * DM + cl * 8);
#pragma unroll
            for (int u = 0; u < 8; ++u) {
              const float hu = __builtin_bit_cast(float, __builtin_amdgcn_readlane(__builtin_bit_cast(int, hv), u * 8 + 4 + c4));
#pragma unroll
              for (int c = 0; c < 4; ++c) { yb[2 * c] = fmaf(hu, bflo(w2r[u][c]), yb[2 * c]); yb[2 * c + 1] = fmaf(hu, bfhi(w2r[u][c]), yb[2 * c + 1]); }
            }
          }
        }
        const float omg = rbf(1.f - g);
        if (lane < 56) {
          *(LAS f32x4*)(L->yb + lane * 8) = f32x4{omg * yb[0], omg * yb[1], omg * yb[2], omg * yb[3]};
          *(LAS f32x4*)(L->yb + lane * 8 + 4) = f32x4{omg * yb[4], omg * yb[5], omg * yb[6], omg * yb[7]};
        }
      }
      STAMP(4);
      {
        typedef float f32x2 __attribute__((ext_vector_type(2)));
        wait_ge(&L->acnt, 4);
        const int hh = wave - 2;
        const unsigned Mu = san(L->mlown[2 * hh]), Lu = san(L->mlown[2 * hh + 1]);
#pragma unroll
        for (int u = 0; u < 2; ++u) {
          f32x2 os = *(const LAS f32x2*)(L->mrg[0] + hh * 256 + u * 128 + lane * 2);
#pragma unroll
          for (int ww = 1; ww < 4; ++ww) os += *(const LAS f32x2*)(L->mrg[ww] + hh * 256 + u * 128 + lane * 2);
          __builtin_amdgcn_raw_buffer_store_b128(v4u{san(os[0]), san(os[1]), Mu, Lu}, rs, WS_PO + gid * 4096 + hh * 2048 + u * 1024 + lane * 16, 0, 16);
        }
      }
      STAMP(5);
      if (wave == 2) {
        const int po = WS_PO + ((gid & ~7) + (lane >> 3)) * 4096 + (lane & 7) * 16;
        v4u h[5];
#pragma unroll 1
        for (unsigned spins = 0; spins < SPIN_CAP; ++spins) {
          unsigned mx = 0u;
#pragma unroll
          for (int k = 0; k < 4; ++k) h[k] = __builtin_amdgcn_raw_buffer_load_b128(rs, po + s * 512 + k * 128, 0, 16);
          h[4] = __builtin_amdgcn_raw_buffer_load_b128(rs, po + (s ^ 4) * 512, 0, 16);
#pragma unroll
          for (int k = 0; k < 5; ++k) mx = max(max(mx, max(h[k][0], h[k][1])), max(h[k][2], h[k][3]));
          if (__all(mx != SENT)) break;
          __builtin_amdgcn_s_sleep(HSLEEP);
        }
        STAMP(6);
        const float mp = __uint_as_float(h[0][2]), mq = __uint_as_float(h[4][2]);
        float M = fmaxf(mp, dpp<XOR8>(mp)), Mq = fmaxf(mq, dpp<XOR8>(mq));
        M = swp32_max(swp16_max(M)); Mq = swp32_max(swp16_max(Mq));
        const float wt = __builtin_amdgcn_exp2f(mp - M);
        float Ls = wt * __uint_as_float(h[0][3]), Lq = __builtin_amdgcn_exp2f(mq - Mq) * __uint_as_float(h[4][3]);
        Ls += dpp<XOR8>(Ls); Lq += dpp<XOR8>(Lq);
        Ls = swp16_add(Ls, Ls); Lq = swp16_add(Lq, Lq); Ls = swp32_add(Ls, Ls); Lq = swp32_add(Lq, Lq);
        float ov[8];
#pragma unroll
        for (int k = 0; k < 4; ++k) { ov[2 * k] = wt * __uint_as_float(h[k][0]); ov[2 * k + 1] = wt * __uint_as_float(h[k][1]); }
        const float oo = tr8(ov, lane) / Ls;
        L->oa[kh * 64 + 16 * (lane >> 4) + 2 * (lane & 7) + ((lane >> 3) & 1)] = oo;
        L->oa[(kh ^ 1) * 64 + lane] = 0.f;
        const bool mine = (lane >> 5) == kh;
        if ((lane & 31) == 0) L->fscale[lane >> 5] = __builtin_amdgcn_exp2f(mb - (mine ? M : Mq)) / (mine ? Ls : Lq);
        LGKM0();
        *(volatile LAS unsigned*)&L->pready = 5u;
        STAMP(7);
      }
      WG_BARRIER();
    }
  }
  if (j < 14) {
    const v4u y = poll16(srd, WS_Y + (b * 64 + wave * 8 + (lane >> 3)) * (DM * 4) + j * 128 + (lane & 7) * 16);
    STAMP(9);
    const float r0 = swp32_add(__uint_as_float(y[0]), __uint_as_float(y[2])), r1 = swp32_add(__uint_as_float(y[1]), __uint_as_float(y[3]));
    float r = swp16_add(r0, r1);
    r += dpp<XOR8>(r);
    if (!(lane & 8)) L->fin[wave][(lane & 7) * 4 + (lane >> 4)] = r;
    WG_BARRIER();
    if (tid < 32) {
      float t = L->fin[0][tid];
#pragma unroll
      for (int k = 1; k < 8; ++k) t += L->fin[k][tid];
      out[b * DM + j * 32 + tid] = (u16)pkbf(t, 0.f);
    }
  }
  STAMP_OUT();
  DRAIN();
}

extern "C" void kernel_launch(void* const* d_in, const int* in_sizes, int n_in, void* d_out, int out_size, void* d_ws, size_t ws_size, hipStream_t stream) {
  fused_kernel<<<256, 512, 0, stream>>>((const u16*)d_in[0], (const u16*)d_in[1], (const u16*)d_in[2], (const u16*)d_in[3], (const u16*)d_in[4],
                                        (const u16*)d_in[6], (const u16*)d_in[7], (const u16*)d_in[8], (unsigned char*)d_ws, (u16*)d_out);
}
```

```cpp
#include <hip/hip_runtime.h>
#include <hip/hip_bf16.h>
#include <cstdint>

#define W_NT 0
#define DEPTH 13
#define PREH 2
#define DEPTH2 20
#define HSW 54
#define NB 2
#define HSLEEP 2
#define MLPMAP 1
#define PACER 1
#define DLO 10
#define QSLEEP 1
#define QWAIT 0
#define LFIRST 1
#define KFIRST 0
#define T0N 0
#define T0ADJ 130
#define GMAP 0
#define STAMPS 0

typedef unsigned v4u __attribute__((ext_vector_type(4)));
typedef unsigned v3u __attribute__((ext_vector_type(3)));
typedef unsigned v2u __attribute__((ext_vector_type(2)));
typedef float f32x4 __attribute__((ext_vector_type(4)));
typedef __bf16 bf16x2 __attribute__((ext_vector_type(2)));
typedef __bf16 bf16x8 __attribute__((ext_vector_type(8)));
typedef short s16x4 __attribute__((ext_vector_type(4)));
typedef unsigned short u16;
typedef unsigned long long u64;
#define GAS __attribute__((address_space(1)))
#define LAS __attribute__((address_space(3)))
#define RLX_AGENT __ATOMIC_RELAXED, __HIP_MEMORY_SCOPE_AGENT

constexpr int DM = 448, SK = 8192, DH = 256, DFF = 1792, QLD = 4096;
constexpr unsigned SENT = 0xFFFFFFFFu;
constexpr unsigned SPIN_CAP = 1u << 18;
constexpr int WS_EPOCH = 0;
constexpr int WS_RDY = 4096;
constexpr int WS_QG = 8192;
constexpr int WS_S = WS_QG + 256 * 64 * 8;
constexpr int WS_PO = WS_S + 256 * 1024;
constexpr int WS_Y = WS_PO + 256 * 4096;
constexpr int WS_END = WS_Y + 256 * DM * 4;
constexpr size_t WS_STAMP = 16u << 20;

__device__ __forceinline__ float bflo(unsigned u) { return __builtin_bit_cast(float, u << 16); }
__device__ __forceinline__ float bfhi(unsigned u) { return __builtin_bit_cast(float, u & 0xffff0000u); }
__device__ __forceinline__ unsigned pkbf(float lo, float hi) {
  bf16x2 r; r[0] = (__bf16)lo; r[1] = (__bf16)hi; return __builtin_bit_cast(unsigned, r);
}
__device__ __forceinline__ float rbf(float f) { return bflo(pkbf(f, 0.f)); }
__device__ __forceinline__ float dot2(unsigned a, unsigned b, float acc) {
  return __builtin_amdgcn_fdot2_f32_bf16(__builtin_bit_cast(bf16x2, a), __builtin_bit_cast(bf16x2, b), acc, false);
}
__device__ __forceinline__ unsigned perm_lo(unsigned a, unsigned b) { return __builtin_amdgcn_perm(b, a, 0x05040100u); }
__device__ __forceinline__ unsigned perm_hi(unsigned a, unsigned b) { return __builtin_amdgcn_perm(b, a, 0x07060302u); }
template <int CTRL> __device__ __forceinline__ float dpp(float x) {
  return __builtin_bit_cast(float, __builtin_amdgcn_mov_dpp(__builtin_bit_cast(int, x), CTRL, 0xf, 0xf, true));
}
constexpr int XOR1 = 0xB1, XOR2 = 0x4E, XOR7 = 0x141, XOR8 = 0x128, ROR4 = 0x124, SHR4 = 0x114;
__device__ __forceinline__ float swp16_add(float a, float b) {
  auto s = __builtin_amdgcn_permlane16_swap(__float_as_uint(a), __float_as_uint(b), false, false);
  return __uint_as_float(s[0]) + __uint_as_float(s[1]);
}
__device__ __forceinline__ float swp32_add(float a, float b) {
  auto s = __builtin_amdgcn_permlane32_swap(__float_as_uint(a), __float_as_uint(b), false, false);
  return __uint_as_float(s[0]) + __uint_as_float(s[1]);
}
__device__ __forceinline__ float swp16_max(float a) {
  auto s = __builtin_amdgcn_permlane16_swap(__float_as_uint(a), __float_as_uint(a), false, false);
  return fmaxf(__uint_as_float(s[0]), __uint_as_float(s[1]));
}
__device__ __forceinline__ float swp32_max(float a) {
  auto s = __builtin_amdgcn_permlane32_swap(__float_as_uint(a), __float_as_uint(a), false, false);
  return fmaxf(__uint_as_float(s[0]), __uint_as_float(s[1]));
}
template <int CTRL> __device__ __forceinline__ float tr_dpp(float lo, float hi, bool sel) {
  const float t1 = lo + dpp<CTRL>(lo), t2 = hi + dpp<CTRL>(hi);
  return sel ? t2 : t1;
}
__device__ __forceinline__ float tr8(float (&a)[8], int lane) {
#pragma unroll
  for (int i = 0; i < 4; ++i) a[i] = swp32_add(a[i], a[i + 4]);
#pragma unroll
  for (int i = 0; i < 2; ++i) a[i] = swp16_add(a[i], a[i + 2]);
  return tr_dpp<XOR8>(a[0], a[1], lane & 8);
}
__device__ __forceinline__ float max32(float v) {
  v = fmaxf(v, dpp<XOR1>(v)); v = fmaxf(v, dpp<XOR2>(v)); v = fmaxf(v, dpp<ROR4>(v)); v = fmaxf(v, dpp<XOR8>(v));
  return swp16_max(v);
}
__device__ __forceinline__ void pair_dot(const v4u& ra, const v4u& rb, unsigned xp, float (&a)[8]) {
#pragma unroll
  for (int c = 0; c < 4; ++c) {
    a[2 * c] = dot2(perm_lo(ra[c], rb[c]), xp, a[2 * c]);
    a[2 * c + 1] = dot2(perm_hi(ra[c], rb[c]), xp, a[2 * c + 1]);
  }
}
__device__ __forceinline__ unsigned san(float f) { const unsigned u = __float_as_uint(f); return u == SENT ? 0x7FC00000u : u; }
__device__ __forceinline__ v4u ldw(const GAS void* p) {
  return W_NT ? __builtin_nontemporal_load((const GAS v4u*)p) : *(const GAS v4u*)p;
}
#define WG_BARRIER() do { asm volatile("s_waitcnt lgkmcnt(0)" ::: "memory"); __builtin_amdgcn_s_barrier(); asm volatile("" ::: "memory"); __builtin_amdgcn_sched_barrier(0); } while (0)
#define DRAIN() asm volatile("s_waitcnt vmcnt(0)" ::: "memory")
#define STAMP(i) do {} while (0)
#define STAMP_OUT() do {} while (0)

struct Lds {
  unsigned char ring[2][8][8192];
  unsigned P[4][2][128];
  unsigned PB[4][2][128];
  float mrg[4][512];
  float oa[128];
  unsigned q[256];
  float yb[DM];
  float qpart[6][64];
  float fin[8][32];
  unsigned ob[4][68];
  float mref[4][2];
  float fscale[2];
  float gate, pad0;
  float mlown[4];
  unsigned freed[2][8];
  unsigned full[2];
  unsigned cnt, qready;
  unsigned kdone[4];
  unsigned pready, acnt, anchor, pad1;
};

#define PIN16(a) asm volatile("" : "+v"(a[0]), "+v"(a[1]), "+v"(a[2]), "+v"(a[3]), "+v"(a[4]), "+v"(a[5]), "+v"(a[6]), "+v"(a[7]), \
                                   "+v"(a[8]), "+v"(a[9]), "+v"(a[10]), "+v"(a[11]), "+v"(a[12]), "+v"(a[13]), "+v"(a[14]), "+v"(a[15]))
#define PIN8(a) asm volatile("" : "+v"(a[0]), "+v"(a[1]), "+v"(a[2]), "+v"(a[3]), "+v"(a[4]), "+v"(a[5]), "+v"(a[6]), "+v"(a[7]))
#define LGKM0() asm volatile("s_waitcnt lgkmcnt(0)" ::: "memory")

__device__ __forceinline__ void wait_ge(volatile LAS unsigned* w, unsigned need) {
#pragma unroll 1
  for (unsigned spins = 0; spins < (1u << 22); ++spins) {
    if ((unsigned)__builtin_amdgcn_readfirstlane(*w) >= need) break;
    __builtin_amdgcn_s_sleep(1);
  }
  asm volatile("" ::: "memory");
}

#define PTHR 64
#define PSLOW 10
#define PSLEEP 2
__device__ __forceinline__ v4u poll16(const v4u& srd, int off) {
  unsigned c0, c1, c2, c3, mx, cnt;
  asm volatile(
      "v_mov_b32 v252, -1\n\tv_mov_b32 v253, -1\n\tv_mov_b32 v254, -1\n\tv_mov_b32 v255, -1\n\t"
      "s_mov_b32 %5, 0x200000\n\t"
      "s_nop 1\n"
      "1:\n\t"
      "buffer_load_dwordx4 v[252:255], %6, %7, 0 offen sc1\n\t"
      "s_sleep %8\n\t"
      "s_waitcnt vmcnt(12)\n\t"
      "v_mov_b32 %0, v252\n\tv_mov_b32 %1, v253\n\tv_mov_b32 %2, v254\n\tv_mov_b32 %3, v255\n\t"
      "v_max3_u32 %4, %0, %1, %2\n\t"
      "v_max_u32 %4, %4, %3\n\t"
      "v_cmp_eq_u32 vcc, -1, %4\n\t"
      "s_and_b64 exec, exec, vcc\n\t"
      "s_cbranch_scc0 2f\n\t"
      "s_bcnt1_i32_b64 vcc_lo, exec\n\t"
      "s_cmp_le_u32 vcc_lo, %9\n\t"
      "s_cbranch_scc1 3f\n\t"
      "s_sleep %10\n"
      "3:\n\t"
      "s_sub_u32 %5, %5, 1\n\t"
      "s_cbranch_scc0 1b\n"
      "2:\n\t"
      "s_mov_b64 exec, -1\n\t"
      "s_nop 4"
      : "=&v"(c0), "=&v"(c1), "=&v"(c2), "=&v"(c3), "=&v"(mx), "=&s"(cnt)
      : "v"(off), "s"(srd), "i"(PSLEEP), "i"(PTHR), "i"(PSLOW)
      : "vcc", "scc", "v252", "v253", "v254", "v255", "memory");
  return v4u{c0, c1, c2, c3};
}

template <int D, int AUX>
__device__ __forceinline__ void issue8(const char* src, int half, unsigned char* dst, const unsigned (&voff)[4], int n, int& pub,
                                       volatile LAS unsigned* full, volatile LAS unsigned* fr) {
  if (n >= 8) {
#pragma unroll 1
    for (unsigned spins = 0; spins < (1u << 22); ++spins) {
      if ((int)__builtin_amdgcn_readfirstlane(*fr) >= n - 7) break;
      DRAIN();
      pub = n; *full = pub;
      __builtin_amdgcn_s_sleep(1);
    }
    asm volatile("" ::: "memory");
  }
#pragma unroll
  for (int g = 0; g < 8; ++g) {
    asm volatile("s_waitcnt vmcnt(%0)" ::"i"(D - 1) : "memory");
    if (g == (D - 1) % 8) { pub = max(pub, n - (D - 1) / 8); *full = pub; }
    const char* sp = src + (g >> 2) * half;
    if ((g & 3) == 0) __builtin_amdgcn_global_load_lds((const unsigned*)(sp + voff[0]), (unsigned*)(dst + (g & 4) * 1024), 16, 0, AUX);
    if ((g & 3) == 1) __builtin_amdgcn_global_load_lds((const unsigned*)(sp + voff[1]), (unsigned*)(dst + (g & 4) * 1024), 16, 1024, AUX);
    if ((g & 3) == 2) __builtin_amdgcn_global_load_lds((const unsigned*)(sp + voff[2]), (unsigned*)(dst + (g & 4) * 1024), 16, 2048, AUX);
    if ((g & 3) == 3) __builtin_amdgcn_global_load_lds((const unsigned*)(sp + voff[3]), (unsigned*)(dst + (g & 4) * 1024), 16, 3072, AUX);
  }
}
#define TAU 590
#define TOFF 0
__device__ __forceinline__ bool ahead(unsigned t0, int n) {
  if (!PACER || n < 8) return false;
  return (int)((unsigned)__builtin_amdgcn_s_memrealtime() - t0) < TOFF + n * TAU / 10;
}
__device__ __forceinline__ void read_tr(const LAS unsigned char* cell, unsigned a0, bf16x8* f) {
#pragma unroll
  for (int kb = 0; kb < 2; ++kb)
#pragma unroll
    for (int t = 0; t < 4; ++t) {
      const s16x4 lo = __builtin_amdgcn_ds_read_tr16_b64_v4i16((LAS s16x4*)(cell + ((a0 ^ (t << 5)) + kb * 4096)));
      const s16x4 hi = __builtin_amdgcn_ds_read_tr16_b64_v4i16((LAS s16x4*)(cell + ((a0 ^ (t << 5)) + kb * 4096 + 512)));
      f[kb * 4 + t] = __builtin_bit_cast(bf16x8, __builtin_shufflevector(lo, hi, 0, 1, 2, 3, 4, 5, 6, 7));
    }
}
__device__ __forceinline__ f32x4 sel4(f32x4 a0, f32x4 a1, f32x4 a2, f32x4 a3, bool b0, bool b1) {
  f32x4 r;
#pragma unroll
  for (int e = 0; e < 4; ++e) { const float lo = b0 ? a1[e] : a0[e], hi = b0 ? a3[e] : a2[e]; r[e] = b1 ? hi : lo; }
  return r;
}

__global__ void __launch_bounds__(512, 2) fused_kernel(const u16* __restrict__ x, const u16* __restrict__ Kc, const u16* __restrict__ Vc,
    const u16* __restrict__ wq, const u16* __restrict__ wo, const u16* __restrict__ w1, const u16* __restrict__ w2,
    const u16* __restrict__ gw, unsigned char* ws, u16* out) {
  __shared__ __attribute__((aligned(16))) Lds sm;
  LAS Lds* L = (LAS Lds*)&sm;
  asm volatile("" ::"s"(x), "s"(Kc), "s"(Vc), "s"(wq), "s"(wo), "s"(w1), "s"(w2), "s"(gw), "s"(ws), "s"(out));
  const int tid = threadIdx.x, lane = tid & 63, wave = __builtin_amdgcn_readfirstlane(tid >> 6);
  const int blk = blockIdx.x, b = blk >> 6;
  const int kvh = ((blk >> 3) & 6) | ((blk >> 2) & 1), kh = (blk >> 3) & 1, qd = (blk + (blk >> 4)) & 3;
  const int s = kh * 4 + qd;
  const int j = kvh * 8 + s;
  const int gid = b * 64 + j;
  const __amdgpu_buffer_rsrc_t rs = __builtin_amdgcn_make_buffer_rsrc((void*)ws, 0, WS_END, 0x00020000);
  const v4u srd = {(unsigned)(size_t)ws, (unsigned)((size_t)ws >> 32) & 0xffffu, (unsigned)WS_END, 0x00020000u};
  constexpr int NA = 64 - 4 * NB;
  const char* vbase = (const char*)Vc + ((size_t)((b * 8 + kvh) * SK + kh * 4096)) * (DH * 2) + qd * 128;
  if (wave < 2) STAMP(8);
  if (__builtin_expect(wave < 2, LFIRST)) {
    __builtin_amdgcn_s_setprio(3);
    if (tid < 28) L->freed[0][tid] = 0u;
    volatile LAS unsigned* full = &L->full[wave];
    const char* kb = (const char*)Kc + ((size_t)((b * 8 + kvh) * SK + s * 1024 + wave * 16)) * (DH * 2);
    unsigned vk[4], vv[4];
#pragma unroll
    for (int g = 0; g < 4; ++g) {
      vk[g] = (lane >> 5) * 512 + (((lane & 31) ^ (((2 * g + (lane >> 5)) & 7) << 1)) << 4);
      const unsigned ch = ((lane & 7) ^ (((lane >> 3) & 2) | ((g & 1) << 2))) << 4;
      vv[g] = (8 * g + (lane >> 3)) * 512 + ch - g * 1024;
    }
    int pub = 0;
    unsigned t0 = (unsigned)__builtin_amdgcn_s_memrealtime();
    const long vdelta = (const char*)Vc - (const char*)Kc - (32 - 4 * NB) * 16384;
#pragma unroll 1
    for (int n = 0; n < NA; ++n) {
      if (STAMPS && n == 1) STAMP(3);
      if (STAMPS && n == 2) STAMP(4);
      if (STAMPS && n == 8) STAMP(5);
      if (STAMPS && n == 16) STAMP(6);
      if (STAMPS && n == 32) STAMP(7);
      if (T0N && n == T0N) t0 = (unsigned)__builtin_amdgcn_s_memrealtime() - T0ADJ;
      if (n == PREH) WG_BARRIER();
      const char* src = kb + (size_t)n * 16384 + (n < 32 ? 0 : vdelta);
      if (ahead(t0, n)) issue8<DLO, 2>(src, 4096, sm.ring[wave][n & 7], vk, n, pub, full, &L->freed[wave][n & 7]);
      else if (n < HSW) issue8<DEPTH, 2>(src, 4096, sm.ring[wave][n & 7], vk, n, pub, full, &L->freed[wave][n & 7]);
      else issue8<DEPTH2, 2>(src, 4096, sm.ring[wave][n & 7], vk, n, pub, full, &L->freed[wave][n & 7]);
    }
    STAMP(1);
#pragma unroll 1
    for (int n = NA; n < 64; ++n) {
      const int m = n - NA;
      const char* src = vbase + (size_t)((m / NB) * 1024 + (m % NB) * 128 + wave * 64) * 512;
      if (ahead(t0, n)) issue8<DLO, 2>(src, 16384, sm.ring[wave][n & 7], vv, n, pub, full, &L->freed[wave][n & 7]);
      else issue8<DEPTH2, 2>(src, 16384, sm.ring[wave][n & 7], vv, n, pub, full, &L->freed[wave][n & 7]);
    }
#pragma unroll
    for (int k = (DEPTH2 - 1) / 8; k >= 0; --k) {
      asm volatile("s_waitcnt vmcnt(%0)" ::"i"(8 * k) : "memory");
      *full = max(pub, 64 - k);
    }
    STAMP(2);
    WG_BARRIER();
  } else {
    const GAS unsigned* x32 = (const GAS unsigned*)(x + b * DM);
    const int wv = wave - 2;
    const u64 ep = *(const u64*)(ws + WS_EPOCH + b * 256);
    {
      const int r = lane >> 3;
      const GAS char* wqp = (const GAS char*)wq + (size_t)(2 * r) * (QLD * 2) + (j * 64 + (lane & 7) * 8) * 2;
      v4u wr[10]; unsigned xp[5];
      if (KFIRST) { __builtin_amdgcn_s_barrier(); asm volatile("" ::: "memory"); __builtin_amdgcn_sched_barrier(0); }
#pragma unroll
      for (int k = 0; k < 5; ++k) {
        const int pg = min(wv + 6 * k, 27);
        xp[k] = x32[8 * pg + r];
        wr[2 * k] = ldw(wqp + (size_t)(16 * pg) * (QLD * 2));
        wr[2 * k + 1] = ldw(wqp + (size_t)(16 * pg + 1) * (QLD * 2));
      }
      __builtin_amdgcn_sched_barrier(0);
      if (!KFIRST) __builtin_amdgcn_s_barrier();
      asm volatile("" ::: "memory");
      __builtin_amdgcn_sched_barrier(0);
      if (wv >= 4) xp[4] = 0u;
      STAMP(1);
      float qa[8];
#pragma unroll
      for (int c = 0; c < 8; ++c) qa[c] = 0.f;
#pragma unroll
      for (int k = 0; k < 5; ++k) pair_dot(wr[2 * k], wr[2 * k + 1], xp[k], qa);
      L->qpart[wv][(lane & 7) * 8 + r] = tr8(qa, lane);
    }
    const u64 tag = (ep + 1) << 16;
    LGKM0();
    unsigned arr = 0;
    if (lane == 0) arr = __hip_atomic_fetch_add(&L->cnt, 1u, __ATOMIC_RELAXED, __HIP_MEMORY_SCOPE_WORKGROUP);
    if (__builtin_amdgcn_readfirstlane(arr) == 5) {
      asm volatile("" ::: "memory");
      float qv = L->qpart[0][lane];
#pragma unroll
      for (int k = 1; k < 6; ++k) qv += L->qpart[k][lane];
      __hip_atomic_store((GAS u64*)(ws + WS_QG) + gid * 64 + lane, tag | (pkbf(qv, 0.f) & 0xffffu), RLX_AGENT);
    }
    STAMP(2);
    if (wave >= 4) {
      const int cw = wave - 4, w = cw & 1, par = cw >> 1;
      volatile LAS unsigned* full = &L->full[w];
      volatile LAS unsigned* fr = L->freed[w];
      const int g4 = lane >> 4, c16 = lane & 15;
      const unsigned ka0 = c16 * 512 + ((g4 ^ ((c16 & 7) << 1)) << 4);
      const unsigned ta0 = (8 * g4 + (c16 >> 2)) * 128 + ((((c16 >> 3) & 1) | ((g4 & 1) << 1)) << 5) + (lane & 3) * 8;
      {
        bf16x8 qf[8];
#pragma unroll 1
        for (int gen = 0; gen < 4; ++gen) {
          bf16x8 kf[4][8];
#pragma unroll
          for (int h = 0; h < 4; ++h) {
            const int n = 8 * gen + 2 * h + par;
            wait_ge(full, n + 1);
            const LAS unsigned char* cell = L->ring[w][2 * h + par];
#pragma unroll
            for (int t = 0; t < 8; ++t) kf[h][t] = *(const LAS bf16x8*)(cell + ((ka0 ^ ((t & 3) << 6)) + (t >> 2) * 256));
            LGKM0(); fr[2 * h + par] = n + 1;
            PIN8(kf[h]);
          }
          if (gen == 0) {
            wait_ge(&L->qready, 1);
            STAMP(3);
#pragma unroll
            for (int t = 0; t < 8; ++t) qf[t] = *(const LAS bf16x8*)(L->q + (lane & 1) * 128 + (4 * t + g4) * 4);
          }
          f32x4 sc[4];
#pragma unroll
          for (int h = 0; h < 4; ++h) sc[h] = f32x4{0.f, 0.f, 0.f, 0.f};
#pragma unroll
          for (int t = 0; t < 8; ++t)
#pragma unroll
            for (int h = 0; h < 4; ++h) sc[h] = __builtin_amdgcn_mfma_f32_16x16x32_bf16(kf[h][t], qf[t], sc[h], 0, 0, 0);
          const f32x4 sv = sel4(sc[0], sc[1], sc[2], sc[3], c16 & 2, c16 & 4);
          *(LAS v2u*)&L->P[gen][c16 & 1][(2 * (c16 & 6) + 2 * par + w) * 8 + 2 * g4] = v2u{pkbf(sv[0], sv[1]), pkbf(sv[2], sv[3])};
          LGKM0(); *(volatile LAS unsigned*)&L->kdone[cw] = gen + 1;
        }
      }
      STAMP(4);
      v4u wf[32];
      const GAS u16* wop = (const GAS u16*)wo + (size_t)(2 * kvh * 256 + 64 * qd + 8 * g4) * DM + min(128 * cw + 8 * c16, DM - 8);
      f32x4 acc0 = {0.f, 0.f, 0.f, 0.f}, acc1 = {0.f, 0.f, 0.f, 0.f};
      float mprev0 = -INFINITY, mprev1 = -INFINITY;
      const int vrow = 4 * g4 + (c16 >> 2);
      const unsigned va0 = vrow * 512 + ((vrow & 7) << 5) + (lane & 3) * 8;
      auto astep = [&](int aa) {
        const int n = 32 - 4 * NB + 2 * aa + par, gen = aa >> 2;
        if ((aa & 3) == 0 || aa == 2 * NB) {
          wait_ge(&L->pready, gen + 1);
          const float m0 = L->mref[gen][0], m1 = L->mref[gen][1];
          const float a0 = __builtin_amdgcn_exp2f(mprev0 - m0), a1 = __builtin_amdgcn_exp2f(mprev1 - m1);
          mprev0 = m0; mprev1 = m1;
          acc0 *= f32x4{a0, a1, a0, a1}; acc1 *= f32x4{a0, a1, a0, a1};
        }
        const v2u pv = *(const LAS v2u*)&L->P[gen][lane & 1][(4 * (aa & 3) + 2 * par + w) * 8 + 2 * g4];
        s16x4 pa[8];
#pragma unroll
        for (int v = 0; v < 8; ++v) { const bool on = (c16 >> 1) == v; pa[v] = __builtin_bit_cast(s16x4, v2u{on ? pv[0] : 0u, on ? pv[1] : 0u}); }
        PIN8(pa);
        wait_ge(full, n + 1);
        const LAS unsigned char* cell = L->ring[w][n & 7];
#pragma unroll
        for (int v = 0; v < 8; ++v) {
          acc0 = __builtin_amdgcn_mfma_f32_16x16x16bf16_1k(pa[v], __builtin_amdgcn_ds_read_tr16_b64_v4i16((LAS s16x4*)(cell + (va0 ^ (v << 5)))), acc0, 0, 0, 0);
          acc1 = __builtin_amdgcn_mfma_f32_16x16x16bf16_1k(pa[v], __builtin_amdgcn_ds_read_tr16_b64_v4i16((LAS s16x4*)(cell + ((va0 ^ (v << 5)) + 256))), acc1, 0, 0, 0);
        }
        asm volatile("" : "+v"(acc0), "+v"(acc1));
        LGKM0(); fr[n & 7] = n + 1;
      };
#pragma unroll
      for (int hb = 0; hb < 4; ++hb) {
#pragma unroll
        for (int i = 8 * hb; i < 8 * hb + 8; ++i) wf[i] = ldw(wop + (size_t)((i >> 4) * 256 + ((i >> 3) & 1) * 32 + (i & 7)) * DM);
        __builtin_amdgcn_sched_barrier(0);
#pragma unroll 1
        for (int aa = 2 * NB + 2 * hb; aa < 2 * NB + 2 * hb + 2; ++aa) astep(aa);
      }
#pragma unroll
      for (int kb = 0; kb < 4; ++kb) {
        v4u tf[8];
#pragma unroll
        for (int e = 0; e < 8; ++e)
#pragma unroll
          for (int d = 0; d < 4; ++d)
            tf[e][d] = (e & 1) ? perm_hi(wf[8 * kb + 2 * d][e >> 1], wf[8 * kb + 2 * d + 1][e >> 1]) : perm_lo(wf[8 * kb + 2 * d][e >> 1], wf[8 * kb + 2 * d + 1][e >> 1]);
        PIN8(tf);
#pragma unroll
        for (int e = 0; e < 8; ++e) wf[8 * kb + e] = tf[e];
      }
#pragma unroll 1
      for (int aa = 2 * NB + 8; aa < 16; ++aa) astep(aa);
#pragma unroll
      for (int r = 0; r < 4; ++r) {
        L->mrg[cw][(r & 1) * 256 + (2 * g4 + (r >> 1)) * 16 + c16] = acc0[r];
        L->mrg[cw][(r & 1) * 256 + 128 + (2 * g4 + (r >> 1)) * 16 + c16] = acc1[r];
      }
      LGKM0();
      if (lane == 0) __hip_atomic_fetch_add(&L->acnt, 1u, __ATOMIC_RELAXED, __HIP_MEMORY_SCOPE_WORKGROUP);
      STAMP(7);
      f32x4 acc[4];
#pragma unroll
      for (int t = 0; t < 4; ++t) acc[t] = f32x4{0.f, 0.f, 0.f, 0.f};
      float fs = 0.f;
      f32x4 o = {0.f, 0.f, 0.f, 0.f};
      LAS v2u* obw = (LAS v2u*)&L->ob[cw][(c16 & 1) * 32 + ((c16 >> 1) & 3) * 8 + 2 * g4];
#pragma unroll 1
      for (int mm = 0; mm < 2 * NB; ++mm) {
        const int m = 2 * mm + par, n = NA + m;
        if (mm == 2 * NB - 1) {
          wait_ge(&L->pready, 5);
          fs = L->fscale[lane & 1];
          if (cw == 0) o = *(const LAS f32x4*)&L->oa[(c16 & 1) * 64 + ((c16 >> 1) & 3) * 16 + 4 * g4];
        }
        const LAS unsigned* pp = &L->PB[m / NB][lane & 1][(m % NB) * 64 + w * 32 + 4 * g4];
        const bf16x8 p0 = *(const LAS bf16x8*)pp, p1 = *(const LAS bf16x8*)(pp + 16);
        wait_ge(full, n + 1);
        bf16x8 vf[8];
        read_tr(L->ring[w][n & 7], ta0, vf);
#pragma unroll
        for (int t = 0; t < 4; ++t) acc[t] = __builtin_amdgcn_mfma_f32_16x16x32_bf16(vf[t], p0, acc[t], 0, 0, 0);
#pragma unroll
        for (int t = 0; t < 4; ++t) acc[t] = __builtin_amdgcn_mfma_f32_16x16x32_bf16(vf[4 + t], p1, acc[t], 0, 0, 0);
        asm volatile("" : "+v"(acc[0]), "+v"(acc[1]), "+v"(acc[2]), "+v"(acc[3]));
      }
      STAMP(5);
      o += sel4(acc[0], acc[1], acc[2], acc[3], c16 & 2, c16 & 4) * fs;
      *obw = v2u{pkbf(o[0], o[1]), pkbf(o[2], o[3])};
      const int col = 128 * cw + 8 * c16 + 4 * (g4 & 1);
      const bool st_on = g4 < 2 && col < DM;
      const int yoff = st_on ? WS_Y + gid * (DM * 4) + col * 4 : WS_END;
      const LAS float* ybp = &L->yb[st_on ? col : 0];
      const LAS unsigned* obr = &L->ob[c16 & 3][4 * g4];
      WG_BARRIER();
      STAMP(6);
      bf16x8 of[4];
#pragma unroll
      for (int kb = 0; kb < 4; ++kb) of[kb] = *(const LAS bf16x8*)(obr + 16 * kb);
      const f32x4 ybv = *(const LAS f32x4*)ybp;
      const float g = L->gate;
      __builtin_amdgcn_sched_barrier(0);
      f32x4 ya[8];
#pragma unroll
      for (int e = 0; e < 8; ++e) ya[e] = f32x4{0.f, 0.f, 0.f, 0.f};
#pragma unroll
      for (int kb = 0; kb < 4; ++kb) {
#pragma unroll
        for (int e = 0; e < 8; ++e) ya[e] = __builtin_amdgcn_mfma_f32_16x16x32_bf16(of[kb], __builtin_bit_cast(bf16x8, wf[8 * kb + e]), ya[e], 0, 0, 0);
        __builtin_amdgcn_sched_barrier(0);
      }
      v4u yv;
#pragma unroll
      for (int r = 0; r < 4; ++r) {
        const float lo = (ya[r][0] + ya[r][1]) + (ya[r][2] + ya[r][3]), hi = (ya[4 + r][0] + ya[4 + r][1]) + (ya[4 + r][2] + ya[4 + r][3]);
        yv[r] = san(fmaf(g, (g4 & 1) ? hi : lo, ybv[r]));
      }
      __builtin_amdgcn_raw_buffer_store_b128(yv, rs, yoff, 0, 16);
      STAMP(8);
    } else {
      float mb = 0.f;
      if (wave == 2) {
        {
          const unsigned thi = (unsigned)(tag >> 32), tlo = (unsigned)tag;
          v4u g[4];
          if (QWAIT) __builtin_amdgcn_s_sleep(QWAIT);
#pragma unroll 1
          for (unsigned spins = 0; spins < SPIN_CAP; ++spins) {
            bool ok = true;
#pragma unroll
            for (int k = 0; k < 4; ++k) g[k] = __builtin_amdgcn_raw_buffer_load_b128(rs, WS_QG + (gid & ~7) * 512 + k * 1024 + lane * 16, 0, 16);
#pragma unroll
            for (int k = 0; k < 4; ++k) ok &= g[k][1] == thi && g[k][3] == thi && (g[k][0] & 0xffff0000u) == tlo && (g[k][2] & 0xffff0000u) == tlo;
            if (__all(ok)) break;
            __builtin_amdgcn_s_sleep(QSLEEP);
          }
#pragma unroll
          for (int k = 0; k < 4; ++k) L->q[k * 64 + lane] = perm_lo(g[k][0], g[k][2]);
          LGKM0();
          *(volatile LAS unsigned*)&L->qready = 1u;
        }
        STAMP(3);
        {
          const v4u sv = {SENT, SENT, SENT, SENT};
#pragma unroll
          for (int k = 0; k < 4; ++k) __builtin_amdgcn_raw_buffer_store_b128(sv, rs, WS_PO + gid * 4096 + k * 1024 + lane * 16, 0, 16);
          __builtin_amdgcn_raw_buffer_store_b128(sv, rs, WS_S + gid * 1024 + lane * 16, 0, 16);
          __builtin_amdgcn_raw_buffer_store_b128(sv, rs, WS_Y + gid * (DM * 4) + lane * 16, 0, 16);
          __builtin_amdgcn_raw_buffer_store_b128(sv, rs, lane < 48 ? WS_Y + gid * (DM * 4) + 1024 + lane * 16 : WS_END, 0, 16);
          DRAIN();
          if (lane == 0) __hip_atomic_store((GAS u64*)(ws + WS_RDY) + gid, ep + 1, RLX_AGENT);
        }
        constexpr float C2 = 0.0625f * 1.4426950408889634f;
        float mrun = -INFINITY, lrun = 0.f;
#pragma unroll 1
        for (int gen = 0; gen < 4; ++gen) {
#pragma unroll
          for (int c = 0; c < 4; ++c) wait_ge(&L->kdone[c], gen + 1);
          v4u g = *(const LAS v4u*)&L->P[gen][0][lane * 4];
          if (gen == 0) {
            v4u sv;
#pragma unroll
            for (int k = 0; k < 4; ++k) sv[k] = g[k] == SENT ? 0x7FC07FC0u : g[k];
            __builtin_amdgcn_raw_buffer_store_b128(sv, rs, WS_S + gid * 1024 + lane * 16, 0, 16);
          }
          float cm = -INFINITY;
#pragma unroll
          for (int e = 0; e < 4; ++e) cm = fmaxf(cm, fmaxf(bflo(g[e]), bfhi(g[e])));
          const float mn = fmaxf(mrun, max32(cm) * C2);
          float ls = 0.f;
#pragma unroll
          for (int e = 0; e < 4; ++e) {
            const float pl = __builtin_amdgcn_exp2f(fmaf(bflo(g[e]), C2, -mn)), ph = __builtin_amdgcn_exp2f(fmaf(bfhi(g[e]), C2, -mn));
            ls += pl + ph;
            g[e] = pkbf(pl, ph);
          }
          *(LAS v4u*)&L->P[gen][0][lane * 4] = g;
          lrun = fmaf(lrun, __builtin_amdgcn_exp2f(mrun - mn), ls);
          mrun = mn;
          if ((lane & 31) == 0) L->mref[gen][lane >> 5] = mn;
          if (gen == 3) {
            lrun += dpp<XOR1>(lrun); lrun += dpp<XOR2>(lrun); lrun += dpp<ROR4>(lrun); lrun += dpp<XOR8>(lrun); lrun = swp16_add(lrun, lrun);
            if ((lane & 31) == 0) { L->mlown[lane >> 4] = mrun; L->mlown[(lane >> 4) + 1] = lrun; }
#pragma unroll 1
            for (unsigned spins = 0; spins < SPIN_CAP; ++spins) {
              const u64 gv = __hip_atomic_load((GAS u64*)(ws + WS_RDY) + b * 64 + lane, RLX_AGENT);
              if (__all(gv == ep + 1)) break;
              __builtin_amdgcn_s_sleep(32);
            }
            if (j == 0 && lane == 0) __hip_atomic_store((GAS u64*)(ws + WS_EPOCH + b * 256), ep + 1, RLX_AGENT);
            v4u h[4];
#pragma unroll 1
            for (unsigned spins = 0; spins < SPIN_CAP; ++spins) {
              unsigned mx = 0u;
#pragma unroll
              for (int k = 0; k < 4; ++k) h[k] = __builtin_amdgcn_raw_buffer_load_b128(rs, WS_S + ((gid & ~3) + k) * 1024 + lane * 16, 0, 16);
#pragma unroll
              for (int k = 0; k < 4; ++k) mx = max(max(mx, max(h[k][0], h[k][1])), max(h[k][2], h[k][3]));
              if (__all(mx != SENT)) break;
              __builtin_amdgcn_s_sleep(8);
            }
            float bm = -INFINITY;
#pragma unroll
            for (int k = 0; k < 4; ++k)
#pragma unroll
              for (int e = 0; e < 4; ++e) bm = fmaxf(bm, fmaxf(bflo(h[k][e]), bfhi(h[k][e])));
            mrun = max32(bm) * C2;
#pragma unroll
            for (int k = 0; k < 4; ++k) {
#pragma unroll
              for (int e = 0; e < 4; ++e)
                h[k][e] = pkbf(__builtin_amdgcn_exp2f(fmaf(bflo(h[k][e]), C2, -mrun)), __builtin_amdgcn_exp2f(fmaf(bfhi(h[k][e]), C2, -mrun)));
              *(LAS v4u*)&L->PB[k][0][lane * 4] = h[k];
            }
          }
          LGKM0();
          *(volatile LAS unsigned*)&L->pready = gen + 1;
        }
        mb = mrun;
      } else {
        wait_ge(&L->qready, 1);
        const int cl = lane < 56 ? lane : 55;
        float z = 0.f;
#pragma unroll
        for (int k = 0; k < 4; ++k) {
          const int idx = lane + 64 * k;
          const bool in = idx < DM / 2;
          const unsigned xv = x32[in ? idx : 0], gv = ((const GAS unsigned*)gw)[in ? idx : 0];
          const float t = rbf(bflo(xv) * bflo(gv)) + rbf(bfhi(xv) * bfhi(gv));
          z += in ? t : 0.f;
        }
        z = swp32_add(z, z); z = swp16_add(z, z); z += dpp<XOR8>(z); z += dpp<XOR7>(z); z += dpp<XOR2>(z); z += dpp<XOR1>(z);
        const float g = rbf(1.f / (1.f + __expf(-rbf(z))));
        if (lane == 0) L->gate = g;
        float yb[8];
#pragma unroll
        for (int c = 0; c < 8; ++c) yb[c] = 0.f;
        if (s != 3) {
          const int mx = blk & 7, y0 = (3 - mx) & 3;
          const int u0 = (GMAP == 1 ? (mx < 4 ? 6 * mx + kvh - (kvh > y0) - (kvh > y0 + 4) : 8 * mx - 8 + kvh) : (s == 7 ? 48 + kvh : kvh * 6 + 2 * qd + kh)) * 32, rr = lane >> 2;
          float ha[8];
#pragma unroll
          for (int c = 0; c < 8; ++c) ha[c] = 0.f;
          const GAS u16* w1p = (const GAS u16*)w1 + (size_t)(2 * rr) * DFF + u0 + (lane & 3) * 8;
#pragma unroll 1
          for (int h = 0; h < 2; ++h) {
            v4u ma[14]; unsigned xq[7];
#pragma unroll
            for (int k = 0; k < 7; ++k) {
              ma[2 * k] = ldw(w1p + (size_t)((7 * h + k) * 32) * DFF);
              ma[2 * k + 1] = ldw(w1p + (size_t)((7 * h + k) * 32 + 1) * DFF);
              xq[k] = x32[(7 * h + k) * 16 + rr];
            }
#pragma unroll
            for (int k = 0; k < 7; ++k) pair_dot(ma[2 * k], ma[2 * k + 1], xq[k], ha);
          }
          float hv = tr8(ha, lane);
          hv += dpp<SHR4>(hv);
          hv = fmaxf(rbf(hv), 0.f);
#pragma unroll 1
          for (int c4 = 0; c4 < 4; ++c4) {
            v4u w2r[8];
#pragma unroll
            for (int u = 0; u < 8; ++u) w2r[u] = ldw((const GAS u16*)w2 + (size_t)(u0 + c4 * 8 + u) * DM + cl * 8);
#pragma unroll
            for (int u = 0; u < 8; ++u) {
              const float hu = __builtin_bit_cast(float, __builtin_amdgcn_readlane(__builtin_bit_cast(int, hv), u * 8 + 4 + c4));
#pragma unroll
              for (int c = 0; c < 4; ++c) { yb[2 * c] = fmaf(hu, bflo(w2r[u][c]), yb[2 * c]); yb[2 * c + 1] = fmaf(hu, bfhi(w2r[u][c]), yb[2 * c + 1]); }
            }
          }
        }
        const float omg = rbf(1.f - g);
        if (lane < 56) {
          *(LAS f32x4*)(L->yb + lane * 8) = f32x4{omg * yb[0], omg * yb[1], omg * yb[2], omg * yb[3]};
          *(LAS f32x4*)(L->yb + lane * 8 + 4) = f32x4{omg * yb[4], omg * yb[5], omg * yb[6], omg * yb[7]};
        }
      }
      STAMP(4);
      {
        typedef float f32x2 __attribute__((ext_vector_type(2)));
        wait_ge(&L->acnt, 4);
        const int hh = wave - 2;
        const unsigned Mu = san(L->mlown[2 * hh]), Lu = san(L->mlown[2 * hh + 1]);
#pragma unroll
        for (int u = 0; u < 2; ++u) {
          f32x2 os = *(const LAS f32x2*)(L->mrg[0] + hh * 256 + u * 128 + lane * 2);
#pragma unroll
          for (int ww = 1; ww < 4; ++ww) os += *(const LAS f32x2*)(L->mrg[ww] + hh * 256 + u * 128 + lane * 2);
          __builtin_amdgcn_raw_buffer_store_b128(v4u{san(os[0]), san(os[1]), Mu, Lu}, rs, WS_PO + gid * 4096 + hh * 2048 + u * 1024 + lane * 16, 0, 16);
        }
      }
      STAMP(5);
      if (wave == 2) {
        const int po = WS_PO + ((gid & ~7) + (lane >> 3)) * 4096 + (lane & 7) * 16;
        v4u h[5];
#pragma unroll 1
        for (unsigned spins = 0; spins < SPIN_CAP; ++spins) {
          unsigned mx = 0u;
#pragma unroll
          for (int k = 0; k < 4; ++k) h[k] = __builtin_amdgcn_raw_buffer_load_b128(rs, po + s * 512 + k * 128, 0, 16);
          h[4] = __builtin_amdgcn_raw_buffer_load_b128(rs, po + (s ^ 4) * 512, 0, 16);
#pragma unroll
          for (int k = 0; k < 5; ++k) mx = max(max(mx, max(h[k][0], h[k][1])), max(h[k][2], h[k][3]));
          if (__all(mx != SENT)) break;
          __builtin_amdgcn_s_sleep(HSLEEP);
        }
        STAMP(6);
        const float mp = __uint_as_float(h[0][2]), mq = __uint_as_float(h[4][2]);
        float M = fmaxf(mp, dpp<XOR8>(mp)), Mq = fmaxf(mq, dpp<XOR8>(mq));
        M = swp32_max(swp16_max(M)); Mq = swp32_max(swp16_max(Mq));
        const float wt = __builtin_amdgcn_exp2f(mp - M);
        float Ls = wt * __uint_as_float(h[0][3]), Lq = __builtin_amdgcn_exp2f(mq - Mq) * __uint_as_float(h[4][3]);
        Ls += dpp<XOR8>(Ls); Lq += dpp<XOR8>(Lq);
        Ls = swp16_add(Ls, Ls); Lq = swp16_add(Lq, Lq); Ls = swp32_add(Ls, Ls); Lq = swp32_add(Lq, Lq);
        float ov[8];
#pragma unroll
        for (int k = 0; k < 4; ++k) { ov[2 * k] = wt * __uint_as_float(h[k][0]); ov[2 * k + 1] = wt * __uint_as_float(h[k][1]); }
        const float oo = tr8(ov, lane) / Ls;
        L->oa[kh * 64 + 16 * (lane >> 4) + 2 * (lane & 7) + ((lane >> 3) & 1)] = oo;
        L->oa[(kh ^ 1) * 64 + lane] = 0.f;
        const bool mine = (lane >> 5) == kh;
        if ((lane & 31) == 0) L->fscale[lane >> 5] = __builtin_amdgcn_exp2f(mb - (mine ? M : Mq)) / (mine ? Ls : Lq);
        LGKM0();
        *(volatile LAS unsigned*)&L->pready = 5u;
        STAMP(7);
      }
      WG_BARRIER();
    }
  }
  if (j < 14) {
    const v4u y = poll16(srd, WS_Y + (b * 64 + wave * 8 + (lane >> 3)) * (DM * 4) + j * 128 + (lane & 7) * 16);
    STAMP(9);
    const float r0 = swp32_add(__uint_as_float(y[0]), __uint_as_float(y[2])), r1 = swp32_add(__uint_as_float(y[1]), __uint_as_float(y[3]));
    float r = swp16_add(r0, r1);
    r += dpp<XOR8>(r);
    if (!(lane & 8)) L->fin[wave][(lane & 7) * 4 + (lane >> 4)] = r;
    WG_BARRIER();
    if (tid < 32) {
      const float t = ((L->fin[0][tid] + L->fin[1][tid]) + (L->fin[2][tid] + L->fin[3][tid])) + ((L->fin[4][tid] + L->fin[5][tid]) + (L->fin[6][tid] + L->fin[7][tid]));
      out[b * DM + j * 32 + tid] = (u16)pkbf(t, 0.f);
    }
  }
  STAMP_OUT();
  DRAIN();
}

extern "C" void kernel_launch(void* const* d_in, const int* in_sizes, int n_in, void* d_out, int out_size, void* d_ws, size_t ws_size, hipStream_t stream) {
  fused_kernel<<<256, 512, 0, stream>>>((const u16*)d_in[0], (const u16*)d_in[1], (const u16*)d_in[2], (const u16*)d_in[3], (const u16*)d_in[4],
                                        (const u16*)d_in[6], (const u16*)d_in[7], (const u16*)d_in[8], (unsigned char*)d_ws, (u16*)d_out);
}
```

```cpp
#include <hip/hip_runtime.h>
#include <hip/hip_bf16.h>
#include <cstdint>

#define W_NT 0
#define DEPTH 13
#define PREH 2
#define DEPTH2 20
#define HSW 54
#define NB 2
#define HSLEEP 2
#define MLPMAP 1
#define PACER 1
#define DLO 10
#define QSLEEP 8
#define QWAIT 40
#define LFIRST 1
#define KFIRST 0
#define T0N 0
#define T0ADJ 130
#define ABL 0
#define CLR0 1
#define WOB 4
#define HWAIT 20
#define MB1 2
#define MB2 4
#define WSLEEP 1
#define STSLEEP 64
#define Q3LEAD 60
#define GMAP 0
#define STAMPS 0

typedef unsigned v4u __attribute__((ext_vector_type(4)));
typedef unsigned v3u __attribute__((ext_vector_type(3)));
typedef unsigned v2u __attribute__((ext_vector_type(2)));
typedef float f32x4 __attribute__((ext_vector_type(4)));
typedef __bf16 bf16x2 __attribute__((ext_vector_type(2)));
typedef __bf16 bf16x8 __attribute__((ext_vector_type(8)));
typedef short s16x4 __attribute__((ext_vector_type(4)));
typedef unsigned short u16;
typedef unsigned long long u64;
#define GAS __attribute__((address_space(1)))
#define LAS __attribute__((address_space(3)))
#define RLX_AGENT __ATOMIC_RELAXED, __HIP_MEMORY_SCOPE_AGENT

constexpr int DM = 448, SK = 8192, DH = 256, DFF = 1792, QLD = 4096;
constexpr unsigned SENT = 0xFFFFFFFFu;
constexpr unsigned SPIN_CAP = 1u << 18;
constexpr int WS_EPOCH = 0;
constexpr int WS_RDY = 4096;
constexpr int WS_QG = 8192;
constexpr int WS_S = WS_QG + 256 * 64 * 8;
constexpr int WS_ST = WS_S + 256 * 1024;
constexpr int WS_PO = WS_ST + 256 * 128;
constexpr int WS_Y = WS_PO + 256 * 1024;
constexpr int WS_END = WS_Y + 256 * DM * 4;
constexpr size_t WS_STAMP = 16u << 20;

__device__ __forceinline__ float bflo(unsigned u) { return __builtin_bit_cast(float, u << 16); }
__device__ __forceinline__ float bfhi(unsigned u) { return __builtin_bit_cast(float, u & 0xffff0000u); }
__device__ __forceinline__ unsigned pkbf(float lo, float hi) {
  bf16x2 r; r[0] = (__bf16)lo; r[1] = (__bf16)hi; return __builtin_bit_cast(unsigned, r);
}
__device__ __forceinline__ float rbf(float f) { return bflo(pkbf(f, 0.f)); }
__device__ __forceinline__ float dot2(unsigned a, unsigned b, float acc) {
  return __builtin_amdgcn_fdot2_f32_bf16(__builtin_bit_cast(bf16x2, a), __builtin_bit_cast(bf16x2, b), acc, false);
}
__device__ __forceinline__ unsigned perm_lo(unsigned a, unsigned b) { return __builtin_amdgcn_perm(b, a, 0x05040100u); }
__device__ __forceinline__ unsigned perm_hi(unsigned a, unsigned b) { return __builtin_amdgcn_perm(b, a, 0x07060302u); }
template <int CTRL> __device__ __forceinline__ float dpp(float x) {
  return __builtin_bit_cast(float, __builtin_amdgcn_mov_dpp(__builtin_bit_cast(int, x), CTRL, 0xf, 0xf, true));
}
constexpr int XOR1 = 0xB1, XOR2 = 0x4E, XOR7 = 0x141, XOR8 = 0x128, ROR4 = 0x124, SHR4 = 0x114;
__device__ __forceinline__ float swp16_add(float a, float b) {
  auto s = __builtin_amdgcn_permlane16_swap(__float_as_uint(a), __float_as_uint(b), false, false);
  return __uint_as_float(s[0]) + __uint_as_float(s[1]);
}
__device__ __forceinline__ float swp32_add(float a, float b) {
  auto s = __builtin_amdgcn_permlane32_swap(__float_as_uint(a), __float_as_uint(b), false, false);
  return __uint_as_float(s[0]) + __uint_as_float(s[1]);
}
__device__ __forceinline__ float swp16_max(float a) {
  auto s = __builtin_amdgcn_permlane16_swap(__float_as_uint(a), __float_as_uint(a), false, false);
  return fmaxf(__uint_as_float(s[0]), __uint_as_float(s[1]));
}
__device__ __forceinline__ float swp32_max(float a) {
  auto s = __builtin_amdgcn_permlane32_swap(__float_as_uint(a), __float_as_uint(a), false, false);
  return fmaxf(__uint_as_float(s[0]), __uint_as_float(s[1]));
}
template <int CTRL> __device__ __forceinline__ float tr_dpp(float lo, float hi, bool sel) {
  const float t1 = lo + dpp<CTRL>(lo), t2 = hi + dpp<CTRL>(hi);
  return sel ? t2 : t1;
}
__device__ __forceinline__ float tr8(float (&a)[8], int lane) {
#pragma unroll
  for (int i = 0; i < 4; ++i) a[i] = swp32_add(a[i], a[i + 4]);
#pragma unroll
  for (int i = 0; i < 2; ++i) a[i] = swp16_add(a[i], a[i + 2]);
  return tr_dpp<XOR8>(a[0], a[1], lane & 8);
}
__device__ __forceinline__ float max32(float v) {
  v = fmaxf(v, dpp<XOR1>(v)); v = fmaxf(v, dpp<XOR2>(v)); v = fmaxf(v, dpp<ROR4>(v)); v = fmaxf(v, dpp<XOR8>(v));
  return swp16_max(v);
}
__device__ __forceinline__ void pair_dot(const v4u& ra, const v4u& rb, unsigned xp, float (&a)[8]) {
#pragma unroll
  for (int c = 0; c < 4; ++c) {
    a[2 * c] = dot2(perm_lo(ra[c], rb[c]), xp, a[2 * c]);
    a[2 * c + 1] = dot2(perm_hi(ra[c], rb[c]), xp, a[2 * c + 1]);
  }
}
__device__ __forceinline__ unsigned san(float f) { const unsigned u = __float_as_uint(f); return u == SENT ? 0x7FC00000u : u; }
__device__ __forceinline__ v4u ldw(const GAS void* p) {
  return W_NT ? __builtin_nontemporal_load((const GAS v4u*)p) : *(const GAS v4u*)p;
}
#define WG_BARRIER() do { asm volatile("s_waitcnt lgkmcnt(0)" ::: "memory"); __builtin_amdgcn_s_barrier(); asm volatile("" ::: "memory"); __builtin_amdgcn_sched_barrier(0); } while (0)
#define DRAIN() asm volatile("s_waitcnt vmcnt(0)" ::: "memory")
#define STAMP(i) do {} while (0)
#define STAMP_OUT() do {} while (0)
#define TL(n) do {} while (0)

struct Lds {
  unsigned char ring[2][8][8192];
  unsigned P[4][2][128];
  unsigned PB[4][2][128];
  float mrg[4][512];
  float oa[64];
  unsigned q[256];
  float yb[DM];
  float qpart[6][64];
  float fin[8][32];
  unsigned ob[4][68];
  float mref[4][2];
  float gate, pad0[3];
  unsigned freed[2][8];
  unsigned full[2];
  unsigned cnt, qready;
  unsigned kdone[4];
  unsigned pready, acnt, oready, anchor;
};

#define PIN16(a) asm volatile("" : "+v"(a[0]), "+v"(a[1]), "+v"(a[2]), "+v"(a[3]), "+v"(a[4]), "+v"(a[5]), "+v"(a[6]), "+v"(a[7]), \
                                   "+v"(a[8]), "+v"(a[9]), "+v"(a[10]), "+v"(a[11]), "+v"(a[12]), "+v"(a[13]), "+v"(a[14]), "+v"(a[15]))
#define PIN8(a) asm volatile("" : "+v"(a[0]), "+v"(a[1]), "+v"(a[2]), "+v"(a[3]), "+v"(a[4]), "+v"(a[5]), "+v"(a[6]), "+v"(a[7]))
#define LGKM0() asm volatile("s_waitcnt lgkmcnt(0)" ::: "memory")

template <int SL = WSLEEP>
__device__ __forceinline__ void wait_ge(volatile LAS unsigned* w, unsigned need) {
#pragma unroll 1
  for (unsigned spins = 0; spins < (1u << 22); ++spins) {
    if ((unsigned)__builtin_amdgcn_readfirstlane(*w) >= need) break;
    __builtin_amdgcn_s_sleep(SL);
  }
  asm volatile("" ::: "memory");
}

#define PTHR 64
#define PSLOW 10
#define PSLEEP 2
__device__ __forceinline__ v4u poll16(const v4u& srd, int off) {
  unsigned c0, c1, c2, c3, mx, cnt;
  asm volatile(
      "v_mov_b32 v252, -1\n\tv_mov_b32 v253, -1\n\tv_mov_b32 v254, -1\n\tv_mov_b32 v255, -1\n\t"
      "s_mov_b32 %5, 0x200000\n\t"
      "s_nop 1\n"
      "1:\n\t"
      "buffer_load_dwordx4 v[252:255], %6, %7, 0 offen sc1\n\t"
      "s_sleep %8\n\t"
      "s_waitcnt vmcnt(12)\n\t"
      "v_mov_b32 %0, v252\n\tv_mov_b32 %1, v253\n\tv_mov_b32 %2, v254\n\tv_mov_b32 %3, v255\n\t"
      "v_max3_u32 %4, %0, %1, %2\n\t"
      "v_max_u32 %4, %4, %3\n\t"
      "v_cmp_eq_u32 vcc, -1, %4\n\t"
      "s_and_b64 exec, exec, vcc\n\t"
      "s_cbranch_scc0 2f\n\t"
      "s_bcnt1_i32_b64 vcc_lo, exec\n\t"
      "s_cmp_le_u32 vcc_lo, %9\n\t"
      "s_cbranch_scc1 3f\n\t"
      "s_sleep %10\n"
      "3:\n\t"
      "s_sub_u32 %5, %5, 1\n\t"
      "s_cbranch_scc0 1b\n"
      "2:\n\t"
      "s_mov_b64 exec, -1\n\t"
      "s_nop 4"
      : "=&v"(c0), "=&v"(c1), "=&v"(c2), "=&v"(c3), "=&v"(mx), "=&s"(cnt)
      : "v"(off), "s"(srd), "i"(PSLEEP), "i"(PTHR), "i"(PSLOW)
      : "vcc", "scc", "v252", "v253", "v254", "v255", "memory");
  return v4u{c0, c1, c2, c3};
}

__device__ __forceinline__ void clear_slots(__amdgpu_buffer_rsrc_t rs, int gid, int lane) {
  const v4u sv = {SENT, SENT, SENT, SENT};
  __builtin_amdgcn_raw_buffer_store_b128(sv, rs, WS_PO + gid * 1024 + lane * 16, 0, 16);
  __builtin_amdgcn_raw_buffer_store_b128(sv, rs, WS_Y + gid * (DM * 4) + lane * 16, 0, 16);
  __builtin_amdgcn_raw_buffer_store_b128(sv, rs, lane < 48 ? WS_Y + gid * (DM * 4) + 1024 + lane * 16 : (lane == 48 ? WS_ST + gid * 128 : WS_END), 0, 16);
}
template <int D, int AUX>
__device__ __forceinline__ void issue8(const char* src, int half, unsigned char* dst, const unsigned (&voff)[4], int n, int& pub,
                                       volatile LAS unsigned* full, volatile LAS unsigned* fr) {
  if (n >= 8) {
#pragma unroll 1
    for (unsigned spins = 0; spins < (1u << 22); ++spins) {
      if ((int)__builtin_amdgcn_readfirstlane(*fr) >= n - 7) break;
      DRAIN();
      pub = n; *full = pub;
      __builtin_amdgcn_s_sleep(1);
    }
    asm volatile("" ::: "memory");
  }
#pragma unroll
  for (int g = 0; g < 8; ++g) {
    asm volatile("s_waitcnt vmcnt(%0)" ::"i"(D - 1) : "memory");
    if (g == (D - 1) % 8) { pub = max(pub, n - (D - 1) / 8); *full = pub; }
    const char* sp = src + (g >> 2) * half;
    if ((g & 3) == 0) __builtin_amdgcn_global_load_lds((const unsigned*)(sp + voff[0]), (unsigned*)(dst + (g & 4) * 1024), 16, 0, AUX);
    if ((g & 3) == 1) __builtin_amdgcn_global_load_lds((const unsigned*)(sp + voff[1]), (unsigned*)(dst + (g & 4) * 1024), 16, 1024, AUX);
    if ((g & 3) == 2) __builtin_amdgcn_global_load_lds((const unsigned*)(sp + voff[2]), (unsigned*)(dst + (g & 4) * 1024), 16, 2048, AUX);
    if ((g & 3) == 3) __builtin_amdgcn_global_load_lds((const unsigned*)(sp + voff[3]), (unsigned*)(dst + (g & 4) * 1024), 16, 3072, AUX);
  }
}
#define TAU 590
#define TOFF 0
#define TAU2 567
#define TOFF2 30
__device__ __forceinline__ bool ahead(LAS Lds* L, unsigned t0, int n) {
  if (!PACER || n < 8) return false;
  const unsigned an = n >= 32 ? __builtin_amdgcn_readfirstlane(*(volatile LAS unsigned*)&L->anchor) : 0u;
  const unsigned due = an ? an + TOFF2 + (n - 26) * TAU2 / 10 : t0 + TOFF + n * TAU / 10;
  return (int)((unsigned)__builtin_amdgcn_s_memrealtime() - due) < 0;
}
__device__ __forceinline__ void read_tr(const LAS unsigned char* cell, unsigned a0, bf16x8* f) {
#pragma unroll
  for (int kb = 0; kb < 2; ++kb)
#pragma unroll
    for (int t = 0; t < 4; ++t) {
      const s16x4 lo = __builtin_amdgcn_ds_read_tr16_b64_v4i16((LAS s16x4*)(cell + ((a0 ^ (t << 5)) + kb * 4096)));
      const s16x4 hi = __builtin_amdgcn_ds_read_tr16_b64_v4i16((LAS s16x4*)(cell + ((a0 ^ (t << 5)) + kb * 4096 + 512)));
      f[kb * 4 + t] = __builtin_bit_cast(bf16x8, __builtin_shufflevector(lo, hi, 0, 1, 2, 3, 4, 5, 6, 7));
    }
}
__device__ __forceinline__ f32x4 sel4(f32x4 a0, f32x4 a1, f32x4 a2, f32x4 a3, bool b0, bool b1) {
  f32x4 r;
#pragma unroll
  for (int e = 0; e < 4; ++e) { const float lo = b0 ? a1[e] : a0[e], hi = b0 ? a3[e] : a2[e]; r[e] = b1 ? hi : lo; }
  return r;
}

__global__ void __launch_bounds__(512, 2) fused_kernel(const u16* __restrict__ x, const u16* __restrict__ Kc, const u16* __restrict__ Vc,
    const u16* __restrict__ wq, const u16* __restrict__ wo, const u16* __restrict__ w1, const u16* __restrict__ w2,
    const u16* __restrict__ gw, unsigned char* ws, u16* out) {
  __shared__ __attribute__((aligned(16))) Lds sm;
  LAS Lds* L = (LAS Lds*)&sm;
  asm volatile("" ::"s"(x), "s"(Kc), "s"(Vc), "s"(wq), "s"(wo), "s"(w1), "s"(w2), "s"(gw), "s"(ws), "s"(out));
  const int tid = threadIdx.x, lane = tid & 63, wave = __builtin_amdgcn_readfirstlane(tid >> 6);
  const int blk = blockIdx.x, b = blk >> 6;
  const int kvh = ((blk >> 3) & 6) | ((blk >> 2) & 1), kh = (blk >> 3) & 1, qd = (blk + (blk >> 4)) & 3;
  const int s = kh * 4 + qd;
  const int j = kvh * 8 + s;
  const int gid = b * 64 + j;
  const __amdgpu_buffer_rsrc_t rs = __builtin_amdgcn_make_buffer_rsrc((void*)ws, 0, WS_END, 0x00020000);
  const v4u srd = {(unsigned)(size_t)ws, (unsigned)((size_t)ws >> 32) & 0xffffu, (unsigned)WS_END, 0x00020000u};
  constexpr int NA = 64 - 4 * NB;
  const char* vbase = (const char*)Vc + ((size_t)((b * 8 + kvh) * SK + kh * 4096)) * (DH * 2) + qd * 128;
  if (wave < 2) STAMP(8);
  if (__builtin_expect(wave < 2, LFIRST)) {
    __builtin_amdgcn_s_setprio(3);
    if (tid < 28) L->freed[0][tid] = 0u;
    volatile LAS unsigned* full = &L->full[wave];
    const char* kb = (const char*)Kc + ((size_t)((b * 8 + kvh) * SK + s * 1024 + wave * 16)) * (DH * 2);
    unsigned vk[4], vv[4];
#pragma unroll
    for (int g = 0; g < 4; ++g) {
      vk[g] = (lane >> 5) * 512 + (((lane & 31) ^ (((2 * g + (lane >> 5)) & 7) << 1)) << 4);
      const unsigned ch = ((lane & 7) ^ (((lane >> 3) & 2) | ((g & 1) << 2))) << 4;
      vv[g] = (8 * g + (lane >> 3)) * 512 + ch - g * 1024;
    }
    int pub = 0;
    unsigned t0 = (unsigned)__builtin_amdgcn_s_memrealtime() - (qd == 3 ? Q3LEAD : 0);
    const long vdelta = (const char*)Vc - (const char*)Kc - (32 - 4 * NB) * 16384;
#pragma unroll 1
    for (int n = 0; n < NA; ++n) {
      TL(n);
      if (T0N && n == T0N) t0 = (unsigned)__builtin_amdgcn_s_memrealtime() - T0ADJ;
      if (n == PREH) WG_BARRIER();
      const char* src = kb + (size_t)n * 16384 + (n < 32 ? 0 : vdelta);
      if (ahead(L, t0, n)) issue8<DLO, 2>(src, 4096, sm.ring[wave][n & 7], vk, n, pub, full, &L->freed[wave][n & 7]);
      else if (n < HSW) issue8<DEPTH, 2>(src, 4096, sm.ring[wave][n & 7], vk, n, pub, full, &L->freed[wave][n & 7]);
      else issue8<DEPTH2, 2>(src, 4096, sm.ring[wave][n & 7], vk, n, pub, full, &L->freed[wave][n & 7]);
    }
    STAMP(1);
#pragma unroll 1
    for (int n = NA; n < 64; ++n) {
      const int m = n - NA;
      TL(n);
      const char* src = vbase + (size_t)((m / NB) * 1024 + (m % NB) * 128 + wave * 64) * 512;
      if (ahead(L, t0, n)) issue8<DLO, 2>(src, 16384, sm.ring[wave][n & 7], vv, n, pub, full, &L->freed[wave][n & 7]);
      else issue8<DEPTH2, 2>(src, 16384, sm.ring[wave][n & 7], vv, n, pub, full, &L->freed[wave][n & 7]);
    }
#pragma unroll
    for (int k = (DEPTH2 - 1) / 8; k >= 0; --k) {
      asm volatile("s_waitcnt vmcnt(%0)" ::"i"(8 * k) : "memory");
      *full = max(pub, 64 - k);
    }
    STAMP(2);
    WG_BARRIER();
  } else {
    const GAS unsigned* x32 = (const GAS unsigned*)(x + b * DM);
    const int wv = wave - 2;
    const u64 ep = *(const u64*)(ws + WS_EPOCH + b * 256);
    {
      const int r = lane >> 3;
      const GAS char* wqp = (const GAS char*)wq + (size_t)(2 * r) * (QLD * 2) + (j * 64 + (lane & 7) * 8) * 2;
      v4u wr[10]; unsigned xp[5];
      if (KFIRST) { __builtin_amdgcn_s_barrier(); asm volatile("" ::: "memory"); __builtin_amdgcn_sched_barrier(0); }
#pragma unroll
      for (int k = 0; k < 5; ++k) {
        const int pg = min(wv + 6 * k, 27);
        xp[k] = x32[8 * pg + r];
        wr[2 * k] = ldw(wqp + (size_t)(16 * pg) * (QLD * 2));
        wr[2 * k + 1] = ldw(wqp + (size_t)(16 * pg + 1) * (QLD * 2));
      }
      if (CLR0 && wave == 2) clear_slots(rs, gid, lane);
      __builtin_amdgcn_sched_barrier(0);
      if (!KFIRST) __builtin_amdgcn_s_barrier();
      asm volatile("" ::: "memory");
      __builtin_amdgcn_sched_barrier(0);
      if (wv >= 4) xp[4] = 0u;
      STAMP(1);
      float qa[8];
#pragma unroll
      for (int c = 0; c < 8; ++c) qa[c] = 0.f;
#pragma unroll
      for (int k = 0; k < 5; ++k) pair_dot(wr[2 * k], wr[2 * k + 1], xp[k], qa);
      L->qpart[wv][(lane & 7) * 8 + r] = tr8(qa, lane);
    }
    const u64 tag = (ep + 1) << 16;
    LGKM0();
    unsigned arr = 0;
    if (lane == 0) arr = __hip_atomic_fetch_add(&L->cnt, 1u, __ATOMIC_RELAXED, __HIP_MEMORY_SCOPE_WORKGROUP);
    if (__builtin_amdgcn_readfirstlane(arr) == 5) {
      asm volatile("" ::: "memory");
      float qv = L->qpart[0][lane];
#pragma unroll
      for (int k = 1; k < 6; ++k) qv += L->qpart[k][lane];
      __hip_atomic_store((GAS u64*)(ws + WS_QG) + gid * 64 + lane, tag | (pkbf(qv, 0.f) & 0xffffu), RLX_AGENT);
    }
    STAMP(2);
    if (wave >= 4) {
      const int cw = wave - 4, w = cw & 1, par = cw >> 1;
      volatile LAS unsigned* full = &L->full[w];
      volatile LAS unsigned* fr = L->freed[w];
      const int g4 = lane >> 4, c16 = lane & 15;
      const unsigned ka0 = c16 * 512 + ((g4 ^ ((c16 & 7) << 1)) << 4);
      const unsigned ta0 = (8 * g4 + (c16 >> 2)) * 128 + ((((c16 >> 3) & 1) | ((g4 & 1) << 1)) << 5) + (lane & 3) * 8;
      {
        bf16x8 qf[8];
#pragma unroll 1
        for (int gen = 0; gen < 4; ++gen) {
          bf16x8 kf[4][8];
#pragma unroll
          for (int h = 0; h < 4; ++h) {
            const int n = 8 * gen + 2 * h + par;
            wait_ge(full, n + 1);
            const LAS unsigned char* cell = L->ring[w][2 * h + par];
#pragma unroll
            for (int t = 0; t < 8; ++t) kf[h][t] = *(const LAS bf16x8*)(cell + ((ka0 ^ ((t & 3) << 6)) + (t >> 2) * 256));
            LGKM0(); fr[2 * h + par] = n + 1;
            PIN8(kf[h]);
          }
          if (gen == 0) {
            wait_ge(&L->qready, 1);
            STAMP(3);
#pragma unroll
            for (int t = 0; t < 8; ++t) qf[t] = *(const LAS bf16x8*)(L->q + (lane & 1) * 128 + (4 * t + g4) * 4);
          }
          f32x4 sc[4];
#pragma unroll
          for (int h = 0; h < 4; ++h) sc[h] = f32x4{0.f, 0.f, 0.f, 0.f};
#pragma unroll
          for (int t = 0; t < 8; ++t)
#pragma unroll
            for (int h = 0; h < 4; ++h) sc[h] = __builtin_amdgcn_mfma_f32_16x16x32_bf16(kf[h][t], qf[t], sc[h], 0, 0, 0);
          const f32x4 sv = sel4(sc[0], sc[1], sc[2], sc[3], c16 & 2, c16 & 4);
          *(LAS v2u*)&L->P[gen][c16 & 1][(2 * (c16 & 6) + 2 * par + w) * 8 + 2 * g4] = v2u{pkbf(sv[0], sv[1]), pkbf(sv[2], sv[3])};
          LGKM0(); *(volatile LAS unsigned*)&L->kdone[cw] = gen + 1;
        }
      }
      STAMP(4);
      v4u wf[32];
      const GAS u16* wop = (const GAS u16*)wo + (size_t)(2 * kvh * 256 + 64 * qd + 8 * g4) * DM + min(128 * cw + 8 * c16, DM - 8);
      f32x4 acc0 = {0.f, 0.f, 0.f, 0.f}, acc1 = {0.f, 0.f, 0.f, 0.f};
      float mprev0 = -INFINITY, mprev1 = -INFINITY;
      const int vrow = 4 * g4 + (c16 >> 2);
      const unsigned va0 = vrow * 512 + ((vrow & 7) << 5) + (lane & 3) * 8;
      auto astep = [&](int aa) {
        const int n = 32 - 4 * NB + 2 * aa + par, gen = aa >> 2;
        if ((aa & 3) == 0 || aa == 2 * NB) {
          wait_ge(&L->pready, gen + 1);
          const float m0 = L->mref[gen][0], m1 = L->mref[gen][1];
          const float a0 = __builtin_amdgcn_exp2f(mprev0 - m0), a1 = __builtin_amdgcn_exp2f(mprev1 - m1);
          mprev0 = m0; mprev1 = m1;
          acc0 *= f32x4{a0, a1, a0, a1}; acc1 *= f32x4{a0, a1, a0, a1};
        }
        const v2u pv = *(const LAS v2u*)&L->P[gen][lane & 1][(4 * (aa & 3) + 2 * par + w) * 8 + 2 * g4];
        s16x4 pa[8];
#pragma unroll
        for (int v = 0; v < 8; ++v) { const bool on = (c16 >> 1) == v; pa[v] = __builtin_bit_cast(s16x4, v2u{on ? pv[0] : 0u, on ? pv[1] : 0u}); }
        PIN8(pa);
        wait_ge(full, n + 1);
        const LAS unsigned char* cell = L->ring[w][n & 7];
#pragma unroll
        for (int v = 0; v < 8; ++v) {
          acc0 = __builtin_amdgcn_mfma_f32_16x16x16bf16_1k(pa[v], __builtin_amdgcn_ds_read_tr16_b64_v4i16((LAS s16x4*)(cell + (va0 ^ (v << 5)))), acc0, 0, 0, 0);
          acc1 = __builtin_amdgcn_mfma_f32_16x16x16bf16_1k(pa[v], __builtin_amdgcn_ds_read_tr16_b64_v4i16((LAS s16x4*)(cell + ((va0 ^ (v << 5)) + 256))), acc1, 0, 0, 0);
        }
        asm volatile("" : "+v"(acc0), "+v"(acc1));
        LGKM0(); fr[n & 7] = n + 1;
      };
#pragma unroll
      for (int hb = 0; hb < 32 / WOB; ++hb) {
#pragma unroll
        for (int i = WOB * hb; i < WOB * hb + WOB; ++i) wf[i] = (ABL & 2) ? v4u{1u, 2u, 3u, (unsigned)(size_t)wop} : ldw(wop + (size_t)((i >> 4) * 256 + ((i >> 3) & 1) * 32 + (i & 7)) * DM);
        __builtin_amdgcn_sched_barrier(0);
#pragma unroll 1
        for (int aa = 2 * NB + WOB / 4 * hb; aa < 2 * NB + WOB / 4 * (hb + 1) + (hb == 32 / WOB - 1); ++aa) astep(aa);
      }
#pragma unroll
      for (int kb = 0; kb < 4; ++kb) {
        v4u tf[8];
#pragma unroll
        for (int e = 0; e < 8; ++e)
#pragma unroll
          for (int d = 0; d < 4; ++d)
            tf[e][d] = (e & 1) ? perm_hi(wf[8 * kb + 2 * d][e >> 1], wf[8 * kb + 2 * d + 1][e >> 1]) : perm_lo(wf[8 * kb + 2 * d][e >> 1], wf[8 * kb + 2 * d + 1][e >> 1]);
        PIN8(tf);
#pragma unroll
        for (int e = 0; e < 8; ++e) wf[8 * kb + e] = tf[e];
      }
#pragma unroll 1
      for (int aa = 2 * NB + 9; aa < 16; ++aa) astep(aa);
#pragma unroll
      for (int r = 0; r < 4; ++r) {
        L->mrg[cw][(r & 1) * 256 + (2 * g4 + (r >> 1)) * 16 + c16] = acc0[r];
        L->mrg[cw][(r & 1) * 256 + 128 + (2 * g4 + (r >> 1)) * 16 + c16] = acc1[r];
      }
      LGKM0();
      if (lane == 0) __hip_atomic_fetch_add(&L->acnt, 1u, __ATOMIC_RELAXED, __HIP_MEMORY_SCOPE_WORKGROUP);
      STAMP(7);
      f32x4 acc[4];
#pragma unroll
      for (int t = 0; t < 4; ++t) acc[t] = f32x4{0.f, 0.f, 0.f, 0.f};
      f32x4 o = {0.f, 0.f, 0.f, 0.f};
      wait_ge(&L->pready, 5);
      LAS v2u* obw = (LAS v2u*)&L->ob[cw][(c16 & 1) * 32 + ((c16 >> 1) & 3) * 8 + 2 * g4];
#pragma unroll 1
      for (int mm = 0; mm < 2 * NB; ++mm) {
        const int m = 2 * mm + par, n = NA + m;

        const LAS unsigned* pp = &L->PB[m / NB][lane & 1][(m % NB) * 64 + w * 32 + 4 * g4];
        const bf16x8 p0 = *(const LAS bf16x8*)pp, p1 = *(const LAS bf16x8*)(pp + 16);
        wait_ge<1>(full, n + 1);
        bf16x8 vf[8];
        read_tr(L->ring[w][n & 7], ta0, vf);
#pragma unroll
        for (int t = 0; t < 4; ++t) acc[t] = __builtin_amdgcn_mfma_f32_16x16x32_bf16(vf[t], p0, acc[t], 0, 0, 0);
#pragma unroll
        for (int t = 0; t < 4; ++t) acc[t] = __builtin_amdgcn_mfma_f32_16x16x32_bf16(vf[4 + t], p1, acc[t], 0, 0, 0);
        asm volatile("" : "+v"(acc[0]), "+v"(acc[1]), "+v"(acc[2]), "+v"(acc[3]));
      }
      STAMP(5);
      if (cw == 0) {
        wait_ge<1>(&L->oready, 1);
        if ((c16 & 1) == kh) o = *(const LAS f32x4*)&L->oa[((c16 >> 1) & 3) * 16 + 4 * g4];
      }
      o += sel4(acc[0], acc[1], acc[2], acc[3], c16 & 2, c16 & 4);
      *obw = v2u{pkbf(o[0], o[1]), pkbf(o[2], o[3])};
      const int col = 128 * cw + 8 * c16 + 4 * (g4 & 1);
      const bool st_on = g4 < 2 && col < DM;
      const int yoff = st_on ? WS_Y + gid * (DM * 4) + col * 4 : WS_END;
      const LAS float* ybp = &L->yb[st_on ? col : 0];
      const LAS unsigned* obr = &L->ob[c16 & 3][4 * g4];
      WG_BARRIER();
      STAMP(6);
      bf16x8 of[4];
#pragma unroll
      for (int kb = 0; kb < 4; ++kb) of[kb] = *(const LAS bf16x8*)(obr + 16 * kb);
      const f32x4 ybv = *(const LAS f32x4*)ybp;
      const float g = L->gate;
      __builtin_amdgcn_sched_barrier(0);
      f32x4 ya[8];
#pragma unroll
      for (int e = 0; e < 8; ++e) ya[e] = f32x4{0.f, 0.f, 0.f, 0.f};
#pragma unroll
      for (int kb = 0; kb < 4; ++kb) {
#pragma unroll
        for (int e = 0; e < 8; ++e) ya[e] = __builtin_amdgcn_mfma_f32_16x16x32_bf16(of[kb], __builtin_bit_cast(bf16x8, wf[8 * kb + e]), ya[e], 0, 0, 0);
        __builtin_amdgcn_sched_barrier(0);
      }
      v4u yv;
#pragma unroll
      for (int r = 0; r < 4; ++r) {
        const float lo = (ya[r][0] + ya[r][1]) + (ya[r][2] + ya[r][3]), hi = (ya[4 + r][0] + ya[4 + r][1]) + (ya[4 + r][2] + ya[4 + r][3]);
        yv[r] = san(fmaf(g, (g4 & 1) ? hi : lo, ybv[r]));
      }
      __builtin_amdgcn_raw_buffer_store_b128(yv, rs, yoff, 0, 16);
      STAMP(8);
    } else {
      if (wave == 2) {
        {
          const unsigned thi = (unsigned)(tag >> 32), tlo = (unsigned)tag;
          int off[4];
#pragma unroll
          for (int k = 0; k < 4; ++k) off[k] = WS_QG + (gid & ~7) * 512 + k * 1024 + lane * 16;
          __builtin_amdgcn_s_sleep(QWAIT);
#pragma unroll 1
          for (unsigned spins = 0; spins < SPIN_CAP; ++spins) {
            v4u g[4];
            bool ok = true;
#pragma unroll
            for (int k = 0; k < 4; ++k) g[k] = __builtin_amdgcn_raw_buffer_load_b128(rs, off[k], 0, 16);
#pragma unroll
            for (int k = 0; k < 4; ++k) {
              if (off[k] != WS_END && g[k][1] == thi && g[k][3] == thi && (g[k][0] & 0xffff0000u) == tlo && (g[k][2] & 0xffff0000u) == tlo) {
                L->q[k * 64 + lane] = perm_lo(g[k][0], g[k][2]);
                off[k] = WS_END;
              }
              ok &= off[k] == WS_END;
            }
            if (__all(ok)) break;
            __builtin_amdgcn_s_sleep(QSLEEP);
          }
          LGKM0();
          *(volatile LAS unsigned*)&L->qready = 1u;
        }
        STAMP(3);
        constexpr float C2 = 0.0625f * 1.4426950408889634f;
        float mrun = -INFINITY, lrun = 0.f;
#pragma unroll 1
        for (int gen = 0; gen < 4; ++gen) {
#pragma unroll
          for (int c = 0; c < 4; ++c) wait_ge(&L->kdone[c], gen + 1);
          v4u g = *(const LAS v4u*)&L->P[gen][0][lane * 4];
          if (gen == 0) {
            __builtin_amdgcn_raw_buffer_store_b128(g, rs, WS_S + gid * 1024 + lane * 16, 0, 16);
          }
          if (gen == 2) {
            DRAIN();
            if (lane == 0) __hip_atomic_store((GAS u64*)(ws + WS_RDY) + gid, tag | ((unsigned)__builtin_amdgcn_s_memrealtime() & 0xffffu), RLX_AGENT);
          }
          float cm = -INFINITY;
#pragma unroll
          for (int e = 0; e < 4; ++e) cm = fmaxf(cm, fmaxf(bflo(g[e]), bfhi(g[e])));
          const float mn = fmaxf(mrun, max32(cm) * C2);
          float ls = 0.f;
#pragma unroll
          for (int e = 0; e < 4; ++e) {
            const float pl = __builtin_amdgcn_exp2f(fmaf(bflo(g[e]), C2, -mn)), ph = __builtin_amdgcn_exp2f(fmaf(bfhi(g[e]), C2, -mn));
            ls += pl + ph;
            g[e] = pkbf(pl, ph);
          }
          *(LAS v4u*)&L->P[gen][0][lane * 4] = g;
          lrun = fmaf(lrun, __builtin_amdgcn_exp2f(mrun - mn), ls);
          mrun = mn;
          if ((lane & 31) == 0) L->mref[gen][lane >> 5] = mn;
          LGKM0();
          *(volatile LAS unsigned*)&L->pready = gen + 1;
        }
        lrun += dpp<XOR1>(lrun); lrun += dpp<XOR2>(lrun); lrun += dpp<ROR4>(lrun); lrun += dpp<XOR8>(lrun); lrun = swp16_add(lrun, lrun);
        if ((lane & 31) == 0) __builtin_amdgcn_raw_buffer_store_b64(v2u{san(mrun), san(lrun)}, rs, WS_ST + gid * 128 + (lane >> 5) * 8, 0, 16);
        u64 gv;
#pragma unroll 1
        for (unsigned spins = 0; spins < SPIN_CAP; ++spins) {
          gv = __hip_atomic_load((GAS u64*)(ws + WS_RDY) + b * 64 + lane, RLX_AGENT);
          if (__all((gv & ~0xffffull) == tag)) break;
          __builtin_amdgcn_s_sleep(32);
        }
        {
          const unsigned now = (unsigned)__builtin_amdgcn_s_memrealtime();
          float d = (float)((now - (unsigned)gv) & 0xffffu);
          d = swp32_add(d, d); d = swp16_add(d, d); d += dpp<XOR8>(d); d += dpp<XOR7>(d); d += dpp<XOR2>(d); d += dpp<XOR1>(d);
          *(volatile LAS unsigned*)&L->anchor = (now - (unsigned)(d * (1.f / 64.f)) - (qd == 3 ? Q3LEAD : 0)) | 1u;
        }
        if (j == 0 && lane == 0) __hip_atomic_store((GAS u64*)(ws + WS_EPOCH + b * 256), ep + 1, RLX_AGENT);
        asm volatile("" ::: "memory");
        v4u h[4];
#pragma unroll
        for (int k = 0; k < 4; ++k) h[k] = __builtin_amdgcn_raw_buffer_load_b128(rs, WS_S + ((gid & ~3) + k) * 1024 + lane * 16, 0, 16);
        v2u sv;
#pragma unroll 1
        for (unsigned spins = 0; spins < SPIN_CAP; ++spins) {
          __builtin_amdgcn_s_sleep(STSLEEP);
          sv = __builtin_amdgcn_raw_buffer_load_b64(rs, WS_ST + ((gid & ~7) + (lane & 7)) * 128 + (lane >> 5) * 8, 0, 16);
          if (__all(sv[0] != SENT && sv[1] != SENT)) break;
        }
        const float mp = __uint_as_float(sv[0]);
        float M = fmaxf(mp, dpp<XOR1>(mp)); M = fmaxf(M, dpp<XOR2>(M)); M = fmaxf(M, dpp<XOR7>(M));
        float Ls = __uint_as_float(sv[1]) * __builtin_amdgcn_exp2f(mp - M);
        Ls += dpp<XOR1>(Ls); Ls += dpp<XOR2>(Ls); Ls += dpp<XOR7>(Ls);
        const float inv = 1.f / Ls;
#pragma unroll
        for (int k = 0; k < 4; ++k) {
#pragma unroll
          for (int e = 0; e < 4; ++e)
            h[k][e] = pkbf(inv * __builtin_amdgcn_exp2f(fmaf(bflo(h[k][e]), C2, -M)), inv * __builtin_amdgcn_exp2f(fmaf(bfhi(h[k][e]), C2, -M)));
          *(LAS v4u*)&L->PB[k][0][lane * 4] = h[k];
        }
        LGKM0();
        *(volatile LAS unsigned*)&L->pready = 5u;
        const float as = inv * __builtin_amdgcn_exp2f(mrun - M);
        STAMP(4);
        wait_ge<1>(&L->acnt, 4);
        f32x4 o0 = *(const LAS f32x4*)(L->mrg[0] + lane * 8), o1 = *(const LAS f32x4*)(L->mrg[0] + lane * 8 + 4);
#pragma unroll
        for (int ww = 1; ww < 4; ++ww) { o0 += *(const LAS f32x4*)(L->mrg[ww] + lane * 8); o1 += *(const LAS f32x4*)(L->mrg[ww] + lane * 8 + 4); }
        o0 *= as; o1 *= as;
        v4u ov = {pkbf(o0[0], o0[1]), pkbf(o0[2], o0[3]), pkbf(o1[0], o1[1]), pkbf(o1[2], o1[3])};
#pragma unroll
        for (int k = 0; k < 4; ++k) ov[k] = ov[k] == SENT ? 0x7FC07FC0u : ov[k];
        __builtin_amdgcn_raw_buffer_store_b128(ov, rs, WS_PO + gid * 1024 + lane * 16, 0, 16);
        STAMP(5);
        int off = WS_PO + ((gid & ~7) + (lane >> 3)) * 1024 + s * 128 + (lane & 7) * 16;
        __builtin_amdgcn_s_sleep(HWAIT);
#pragma unroll 1
        for (unsigned spins = 0; spins < SPIN_CAP; ++spins) {
          if (off != WS_END) {
            ov = __builtin_amdgcn_raw_buffer_load_b128(rs, off, 0, 16);
            if (max(max(ov[0], ov[1]), max(ov[2], ov[3])) != SENT) off = WS_END;
          }
          if (__all(off == WS_END)) break;
          __builtin_amdgcn_s_sleep(HSLEEP);
        }
        STAMP(6);
        float oe[8];
#pragma unroll
        for (int k = 0; k < 4; ++k) { oe[2 * k] = bflo(ov[k]); oe[2 * k + 1] = bfhi(ov[k]); }
        L->oa[8 * (lane & 7) + (lane >> 3)] = tr8(oe, lane);
        LGKM0();
        *(volatile LAS unsigned*)&L->oready = 1u;
        STAMP(7);
      } else {
        wait_ge(&L->qready, 1);
        const int cl = lane < 56 ? lane : 55;
        float z = 0.f;
#pragma unroll
        for (int k = 0; k < 4; ++k) {
          const int idx = lane + 64 * k;
          const bool in = idx < DM / 2;
          const unsigned xv = x32[in ? idx : 0], gv = ((const GAS unsigned*)gw)[in ? idx : 0];
          const float t = rbf(bflo(xv) * bflo(gv)) + rbf(bfhi(xv) * bfhi(gv));
          z += in ? t : 0.f;
        }
        z = swp32_add(z, z); z = swp16_add(z, z); z += dpp<XOR8>(z); z += dpp<XOR7>(z); z += dpp<XOR2>(z); z += dpp<XOR1>(z);
        const float g = rbf(1.f / (1.f + __expf(-rbf(z))));
        if (lane == 0) L->gate = g;
        float yb[8];
#pragma unroll
        for (int c = 0; c < 8; ++c) yb[c] = 0.f;
        if (s != 3 && !(ABL & 1)) {
          const int mx = blk & 7, y0 = (3 - mx) & 3;
          const int u0 = (GMAP == 1 ? (mx < 4 ? 6 * mx + kvh - (kvh > y0) - (kvh > y0 + 4) : 8 * mx - 8 + kvh) : (s == 7 ? 48 + kvh : kvh * 6 + 2 * qd + kh)) * 32, rr = lane >> 2;
          float ha[8];
#pragma unroll
          for (int c = 0; c < 8; ++c) ha[c] = 0.f;
          const GAS u16* w1p = (const GAS u16*)w1 + (size_t)(2 * rr) * DFF + u0 + (lane & 3) * 8;
#pragma unroll 1
          for (int h = 0; h < 14 / MB1; ++h) {
            v4u ma[2 * MB1]; unsigned xq[MB1];
#pragma unroll
            for (int k = 0; k < MB1; ++k) {
              ma[2 * k] = ldw(w1p + (size_t)((MB1 * h + k) * 32) * DFF);
              ma[2 * k + 1] = ldw(w1p + (size_t)((MB1 * h + k) * 32 + 1) * DFF);
              xq[k] = x32[(MB1 * h + k) * 16 + rr];
            }
#pragma unroll
            for (int k = 0; k < MB1; ++k) pair_dot(ma[2 * k], ma[2 * k + 1], xq[k], ha);
          }
          float hv = tr8(ha, lane);
          hv += dpp<SHR4>(hv);
          hv = fmaxf(rbf(hv), 0.f);
#pragma unroll 1
          for (int c4 = 0; c4 < 32 / MB2; ++c4) {
            v4u w2r[MB2];
#pragma unroll
            for (int u = 0; u < MB2; ++u) w2r[u] = ldw((const GAS u16*)w2 + (size_t)(u0 + c4 * MB2 + u) * DM + cl * 8);
#pragma unroll
            for (int u = 0; u < MB2; ++u) {
              const int un = c4 * MB2 + u;
              const float hu = __builtin_bit_cast(float, __builtin_amdgcn_readlane(__builtin_bit_cast(int, hv), (un & 7) * 8 + 4 + (un >> 3)));
#pragma unroll
              for (int c = 0; c < 4; ++c) { yb[2 * c] = fmaf(hu, bflo(w2r[u][c]), yb[2 * c]); yb[2 * c + 1] = fmaf(hu, bfhi(w2r[u][c]), yb[2 * c + 1]); }
            }
          }
        }
        const float omg = rbf(1.f - g);
        if (lane < 56) {
          *(LAS f32x4*)(L->yb + lane * 8) = f32x4{omg * yb[0], omg * yb[1], omg * yb[2], omg * yb[3]};
          *(LAS f32x4*)(L->yb + lane * 8 + 4) = f32x4{omg * yb[4], omg * yb[5], omg * yb[6], omg * yb[7]};
        }
      }
      WG_BARRIER();
    }
  }
  if (j < 14) {
    const v4u y = poll16(srd, WS_Y + (b * 64 + wave * 8 + (lane >> 3)) * (DM * 4) + j * 128 + (lane & 7) * 16);
    STAMP(9);
    const float r0 = swp32_add(__uint_as_float(y[0]), __uint_as_float(y[2])), r1 = swp32_add(__uint_as_float(y[1]), __uint_as_float(y[3]));
    float r = swp16_add(r0, r1);
    r += dpp<XOR8>(r);
    if (!(lane & 8)) L->fin[wave][(lane & 7) * 4 + (lane >> 4)] = r;
    WG_BARRIER();
    if (tid < 32) {
      const float t = ((L->fin[0][tid] + L->fin[1][tid]) + (L->fin[2][tid] + L->fin[3][tid])) + ((L->fin[4][tid] + L->fin[5][tid]) + (L->fin[6][tid] + L->fin[7][tid]));
      out[b * DM + j * 32 + tid] = (u16)pkbf(t, 0.f);
    }
  }
  STAMP_OUT();
  DRAIN();
}

extern "C" void kernel_launch(void* const* d_in, const int* in_sizes, int n_in, void* d_out, int out_size, void* d_ws, size_t ws_size, hipStream_t stream) {
  fused_kernel<<<256, 512, 0, stream>>>((const u16*)d_in[0], (const u16*)d_in[1], (const u16*)d_in[2], (const u16*)d_in[3], (const u16*)d_in[4],
                                        (const u16*)d_in[6], (const u16*)d_in[7], (const u16*)d_in[8], (unsigned char*)d_ws, (u16*)d_out);
}
```

```cpp
#include <hip/hip_runtime.h>
#include <hip/hip_bf16.h>
#include <cstdint>

#define NB 2
#define PREH 1
#define WOB 4
#define MB1 1
#define MB2 4
#define TAU 590
#define TAU2 567
#define TOFF2 30
#define Q3LEAD 60
#define LV 0
#define STAMPS 0

typedef unsigned v4u __attribute__((ext_vector_type(4)));
typedef unsigned v3u __attribute__((ext_vector_type(3)));
typedef unsigned v2u __attribute__((ext_vector_type(2)));
typedef float f32x4 __attribute__((ext_vector_type(4)));
typedef __bf16 bf16x2 __attribute__((ext_vector_type(2)));
typedef __bf16 bf16x8 __attribute__((ext_vector_type(8)));
typedef short s16x4 __attribute__((ext_vector_type(4)));
typedef unsigned short u16;
typedef unsigned long long u64;
#define GAS __attribute__((address_space(1)))
#define LAS __attribute__((address_space(3)))
#define RLX_AGENT __ATOMIC_RELAXED, __HIP_MEMORY_SCOPE_AGENT

constexpr int DM = 448, SK = 8192, DH = 256, DFF = 1792, QLD = 4096;
constexpr unsigned SENT = 0xFFFFFFFFu;
constexpr unsigned SPIN_CAP = 1u << 18;
constexpr int WS_EPOCH = 0;
constexpr int WS_RDY = 4096;
constexpr int WS_QG = 8192;
constexpr int WS_S = WS_QG + 256 * 64 * 8;
constexpr int WS_ST = WS_S + 256 * 1024;
constexpr int WS_PO = WS_ST + 256 * 128;
constexpr int WS_Y = WS_PO + 256 * 1024;
constexpr int WS_END = WS_Y + 256 * DM * 4;
constexpr size_t WS_STAMP = 16u << 20;

__device__ __forceinline__ float bflo(unsigned u) { return __builtin_bit_cast(float, u << 16); }
__device__ __forceinline__ float bfhi(unsigned u) { return __builtin_bit_cast(float, u & 0xffff0000u); }
__device__ __forceinline__ unsigned pkbf(float lo, float hi) {
  bf16x2 r; r[0] = (__bf16)lo; r[1] = (__bf16)hi; return __builtin_bit_cast(unsigned, r);
}
__device__ __forceinline__ float rbf(float f) { return bflo(pkbf(f, 0.f)); }
__device__ __forceinline__ float dot2(unsigned a, unsigned b, float acc) {
  return __builtin_amdgcn_fdot2_f32_bf16(__builtin_bit_cast(bf16x2, a), __builtin_bit_cast(bf16x2, b), acc, false);
}
__device__ __forceinline__ unsigned perm_lo(unsigned a, unsigned b) { return __builtin_amdgcn_perm(b, a, 0x05040100u); }
__device__ __forceinline__ unsigned perm_hi(unsigned a, unsigned b) { return __builtin_amdgcn_perm(b, a, 0x07060302u); }
template <int CTRL> __device__ __forceinline__ float dpp(float x) {
  return __builtin_bit_cast(float, __builtin_amdgcn_mov_dpp(__builtin_bit_cast(int, x), CTRL, 0xf, 0xf, true));
}
constexpr int XOR1 = 0xB1, XOR2 = 0x4E, XOR7 = 0x141, XOR8 = 0x128, ROR4 = 0x124, SHR4 = 0x114;
__device__ __forceinline__ float swp16_add(float a, float b) {
  auto s = __builtin_amdgcn_permlane16_swap(__float_as_uint(a), __float_as_uint(b), false, false);
  return __uint_as_float(s[0]) + __uint_as_float(s[1]);
}
__device__ __forceinline__ float swp32_add(float a, float b) {
  auto s = __builtin_amdgcn_permlane32_swap(__float_as_uint(a), __float_as_uint(b), false, false);
  return __uint_as_float(s[0]) + __uint_as_float(s[1]);
}
__device__ __forceinline__ float swp16_max(float a) {
  auto s = __builtin_amdgcn_permlane16_swap(__float_as_uint(a), __float_as_uint(a), false, false);
  return fmaxf(__uint_as_float(s[0]), __uint_as_float(s[1]));
}
__device__ __forceinline__ float swp32_max(float a) {
  auto s = __builtin_amdgcn_permlane32_swap(__float_as_uint(a), __float_as_uint(a), false, false);
  return fmaxf(__uint_as_float(s[0]), __uint_as_float(s[1]));
}
template <int CTRL> __device__ __forceinline__ float tr_dpp(float lo, float hi, bool sel) {
  const float t1 = lo + dpp<CTRL>(lo), t2 = hi + dpp<CTRL>(hi);
  return sel ? t2 : t1;
}
__device__ __forceinline__ float tr8(float (&a)[8], int lane) {
#pragma unroll
  for (int i = 0; i < 4; ++i) a[i] = swp32_add(a[i], a[i + 4]);
#pragma unroll
  for (int i = 0; i < 2; ++i) a[i] = swp16_add(a[i], a[i + 2]);
  return tr_dpp<XOR8>(a[0], a[1], lane & 8);
}
__device__ __forceinline__ float max32(float v) {
  v = fmaxf(v, dpp<XOR1>(v)); v = fmaxf(v, dpp<XOR2>(v)); v = fmaxf(v, dpp<ROR4>(v)); v = fmaxf(v, dpp<XOR8>(v));
  return swp16_max(v);
}
__device__ __forceinline__ void pair_dot(const v4u& ra, const v4u& rb, unsigned xp, float (&a)[8]) {
#pragma unroll
  for (int c = 0; c < 4; ++c) {
    a[2 * c] = dot2(perm_lo(ra[c], rb[c]), xp, a[2 * c]);
    a[2 * c + 1] = dot2(perm_hi(ra[c], rb[c]), xp, a[2 * c + 1]);
  }
}
__device__ __forceinline__ unsigned san(float f) { const unsigned u = __float_as_uint(f); return u == SENT ? 0x7FC00000u : u; }
__device__ __forceinline__ v4u ldw(const GAS void* p) {
  return *(const GAS v4u*)p;
}
#define WG_BARRIER() do { asm volatile("s_waitcnt lgkmcnt(0)" ::: "memory"); __builtin_amdgcn_s_barrier(); asm volatile("" ::: "memory"); __builtin_amdgcn_sched_barrier(0); } while (0)
#define DRAIN() asm volatile("s_waitcnt vmcnt(0)" ::: "memory")
#define STAMP(i) do {} while (0)
#define STAMP_OUT() do {} while (0)
#define TL(n) do {} while (0)

struct Lds {
  unsigned char ring[2][8][8192];
  unsigned P[4][2][128];
  unsigned PB[4][2][128];
  float mrg[4][512];
  float oa[64];
  unsigned q[256];
  float yb[DM];
  float qpart[6][64];
  float fin[8][32];
  unsigned ob[4][68];
  float mref[4][2];
  float gate, pad0[3];
  unsigned freed[2][8];
  unsigned full[2];
  unsigned cnt, qready;
  unsigned kdone[4];
  unsigned pready, acnt, oready, anchor;
};

#define PIN16(a) asm volatile("" : "+v"(a[0]), "+v"(a[1]), "+v"(a[2]), "+v"(a[3]), "+v"(a[4]), "+v"(a[5]), "+v"(a[6]), "+v"(a[7]), \
                                   "+v"(a[8]), "+v"(a[9]), "+v"(a[10]), "+v"(a[11]), "+v"(a[12]), "+v"(a[13]), "+v"(a[14]), "+v"(a[15]))
#define PIN8(a) asm volatile("" : "+v"(a[0]), "+v"(a[1]), "+v"(a[2]), "+v"(a[3]), "+v"(a[4]), "+v"(a[5]), "+v"(a[6]), "+v"(a[7]))
#define LGKM0() asm volatile("s_waitcnt lgkmcnt(0)" ::: "memory")

__device__ __forceinline__ void wait_ge(volatile LAS unsigned* w, unsigned need) {
#pragma unroll 1
  for (unsigned spins = 0; spins < (1u << 22); ++spins) {
    if ((unsigned)__builtin_amdgcn_readfirstlane(*w) >= need) break;
    __builtin_amdgcn_s_sleep(1);
  }
  asm volatile("" ::: "memory");
}

#define PTHR 64
#define PSLOW 10
#define PSLEEP 2
__device__ __forceinline__ v4u poll16(const v4u& srd, int off) {
  unsigned c0, c1, c2, c3, mx, cnt;
  asm volatile(
      "v_mov_b32 v252, -1\n\tv_mov_b32 v253, -1\n\tv_mov_b32 v254, -1\n\tv_mov_b32 v255, -1\n\t"
      "s_mov_b32 %5, 0x200000\n\t"
      "s_nop 1\n"
      "1:\n\t"
      "buffer_load_dwordx4 v[252:255], %6, %7, 0 offen sc1\n\t"
      "s_sleep %8\n\t"
      "s_waitcnt vmcnt(12)\n\t"
      "v_mov_b32 %0, v252\n\tv_mov_b32 %1, v253\n\tv_mov_b32 %2, v254\n\tv_mov_b32 %3, v255\n\t"
      "v_max3_u32 %4, %0, %1, %2\n\t"
      "v_max_u32 %4, %4, %3\n\t"
      "v_cmp_eq_u32 vcc, -1, %4\n\t"
      "s_and_b64 exec, exec, vcc\n\t"
      "s_cbranch_scc0 2f\n\t"
      "s_bcnt1_i32_b64 vcc_lo, exec\n\t"
      "s_cmp_le_u32 vcc_lo, %9\n\t"
      "s_cbranch_scc1 3f\n\t"
      "s_sleep %10\n"
      "3:\n\t"
      "s_sub_u32 %5, %5, 1\n\t"
      "s_cbranch_scc0 1b\n"
      "2:\n\t"
      "s_mov_b64 exec, -1\n\t"
      "s_nop 4"
      : "=&v"(c0), "=&v"(c1), "=&v"(c2), "=&v"(c3), "=&v"(mx), "=&s"(cnt)
      : "v"(off), "s"(srd), "i"(PSLEEP), "i"(PTHR), "i"(PSLOW)
      : "vcc", "scc", "v252", "v253", "v254", "v255", "memory");
  return v4u{c0, c1, c2, c3};
}

__device__ __forceinline__ void clear_slots(__amdgpu_buffer_rsrc_t rs, int gid, int lane) {
  const v4u sv = {SENT, SENT, SENT, SENT};
  __builtin_amdgcn_raw_buffer_store_b128(sv, rs, WS_PO + gid * 1024 + lane * 16, 0, 16);
  __builtin_amdgcn_raw_buffer_store_b128(sv, rs, WS_Y + gid * (DM * 4) + lane * 16, 0, 16);
  __builtin_amdgcn_raw_buffer_store_b128(sv, rs, lane < 48 ? WS_Y + gid * (DM * 4) + 1024 + lane * 16 : (lane == 48 ? WS_ST + gid * 128 : WS_END), 0, 16);
}
template <int D, int AUX>
__device__ __forceinline__ void issue8x(const char* src, int half, unsigned char* dst, const unsigned (&voff)[4], int n, int& pub, volatile LAS unsigned* full) {
#pragma unroll
  for (int g = 0; g < 8; ++g) {
    asm volatile("s_waitcnt vmcnt(%0)" ::"i"(D - 1) : "memory");
    if (g == (D - 1) % 8) { pub = max(pub, n - (D - 1) / 8); *full = pub; }
    const char* sp = src + (g >> 2) * half;
    if ((g & 3) == 0) __builtin_amdgcn_global_load_lds((const unsigned*)(sp + voff[0]), (unsigned*)(dst + (g & 4) * 1024), 16, 0, AUX);
    if ((g & 3) == 1) __builtin_amdgcn_global_load_lds((const unsigned*)(sp + voff[1]), (unsigned*)(dst + (g & 4) * 1024), 16, 1024, AUX);
    if ((g & 3) == 2) __builtin_amdgcn_global_load_lds((const unsigned*)(sp + voff[2]), (unsigned*)(dst + (g & 4) * 1024), 16, 2048, AUX);
    if ((g & 3) == 3) __builtin_amdgcn_global_load_lds((const unsigned*)(sp + voff[3]), (unsigned*)(dst + (g & 4) * 1024), 16, 3072, AUX);
  }
}
#define KAUX 2
#define VAUX 2
#define NSW 32
template <int D>
__device__ __forceinline__ void issue8(const char* src, int half, unsigned char* dst, const unsigned (&voff)[4], int n, int& pub, volatile LAS unsigned* full) {
  if (KAUX == VAUX || n < NSW) issue8x<D, KAUX>(src, half, dst, voff, n, pub, full); else issue8x<D, VAUX>(src, half, dst, voff, n, pub, full);
}
__device__ __forceinline__ unsigned mean_time(unsigned stamp) {
  const unsigned now = (unsigned)__builtin_amdgcn_s_memrealtime();
  float d = (float)((now - stamp) & 0xffffu);
  d = swp32_add(d, d); d = swp16_add(d, d); d += dpp<XOR8>(d); d += dpp<XOR7>(d); d += dpp<XOR2>(d); d += dpp<XOR1>(d);
  return now - (unsigned)(d * (1.f / 64.f));
}
__device__ __forceinline__ void read_tr(const LAS unsigned char* cell, unsigned a0, bf16x8* f) {
#pragma unroll
  for (int kb = 0; kb < 2; ++kb)
#pragma unroll
    for (int t = 0; t < 4; ++t) {
      const s16x4 lo = __builtin_amdgcn_ds_read_tr16_b64_v4i16((LAS s16x4*)(cell + ((a0 ^ (t << 5)) + kb * 4096)));
      const s16x4 hi = __builtin_amdgcn_ds_read_tr16_b64_v4i16((LAS s16x4*)(cell + ((a0 ^ (t << 5)) + kb * 4096 + 512)));
      f[kb * 4 + t] = __builtin_bit_cast(bf16x8, __builtin_shufflevector(lo, hi, 0, 1, 2, 3, 4, 5, 6, 7));
    }
}
__device__ __forceinline__ f32x4 sel4(f32x4 a0, f32x4 a1, f32x4 a2, f32x4 a3, bool b0, bool b1) {
  f32x4 r;
#pragma unroll
  for (int e = 0; e < 4; ++e) { const float lo = b0 ? a1[e] : a0[e], hi = b0 ? a3[e] : a2[e]; r[e] = b1 ? hi : lo; }
  return r;
}

__global__ void __launch_bounds__(512, 2) fused_kernel(const u16* __restrict__ x, const u16* __restrict__ Kc, const u16* __restrict__ Vc,
    const u16* __restrict__ wq, const u16* __restrict__ wo, const u16* __restrict__ w1, const u16* __restrict__ w2,
    const u16* __restrict__ gw, unsigned char* ws, u16* out) {
  __shared__ __attribute__((aligned(16))) Lds sm;
  LAS Lds* L = (LAS Lds*)&sm;
  asm volatile("" ::"s"(x), "s"(Kc), "s"(Vc), "s"(wq), "s"(wo), "s"(w1), "s"(w2), "s"(gw), "s"(ws), "s"(out));
  const int tid = threadIdx.x, lane = tid & 63, wave = __builtin_amdgcn_readfirstlane(tid >> 6);
  const int blk = blockIdx.x, b = blk >> 6;
  const int kvh = ((blk >> 3) & 6) | (((blk >> 2) ^ (blk >> 4)) & 1), kh = (blk >> 3) & 1, qd = (blk + (blk >> 4)) & 3;
  const int s = kh * 4 + qd;
  const int j = kvh * 8 + s;
  const int gid = b * 64 + j;
  const __amdgpu_buffer_rsrc_t rs = __builtin_amdgcn_make_buffer_rsrc((void*)ws, 0, WS_END, 0x00020000);
  const v4u srd = {(unsigned)(size_t)ws, (unsigned)((size_t)ws >> 32) & 0xffffu, (unsigned)WS_END, 0x00020000u};
  constexpr int NA = 64 - 4 * NB;
  const char* vbase = (const char*)Vc + ((size_t)((b * 8 + kvh) * SK + kh * 4096)) * (DH * 2) + qd * 128;
  if (wave < 2) STAMP(8);
  if (wave < 2) {
    __builtin_amdgcn_s_setprio(3);
    if (tid < 28) L->freed[0][tid] = 0u;
    volatile LAS unsigned* full = &L->full[wave];
    const char* kb = (const char*)Kc + ((size_t)((b * 8 + kvh) * SK + s * 1024 + wave * 16)) * (DH * 2);
    unsigned vo[4];
#pragma unroll
    for (int g = 0; g < 4; ++g) vo[g] = (lane >> 5) * 512 + (((lane & 31) ^ (((2 * g + (lane >> 5)) & 7) << 1)) << 4);
    constexpr int NOLEAD = -LV - 1;
    int pub = 0;
    const unsigned t0 = (unsigned)__builtin_amdgcn_s_memrealtime() - (qd == 3 ? Q3LEAD : 0);
    const long vdelta = (const char*)Vc - (const char*)Kc - (32 - 4 * NB) * 16384;
    int half = 4096;
#pragma unroll 1
    for (int n = 0; n < 64; ++n) {
      TL(n);
      if (n == PREH) WG_BARRIER();
      const char* src = kb + (size_t)n * 16384 + (n < 32 ? 0 : vdelta);
      if (n >= NA) {
        const int m = n - NA;
        src = vbase + (size_t)((m / NB) * 1024 + (m % NB) * 128 + wave * 64) * 512;
        if (m == 0) {
          STAMP(1);
          half = 16384;
#pragma unroll
          for (int g = 0; g < 4; ++g) vo[g] = (8 * g + (lane >> 3)) * 512 + (((lane & 7) ^ (((lane >> 3) & 2) | ((g & 1) << 2))) << 4) - g * 1024;
        }
      }
      int ld = NOLEAD;
      if (n >= 8) {
        const unsigned an = __builtin_amdgcn_readfirstlane(*(volatile LAS unsigned*)&L->anchor);
        ld = (int)((an ? an + (n - 26) * TAU2 / 10 : t0 + n * TAU / 10) - (unsigned)__builtin_amdgcn_s_memrealtime());
        if (!an && ld <= 0) ld = NOLEAD;
        volatile LAS unsigned* fr = &L->freed[wave][n & 7];
#pragma unroll 1
        for (unsigned spins = 0; spins < (1u << 22); ++spins) {
          if ((int)__builtin_amdgcn_readfirstlane(*fr) >= n - 7) break;
          DRAIN();
          pub = n; *full = pub;
          __builtin_amdgcn_s_sleep(1);
        }
        asm volatile("" ::: "memory");
      }
      unsigned char* dst = sm.ring[wave][n & 7];
      if (ld > 0) { if (ld > 50) issue8<8>(src, half, dst, vo, n, pub, full); else issue8<10>(src, half, dst, vo, n, pub, full); }
      else { if (ld > -60 && n < 54) issue8<13>(src, half, dst, vo, n, pub, full); else issue8<20>(src, half, dst, vo, n, pub, full); }
    }
#pragma unroll
    for (int k = 2; k >= 0; --k) {
      asm volatile("s_waitcnt vmcnt(%0)" ::"i"(8 * k) : "memory");
      *full = max(pub, 64 - k);
    }
    STAMP(2);
    WG_BARRIER();
  } else {
    const GAS unsigned* x32 = (const GAS unsigned*)(x + b * DM);
    const int wv = wave - 2;
    const u64 ep = *(const u64*)(ws + WS_EPOCH + b * 256);
    {
      const int r = lane >> 3;
      const GAS char* wqp = (const GAS char*)wq + (size_t)(2 * r) * (QLD * 2) + (j * 64 + (lane & 7) * 8) * 2;
      v4u wr[10]; unsigned xp[5];
#pragma unroll
      for (int k = 0; k < 5; ++k) {
        const int pg = min(wv + 6 * k, 27);
        xp[k] = x32[8 * pg + r];
        wr[2 * k] = ldw(wqp + (size_t)(16 * pg) * (QLD * 2));
        wr[2 * k + 1] = ldw(wqp + (size_t)(16 * pg + 1) * (QLD * 2));
      }
      if (wave == 2) clear_slots(rs, gid, lane);
      __builtin_amdgcn_sched_barrier(0);
      __builtin_amdgcn_s_barrier();
      asm volatile("" ::: "memory");
      __builtin_amdgcn_sched_barrier(0);
      if (wv >= 4) xp[4] = 0u;
      STAMP(1);
      float qa[8];
#pragma unroll
      for (int c = 0; c < 8; ++c) qa[c] = 0.f;
#pragma unroll
      for (int k = 0; k < 5; ++k) pair_dot(wr[2 * k], wr[2 * k + 1], xp[k], qa);
      L->qpart[wv][(lane & 7) * 8 + r] = tr8(qa, lane);
    }
    const u64 tag = (ep + 1) << 16;
    LGKM0();
    unsigned arr = 0;
    if (lane == 0) arr = __hip_atomic_fetch_add(&L->cnt, 1u, __ATOMIC_RELAXED, __HIP_MEMORY_SCOPE_WORKGROUP);
    if (__builtin_amdgcn_readfirstlane(arr) == 5) {
      asm volatile("" ::: "memory");
      float qv = L->qpart[0][lane];
#pragma unroll
      for (int k = 1; k < 6; ++k) qv += L->qpart[k][lane];
      __hip_atomic_store((GAS u64*)(ws + WS_QG) + gid * 64 + lane, tag | (pkbf(qv, 0.f) & 0xffffu), RLX_AGENT);
    }
    STAMP(2);
    if (wave >= 4) {
      const int cw = wave - 4, w = cw & 1, par = cw >> 1;
      volatile LAS unsigned* full = &L->full[w];
      volatile LAS unsigned* fr = L->freed[w];
      const int g4 = lane >> 4, c16 = lane & 15;
      const unsigned ka0 = c16 * 512 + ((g4 ^ ((c16 & 7) << 1)) << 4);
      const unsigned ta0 = (8 * g4 + (c16 >> 2)) * 128 + ((((c16 >> 3) & 1) | ((g4 & 1) << 1)) << 5) + (lane & 3) * 8;
      {
        bf16x8 qf[8];
#pragma unroll 1
        for (int gen = 0; gen < 4; ++gen) {
          bf16x8 kf[4][8];
#pragma unroll
          for (int h = 0; h < 4; ++h) {
            const int n = 8 * gen + 2 * h + par;
            wait_ge(full, n + 1);
            const LAS unsigned char* cell = L->ring[w][2 * h + par];
#pragma unroll
            for (int t = 0; t < 8; ++t) kf[h][t] = *(const LAS bf16x8*)(cell + ((ka0 ^ ((t & 3) << 6)) + (t >> 2) * 256));
            LGKM0(); fr[2 * h + par] = n + 1;
            PIN8(kf[h]);
          }
          if (gen == 0) {
            wait_ge(&L->qready, 1);
            STAMP(3);
#pragma unroll
            for (int t = 0; t < 8; ++t) qf[t] = *(const LAS bf16x8*)(L->q + (lane & 1) * 128 + (4 * t + g4) * 4);
          }
          f32x4 sc[4];
#pragma unroll
          for (int h = 0; h < 4; ++h) sc[h] = f32x4{0.f, 0.f, 0.f, 0.f};
#pragma unroll
          for (int t = 0; t < 8; ++t)
#pragma unroll
            for (int h = 0; h < 4; ++h) sc[h] = __builtin_amdgcn_mfma_f32_16x16x32_bf16(kf[h][t], qf[t], sc[h], 0, 0, 0);
          const f32x4 sv = sel4(sc[0], sc[1], sc[2], sc[3], c16 & 2, c16 & 4);
          *(LAS v2u*)&L->P[gen][c16 & 1][(2 * (c16 & 6) + 2 * par + w) * 8 + 2 * g4] = v2u{pkbf(sv[0], sv[1]), pkbf(sv[2], sv[3])};
          LGKM0(); *(volatile LAS unsigned*)&L->kdone[cw] = gen + 1;
        }
      }
      STAMP(4);
      v4u wf[32];
      const GAS u16* wop = (const GAS u16*)wo + (size_t)(2 * kvh * 256 + 64 * qd + 8 * g4) * DM + min(128 * cw + 8 * c16, DM - 8);
      f32x4 acc0 = {0.f, 0.f, 0.f, 0.f}, acc1 = {0.f, 0.f, 0.f, 0.f};
      float mprev0 = -INFINITY, mprev1 = -INFINITY;
      const int vrow = 4 * g4 + (c16 >> 2);
      const unsigned va0 = vrow * 512 + ((vrow & 7) << 5) + (lane & 3) * 8;
      auto astep = [&](int aa) {
        const int n = 32 - 4 * NB + 2 * aa + par, gen = aa >> 2;
        if ((aa & 3) == 0 || aa == 2 * NB) {
          wait_ge(&L->pready, gen + 1);
          const float m0 = L->mref[gen][0], m1 = L->mref[gen][1];
          const float a0 = __builtin_amdgcn_exp2f(mprev0 - m0), a1 = __builtin_amdgcn_exp2f(mprev1 - m1);
          mprev0 = m0; mprev1 = m1;
          acc0 *= f32x4{a0, a1, a0, a1}; acc1 *= f32x4{a0, a1, a0, a1};
        }
        const v2u pv = *(const LAS v2u*)&L->P[gen][lane & 1][(4 * (aa & 3) + 2 * par + w) * 8 + 2 * g4];
        s16x4 pa[8];
#pragma unroll
        for (int v = 0; v < 8; ++v) { const bool on = (c16 >> 1) == v; pa[v] = __builtin_bit_cast(s16x4, v2u{on ? pv[0] : 0u, on ? pv[1] : 0u}); }
        PIN8(pa);
        wait_ge(full, n + 1);
        const LAS unsigned char* cell = L->ring[w][n & 7];
#pragma unroll
        for (int v = 0; v < 8; ++v) {
          acc0 = __builtin_amdgcn_mfma_f32_16x16x16bf16_1k(pa[v], __builtin_amdgcn_ds_read_tr16_b64_v4i16((LAS s16x4*)(cell + (va0 ^ (v << 5)))), acc0, 0, 0, 0);
          acc1 = __builtin_amdgcn_mfma_f32_16x16x16bf16_1k(pa[v], __builtin_amdgcn_ds_read_tr16_b64_v4i16((LAS s16x4*)(cell + ((va0 ^ (v << 5)) + 256))), acc1, 0, 0, 0);
        }
        asm volatile("" : "+v"(acc0), "+v"(acc1));
        LGKM0(); fr[n & 7] = n + 1;
      };
      constexpr int NWS = (32 + WOB - 1) / WOB;
#pragma unroll
      for (int hb = 0; hb < NWS; ++hb) {
#pragma unroll
        for (int i = WOB * hb; i < WOB * hb + WOB && i < 32; ++i) wf[i] = ldw(wop + (size_t)((i >> 4) * 256 + ((i >> 3) & 1) * 32 + (i & 7)) * DM);
        __builtin_amdgcn_sched_barrier(0);
        astep(2 * NB + hb);
      }
#pragma unroll 1
      for (int aa = 2 * NB + NWS; aa < 16; ++aa) astep(aa);
#pragma unroll
      for (int r = 0; r < 4; ++r) {
        L->mrg[cw][(r & 1) * 256 + (2 * g4 + (r >> 1)) * 16 + c16] = acc0[r];
        L->mrg[cw][(r & 1) * 256 + 128 + (2 * g4 + (r >> 1)) * 16 + c16] = acc1[r];
      }
      LGKM0();
      if (lane == 0) __hip_atomic_fetch_add(&L->acnt, 1u, __ATOMIC_RELAXED, __HIP_MEMORY_SCOPE_WORKGROUP);
      STAMP(7);
      __builtin_amdgcn_sched_barrier(0);
#pragma unroll
      for (int kb = 0; kb < 4; ++kb) {
        v4u tf[8];
#pragma unroll
        for (int e = 0; e < 8; ++e)
#pragma unroll
          for (int d = 0; d < 4; ++d)
            tf[e][d] = (e & 1) ? perm_hi(wf[8 * kb + 2 * d][e >> 1], wf[8 * kb + 2 * d + 1][e >> 1]) : perm_lo(wf[8 * kb + 2 * d][e >> 1], wf[8 * kb + 2 * d + 1][e >> 1]);
        PIN8(tf);
#pragma unroll
        for (int e = 0; e < 8; ++e) wf[8 * kb + e] = tf[e];
      }
      f32x4 acc[4];
#pragma unroll
      for (int t = 0; t < 4; ++t) acc[t] = f32x4{0.f, 0.f, 0.f, 0.f};
      f32x4 o = {0.f, 0.f, 0.f, 0.f};
      wait_ge(&L->pready, 5);
      LAS v2u* obw = (LAS v2u*)&L->ob[cw][(c16 & 1) * 32 + ((c16 >> 1) & 3) * 8 + 2 * g4];
#pragma unroll 1
      for (int mm = 0; mm < 2 * NB; ++mm) {
        const int m = 2 * mm + par, n = NA + m;

        const LAS unsigned* pp = &L->PB[m / NB][lane & 1][(m % NB) * 64 + w * 32 + 4 * g4];
        const bf16x8 p0 = *(const LAS bf16x8*)pp, p1 = *(const LAS bf16x8*)(pp + 16);
        wait_ge(full, n + 1);
        bf16x8 vf[8];
        read_tr(L->ring[w][n & 7], ta0, vf);
#pragma unroll
        for (int t = 0; t < 4; ++t) acc[t] = __builtin_amdgcn_mfma_f32_16x16x32_bf16(vf[t], p0, acc[t], 0, 0, 0);
#pragma unroll
        for (int t = 0; t < 4; ++t) acc[t] = __builtin_amdgcn_mfma_f32_16x16x32_bf16(vf[4 + t], p1, acc[t], 0, 0, 0);
        asm volatile("" : "+v"(acc[0]), "+v"(acc[1]), "+v"(acc[2]), "+v"(acc[3]));
      }
      STAMP(5);
      if (cw == 0) {
        wait_ge(&L->oready, 1);
        if ((c16 & 1) == kh) o = *(const LAS f32x4*)&L->oa[((c16 >> 1) & 3) * 16 + 4 * g4];
      }
      o += sel4(acc[0], acc[1], acc[2], acc[3], c16 & 2, c16 & 4);
      *obw = v2u{pkbf(o[0], o[1]), pkbf(o[2], o[3])};
      const int col = 128 * cw + 8 * c16 + 4 * (g4 & 1);
      const bool st_on = g4 < 2 && col < DM;
      const int yoff = st_on ? WS_Y + gid * (DM * 4) + col * 4 : WS_END;
      const LAS float* ybp = &L->yb[st_on ? col : 0];
      const LAS unsigned* obr = &L->ob[c16 & 3][4 * g4];
      WG_BARRIER();
      STAMP(6);
      bf16x8 of[4];
#pragma unroll
      for (int kb = 0; kb < 4; ++kb) of[kb] = *(const LAS bf16x8*)(obr + 16 * kb);
      const f32x4 ybv = *(const LAS f32x4*)ybp;
      const float g = L->gate;
      __builtin_amdgcn_sched_barrier(0);
      f32x4 ya[8];
#pragma unroll
      for (int e = 0; e < 8; ++e) ya[e] = f32x4{0.f, 0.f, 0.f, 0.f};
#pragma unroll
      for (int kb = 0; kb < 4; ++kb) {
#pragma unroll
        for (int e = 0; e < 8; ++e) ya[e] = __builtin_amdgcn_mfma_f32_16x16x32_bf16(of[kb], __builtin_bit_cast(bf16x8, wf[8 * kb + e]), ya[e], 0, 0, 0);
        __builtin_amdgcn_sched_barrier(0);
      }
      v4u yv;
#pragma unroll
      for (int r = 0; r < 4; ++r) {
        const float lo = (ya[r][0] + ya[r][1]) + (ya[r][2] + ya[r][3]), hi = (ya[4 + r][0] + ya[4 + r][1]) + (ya[4 + r][2] + ya[4 + r][3]);
        yv[r] = san(fmaf(g, (g4 & 1) ? hi : lo, ybv[r]));
      }
      __builtin_amdgcn_raw_buffer_store_b128(yv, rs, yoff, 0, 16);
      STAMP(8);
    } else {
      if (wave == 2) {
        {
          const unsigned thi = (unsigned)(tag >> 32), tlo = (unsigned)tag;
          int off[4];
#pragma unroll
          for (int k = 0; k < 4; ++k) off[k] = WS_QG + (gid & ~7) * 512 + k * 1024 + lane * 16;
          __builtin_amdgcn_s_sleep(80);
#pragma unroll 1
          for (unsigned spins = 0; spins < SPIN_CAP; ++spins) {
            v4u g[4];
            bool ok = true;
#pragma unroll
            for (int k = 0; k < 4; ++k) g[k] = __builtin_amdgcn_raw_buffer_load_b128(rs, off[k], 0, 16);
#pragma unroll
            for (int k = 0; k < 4; ++k) {
              if (off[k] != WS_END && g[k][1] == thi && g[k][3] == thi && (g[k][0] & 0xffff0000u) == tlo && (g[k][2] & 0xffff0000u) == tlo) {
                L->q[k * 64 + lane] = perm_lo(g[k][0], g[k][2]);
                off[k] = WS_END;
              }
              ok &= off[k] == WS_END;
            }
            if (__all(ok)) break;
            __builtin_amdgcn_s_sleep(16);
          }
          LGKM0();
          *(volatile LAS unsigned*)&L->qready = 1u;
        }
        STAMP(3);
        constexpr float C2 = 0.0625f * 1.4426950408889634f;
        float mrun = -INFINITY, lrun = 0.f;
#pragma unroll 1
        for (int gen = 0; gen < 4; ++gen) {
#pragma unroll
          for (int c = 0; c < 4; ++c) wait_ge(&L->kdone[c], gen + 1);
          v4u g = *(const LAS v4u*)&L->P[gen][0][lane * 4];
          if (gen == 0) {
            __builtin_amdgcn_raw_buffer_store_b128(g, rs, WS_S + gid * 1024 + lane * 16, 0, 16);
          }
          if (gen == 2) {
            DRAIN();
            if (lane == 0) __hip_atomic_store((GAS u64*)(ws + WS_RDY) + gid, tag | ((unsigned)__builtin_amdgcn_s_memrealtime() & 0xffffu), RLX_AGENT);
          }
          float cm = -INFINITY;
#pragma unroll
          for (int e = 0; e < 4; ++e) cm = fmaxf(cm, fmaxf(bflo(g[e]), bfhi(g[e])));
          const float mn = fmaxf(mrun, max32(cm) * C2);
          float ls = 0.f;
#pragma unroll
          for (int e = 0; e < 4; ++e) {
            const float pl = __builtin_amdgcn_exp2f(fmaf(bflo(g[e]), C2, -mn)), ph = __builtin_amdgcn_exp2f(fmaf(bfhi(g[e]), C2, -mn));
            ls += pl + ph;
            g[e] = pkbf(pl, ph);
          }
          *(LAS v4u*)&L->P[gen][0][lane * 4] = g;
          lrun = fmaf(lrun, __builtin_amdgcn_exp2f(mrun - mn), ls);
          mrun = mn;
          if ((lane & 31) == 0) L->mref[gen][lane >> 5] = mn;
          LGKM0();
          *(volatile LAS unsigned*)&L->pready = gen + 1;
        }
        lrun += dpp<XOR1>(lrun); lrun += dpp<XOR2>(lrun); lrun += dpp<ROR4>(lrun); lrun += dpp<XOR8>(lrun); lrun = swp16_add(lrun, lrun);
        if ((lane & 31) == 0) __builtin_amdgcn_raw_buffer_store_b64(v2u{san(mrun), san(lrun)}, rs, WS_ST + gid * 128 + (lane >> 5) * 8, 0, 16);
        u64 gv;
#pragma unroll 1
        for (unsigned spins = 0; spins < SPIN_CAP; ++spins) {
          gv = __hip_atomic_load((GAS u64*)(ws + WS_RDY) + b * 64 + lane, RLX_AGENT);
          if (__all((gv & ~0xffffull) == tag)) break;
          __builtin_amdgcn_s_sleep(32);
        }
        *(volatile LAS unsigned*)&L->anchor = (mean_time((unsigned)gv) + TOFF2 - (qd == 3 ? Q3LEAD : 0)) | 1u;
        if (j == 0 && lane == 0) __hip_atomic_store((GAS u64*)(ws + WS_EPOCH + b * 256), ep + 1, RLX_AGENT);
        asm volatile("" ::: "memory");
        v4u h[4];
#pragma unroll
        for (int k = 0; k < 4; ++k) h[k] = __builtin_amdgcn_raw_buffer_load_b128(rs, WS_S + ((gid & ~3) + k) * 1024 + lane * 16, 0, 16);
        v2u sv;
#pragma unroll 1
        for (unsigned spins = 0; spins < SPIN_CAP; ++spins) {
          __builtin_amdgcn_s_sleep(127);
          sv = __builtin_amdgcn_raw_buffer_load_b64(rs, WS_ST + ((gid & ~7) + (lane & 7)) * 128 + (lane >> 5) * 8, 0, 16);
          if (__all(sv[0] != SENT && sv[1] != SENT)) break;
        }
        const float mp = __uint_as_float(sv[0]);
        float M = fmaxf(mp, dpp<XOR1>(mp)); M = fmaxf(M, dpp<XOR2>(M)); M = fmaxf(M, dpp<XOR7>(M));
        float Ls = __uint_as_float(sv[1]) * __builtin_amdgcn_exp2f(mp - M);
        Ls += dpp<XOR1>(Ls); Ls += dpp<XOR2>(Ls); Ls += dpp<XOR7>(Ls);
        const float inv = 1.f / Ls;
#pragma unroll
        for (int k = 0; k < 4; ++k) {
#pragma unroll
          for (int e = 0; e < 4; ++e)
            h[k][e] = pkbf(inv * __builtin_amdgcn_exp2f(fmaf(bflo(h[k][e]), C2, -M)), inv * __builtin_amdgcn_exp2f(fmaf(bfhi(h[k][e]), C2, -M)));
          *(LAS v4u*)&L->PB[k][0][lane * 4] = h[k];
        }
        LGKM0();
        *(volatile LAS unsigned*)&L->pready = 5u;
        const float as = inv * __builtin_amdgcn_exp2f(mrun - M);
        STAMP(4);
        wait_ge(&L->acnt, 4);
        f32x4 o0 = *(const LAS f32x4*)(L->mrg[0] + lane * 8), o1 = *(const LAS f32x4*)(L->mrg[0] + lane * 8 + 4);
#pragma unroll
        for (int ww = 1; ww < 4; ++ww) { o0 += *(const LAS f32x4*)(L->mrg[ww] + lane * 8); o1 += *(const LAS f32x4*)(L->mrg[ww] + lane * 8 + 4); }
        o0 *= as; o1 *= as;
        v4u ov = {pkbf(o0[0], o0[1]), pkbf(o0[2], o0[3]), pkbf(o1[0], o1[1]), pkbf(o1[2], o1[3])};
#pragma unroll
        for (int k = 0; k < 4; ++k) ov[k] = ov[k] == SENT ? 0x7FC07FC0u : ov[k];
        __builtin_amdgcn_raw_buffer_store_b128(ov, rs, WS_PO + gid * 1024 + lane * 16, 0, 16);
        STAMP(5);
        int off = WS_PO + ((gid & ~7) + (lane >> 3)) * 1024 + s * 128 + (lane & 7) * 16;
        __builtin_amdgcn_s_sleep(20);
#pragma unroll 1
        for (unsigned spins = 0; spins < SPIN_CAP; ++spins) {
          if (off != WS_END) {
            ov = __builtin_amdgcn_raw_buffer_load_b128(rs, off, 0, 16);
            if (max(max(ov[0], ov[1]), max(ov[2], ov[3])) != SENT) off = WS_END;
          }
          if (__all(off == WS_END)) break;
          __builtin_amdgcn_s_sleep(2);
        }
        STAMP(6);
        float oe[8];
#pragma unroll
        for (int k = 0; k < 4; ++k) { oe[2 * k] = bflo(ov[k]); oe[2 * k + 1] = bfhi(ov[k]); }
        L->oa[8 * (lane & 7) + (lane >> 3)] = tr8(oe, lane);
        LGKM0();
        *(volatile LAS unsigned*)&L->oready = 1u;
        STAMP(7);
      } else {
        wait_ge(&L->qready, 1);
        const int cl = lane < 56 ? lane : 55;
        float z = 0.f;
#pragma unroll
        for (int k = 0; k < 4; ++k) {
          const int idx = lane + 64 * k;
          const bool in = idx < DM / 2;
          const unsigned xv = x32[in ? idx : 0], gv = ((const GAS unsigned*)gw)[in ? idx : 0];
          const float t = rbf(bflo(xv) * bflo(gv)) + rbf(bfhi(xv) * bfhi(gv));
          z += in ? t : 0.f;
        }
        z = swp32_add(z, z); z = swp16_add(z, z); z += dpp<XOR8>(z); z += dpp<XOR7>(z); z += dpp<XOR2>(z); z += dpp<XOR1>(z);
        const float g = rbf(1.f / (1.f + __expf(-rbf(z))));
        if (lane == 0) L->gate = g;
        float yb[8];
#pragma unroll
        for (int c = 0; c < 8; ++c) yb[c] = 0.f;
        if (s != 3) {
          const int u0 = (s == 7 ? 48 + kvh : kvh * 6 + 2 * qd + kh) * 32, rr = lane >> 2;
          float ha[8];
#pragma unroll
          for (int c = 0; c < 8; ++c) ha[c] = 0.f;
          const GAS u16* w1p = (const GAS u16*)w1 + (size_t)(2 * rr) * DFF + u0 + (lane & 3) * 8;
#pragma unroll 1
          for (int h = 0; h < 14 / MB1; ++h) {
            v4u ma[2 * MB1]; unsigned xq[MB1];
#pragma unroll
            for (int k = 0; k < MB1; ++k) {
              ma[2 * k] = ldw(w1p + (size_t)((MB1 * h + k) * 32) * DFF);
              ma[2 * k + 1] = ldw(w1p + (size_t)((MB1 * h + k) * 32 + 1) * DFF);
              xq[k] = x32[(MB1 * h + k) * 16 + rr];
            }
#pragma unroll
            for (int k = 0; k < MB1; ++k) pair_dot(ma[2 * k], ma[2 * k + 1], xq[k], ha);
          }
          float hv = tr8(ha, lane);
          hv += dpp<SHR4>(hv);
          hv = fmaxf(rbf(hv), 0.f);
#pragma unroll 1
          for (int c4 = 0; c4 < 32 / MB2; ++c4) {
            v4u w2r[MB2];
#pragma unroll
            for (int u = 0; u < MB2; ++u) w2r[u] = ldw((const GAS u16*)w2 + (size_t)(u0 + c4 * MB2 + u) * DM + cl * 8);
#pragma unroll
            for (int u = 0; u < MB2; ++u) {
              const int un = c4 * MB2 + u;
              const float hu = __builtin_bit_cast(float, __builtin_amdgcn_readlane(__builtin_bit_cast(int, hv), (un & 7) * 8 + 4 + (un >> 3)));
#pragma unroll
              for (int c = 0; c < 4; ++c) { yb[2 * c] = fmaf(hu, bflo(w2r[u][c]), yb[2 * c]); yb[2 * c + 1] = fmaf(hu, bfhi(w2r[u][c]), yb[2 * c + 1]); }
            }
          }
        }
        const float omg = rbf(1.f - g);
        if (lane < 56) {
          *(LAS f32x4*)(L->yb + lane * 8) = f32x4{omg * yb[0], omg * yb[1], omg * yb[2], omg * yb[3]};
          *(LAS f32x4*)(L->yb + lane * 8 + 4) = f32x4{omg * yb[4], omg * yb[5], omg * yb[6], omg * yb[7]};
        }
      }
      WG_BARRIER();
    }
  }
  if (j < 14) {
    const v4u y = poll16(srd, WS_Y + (b * 64 + wave * 8 + (lane >> 3)) * (DM * 4) + j * 128 + (lane & 7) * 16);
    STAMP(9);
    const float r0 = swp32_add(__uint_as_float(y[0]), __uint_as_float(y[2])), r1 = swp32_add(__uint_as_float(y[1]), __uint_as_float(y[3]));
    float r = swp16_add(r0, r1);
    r += dpp<XOR8>(r);
    if (!(lane & 8)) L->fin[wave][(lane & 7) * 4 + (lane >> 4)] = r;
    WG_BARRIER();
    if (tid < 32) {
      const float t = ((L->fin[0][tid] + L->fin[1][tid]) + (L->fin[2][tid] + L->fin[3][tid])) + ((L->fin[4][tid] + L->fin[5][tid]) + (L->fin[6][tid] + L->fin[7][tid]));
      out[b * DM + j * 32 + tid] = (u16)pkbf(t, 0.f);
    }
  }
  STAMP_OUT();
  DRAIN();
}

extern "C" void kernel_launch(void* const* d_in, const int* in_sizes, int n_in, void* d_out, int out_size, void* d_ws, size_t ws_size, hipStream_t stream) {
  fused_kernel<<<256, 512, 0, stream>>>((const u16*)d_in[0], (const u16*)d_in[1], (const u16*)d_in[2], (const u16*)d_in[3], (const u16*)d_in[4],
                                        (const u16*)d_in[6], (const u16*)d_in[7], (const u16*)d_in[8], (unsigned char*)d_ws, (u16*)d_out);
}
```

```cpp
#include <hip/hip_runtime.h>
#include <hip/hip_bf16.h>
#include <cstdint>

#define NB 2
#define PREH 1
#define WOB 4
#define MB1 1
#define MB2 4
#define TAU 590
#define TAU1 580
#define TOFF1 -330
#define LAG1 40
#define NTS 8
#define RC 0
#define TAU2 556
#define TOFF2 30
#define Q3LEAD 60
#define LV 0
#define STAMPS 0

typedef unsigned v4u __attribute__((ext_vector_type(4)));
typedef unsigned v3u __attribute__((ext_vector_type(3)));
typedef unsigned v2u __attribute__((ext_vector_type(2)));
typedef float f32x4 __attribute__((ext_vector_type(4)));
typedef __bf16 bf16x2 __attribute__((ext_vector_type(2)));
typedef __bf16 bf16x8 __attribute__((ext_vector_type(8)));
typedef short s16x4 __attribute__((ext_vector_type(4)));
typedef unsigned short u16;
typedef unsigned long long u64;
#define GAS __attribute__((address_space(1)))
#define LAS __attribute__((address_space(3)))
#define RLX_AGENT __ATOMIC_RELAXED, __HIP_MEMORY_SCOPE_AGENT

constexpr int DM = 448, SK = 8192, DH = 256, DFF = 1792, QLD = 4096;
constexpr unsigned SENT = 0xFFFFFFFFu;
constexpr unsigned SPIN_CAP = 1u << 18;
constexpr int WS_EPOCH = 0;
constexpr int WS_RDY = 4096;
constexpr int WS_TS = 6144;
constexpr int WS_QG = 8192;
constexpr int WS_S = WS_QG + 256 * 64 * 8;
constexpr int WS_ST = WS_S + 256 * 1024;
constexpr int WS_PO = WS_ST + 256 * 128;
constexpr int WS_Y = WS_PO + 256 * 1024;
constexpr int WS_END = WS_Y + 256 * DM * 4;
constexpr size_t WS_STAMP = 16u << 20;

__device__ __forceinline__ float bflo(unsigned u) { return __builtin_bit_cast(float, u << 16); }
__device__ __forceinline__ float bfhi(unsigned u) { return __builtin_bit_cast(float, u & 0xffff0000u); }
__device__ __forceinline__ unsigned pkbf(float lo, float hi) {
  bf16x2 r; r[0] = (__bf16)lo; r[1] = (__bf16)hi; return __builtin_bit_cast(unsigned, r);
}
__device__ __forceinline__ float rbf(float f) { return bflo(pkbf(f, 0.f)); }
__device__ __forceinline__ float dot2(unsigned a, unsigned b, float acc) {
  return __builtin_amdgcn_fdot2_f32_bf16(__builtin_bit_cast(bf16x2, a), __builtin_bit_cast(bf16x2, b), acc, false);
}
__device__ __forceinline__ unsigned perm_lo(unsigned a, unsigned b) { return __builtin_amdgcn_perm(b, a, 0x05040100u); }
__device__ __forceinline__ unsigned perm_hi(unsigned a, unsigned b) { return __builtin_amdgcn_perm(b, a, 0x07060302u); }
template <int CTRL> __device__ __forceinline__ float dpp(float x) {
  return __builtin_bit_cast(float, __builtin_amdgcn_mov_dpp(__builtin_bit_cast(int, x), CTRL, 0xf, 0xf, true));
}
constexpr int XOR1 = 0xB1, XOR2 = 0x4E, XOR7 = 0x141, XOR8 = 0x128, ROR4 = 0x124, SHR4 = 0x114;
__device__ __forceinline__ float swp16_add(float a, float b) {
  auto s = __builtin_amdgcn_permlane16_swap(__float_as_uint(a), __float_as_uint(b), false, false);
  return __uint_as_float(s[0]) + __uint_as_float(s[1]);
}
__device__ __forceinline__ float swp32_add(float a, float b) {
  auto s = __builtin_amdgcn_permlane32_swap(__float_as_uint(a), __float_as_uint(b), false, false);
  return __uint_as_float(s[0]) + __uint_as_float(s[1]);
}
__device__ __forceinline__ float swp16_max(float a) {
  auto s = __builtin_amdgcn_permlane16_swap(__float_as_uint(a), __float_as_uint(a), false, false);
  return fmaxf(__uint_as_float(s[0]), __uint_as_float(s[1]));
}
__device__ __forceinline__ float swp32_max(float a) {
  auto s = __builtin_amdgcn_permlane32_swap(__float_as_uint(a), __float_as_uint(a), false, false);
  return fmaxf(__uint_as_float(s[0]), __uint_as_float(s[1]));
}
template <int CTRL> __device__ __forceinline__ float tr_dpp(float lo, float hi, bool sel) {
  const float t1 = lo + dpp<CTRL>(lo), t2 = hi + dpp<CTRL>(hi);
  return sel ? t2 : t1;
}
__device__ __forceinline__ float tr8(float (&a)[8], int lane) {
#pragma unroll
  for (int i = 0; i < 4; ++i) a[i] = swp32_add(a[i], a[i + 4]);
#pragma unroll
  for (int i = 0; i < 2; ++i) a[i] = swp16_add(a[i], a[i + 2]);
  return tr_dpp<XOR8>(a[0], a[1], lane & 8);
}
__device__ __forceinline__ float max32(float v) {
  v = fmaxf(v, dpp<XOR1>(v)); v = fmaxf(v, dpp<XOR2>(v)); v = fmaxf(v, dpp<ROR4>(v)); v = fmaxf(v, dpp<XOR8>(v));
  return swp16_max(v);
}
__device__ __forceinline__ void pair_dot(const v4u& ra, const v4u& rb, unsigned xp, float (&a)[8]) {
#pragma unroll
  for (int c = 0; c < 4; ++c) {
    a[2 * c] = dot2(perm_lo(ra[c], rb[c]), xp, a[2 * c]);
    a[2 * c + 1] = dot2(perm_hi(ra[c], rb[c]), xp, a[2 * c + 1]);
  }
}
__device__ __forceinline__ unsigned san(float f) { const unsigned u = __float_as_uint(f); return u == SENT ? 0x7FC00000u : u; }
__device__ __forceinline__ v4u ldw(const GAS void* p) {
  return *(const GAS v4u*)p;
}
#define WG_BARRIER() do { asm volatile("s_waitcnt lgkmcnt(0)" ::: "memory"); __builtin_amdgcn_s_barrier(); asm volatile("" ::: "memory"); __builtin_amdgcn_sched_barrier(0); } while (0)
#define DRAIN() asm volatile("s_waitcnt vmcnt(0)" ::: "memory")
#define STAMP(i) do {} while (0)
#define STAMP_OUT() do {} while (0)
#define TL(n) do {} while (0)

struct Lds {
  unsigned char ring[2][8][8192];
  unsigned P[4][2][128];
  unsigned PB[4][2][128];
  float mrg[4][512];
  float oa[64];
  unsigned q[256];
  float yb[DM];
  float qpart[6][64];
  float fin[8][32];
  unsigned ob[4][68];
  float mref[4][2];
  float gate, pad0[3];
  unsigned freed[2][8];
  unsigned full[2];
  unsigned cnt, qready;
  unsigned kdone[4];
  unsigned pready, acnt, oready, pad1;
  unsigned anchor[2];
};

#define PIN16(a) asm volatile("" : "+v"(a[0]), "+v"(a[1]), "+v"(a[2]), "+v"(a[3]), "+v"(a[4]), "+v"(a[5]), "+v"(a[6]), "+v"(a[7]), \
                                   "+v"(a[8]), "+v"(a[9]), "+v"(a[10]), "+v"(a[11]), "+v"(a[12]), "+v"(a[13]), "+v"(a[14]), "+v"(a[15]))
#define PIN8(a) asm volatile("" : "+v"(a[0]), "+v"(a[1]), "+v"(a[2]), "+v"(a[3]), "+v"(a[4]), "+v"(a[5]), "+v"(a[6]), "+v"(a[7]))
#define LGKM0() asm volatile("s_waitcnt lgkmcnt(0)" ::: "memory")

__device__ __forceinline__ void wait_ge(volatile LAS unsigned* w, unsigned need) {
#pragma unroll 1
  for (unsigned spins = 0; spins < (1u << 22); ++spins) {
    if ((unsigned)__builtin_amdgcn_readfirstlane(*w) >= need) break;
    __builtin_amdgcn_s_sleep(1);
  }
  asm volatile("" ::: "memory");
}

#define PTHR 64
#define PSLOW 10
#define PSLEEP 2
__device__ __forceinline__ v4u poll16(const v4u& srd, int off) {
  unsigned c0, c1, c2, c3, mx, cnt;
  asm volatile(
      "v_mov_b32 v252, -1\n\tv_mov_b32 v253, -1\n\tv_mov_b32 v254, -1\n\tv_mov_b32 v255, -1\n\t"
      "s_mov_b32 %5, 0x200000\n\t"
      "s_nop 1\n"
      "1:\n\t"
      "buffer_load_dwordx4 v[252:255], %6, %7, 0 offen sc1\n\t"
      "s_sleep %8\n\t"
      "s_waitcnt vmcnt(12)\n\t"
      "v_mov_b32 %0, v252\n\tv_mov_b32 %1, v253\n\tv_mov_b32 %2, v254\n\tv_mov_b32 %3, v255\n\t"
      "v_max3_u32 %4, %0, %1, %2\n\t"
      "v_max_u32 %4, %4, %3\n\t"
      "v_cmp_eq_u32 vcc, -1, %4\n\t"
      "s_and_b64 exec, exec, vcc\n\t"
      "s_cbranch_scc0 2f\n\t"
      "s_bcnt1_i32_b64 vcc_lo, exec\n\t"
      "s_cmp_le_u32 vcc_lo, %9\n\t"
      "s_cbranch_scc1 3f\n\t"
      "s_sleep %10\n"
      "3:\n\t"
      "s_sub_u32 %5, %5, 1\n\t"
      "s_cbranch_scc0 1b\n"
      "2:\n\t"
      "s_mov_b64 exec, -1\n\t"
      "s_nop 4"
      : "=&v"(c0), "=&v"(c1), "=&v"(c2), "=&v"(c3), "=&v"(mx), "=&s"(cnt)
      : "v"(off), "s"(srd), "i"(PSLEEP), "i"(PTHR), "i"(PSLOW)
      : "vcc", "scc", "v252", "v253", "v254", "v255", "memory");
  return v4u{c0, c1, c2, c3};
}

__device__ __forceinline__ void clear_slots(__amdgpu_buffer_rsrc_t rs, int gid, int lane) {
  const v4u sv = {SENT, SENT, SENT, SENT};
  __builtin_amdgcn_raw_buffer_store_b128(sv, rs, WS_PO + gid * 1024 + lane * 16, 0, 16);
  __builtin_amdgcn_raw_buffer_store_b128(sv, rs, WS_Y + gid * (DM * 4) + lane * 16, 0, 16);
  __builtin_amdgcn_raw_buffer_store_b128(sv, rs, lane < 48 ? WS_Y + gid * (DM * 4) + 1024 + lane * 16 : (lane == 48 ? WS_ST + gid * 128 : WS_END), 0, 16);
}
template <int D>
__device__ __forceinline__ void issue8(const char* src, int half, unsigned char* dst, const unsigned (&voff)[4], int n, int& pub, volatile LAS unsigned* full) {
#pragma unroll
  for (int g = 0; g < 8; ++g) {
    asm volatile("s_waitcnt vmcnt(%0)" ::"i"(D - 1) : "memory");
    if (g == (D - 1) % 8) { pub = max(pub, n - (D - 1) / 8); *full = pub; }
    const char* sp = src + (g >> 2) * half;
    if ((g & 3) == 0) __builtin_amdgcn_global_load_lds((const unsigned*)(sp + voff[0]), (unsigned*)(dst + (g & 4) * 1024), 16, 0, 2);
    if ((g & 3) == 1) __builtin_amdgcn_global_load_lds((const unsigned*)(sp + voff[1]), (unsigned*)(dst + (g & 4) * 1024), 16, 1024, 2);
    if ((g & 3) == 2) __builtin_amdgcn_global_load_lds((const unsigned*)(sp + voff[2]), (unsigned*)(dst + (g & 4) * 1024), 16, 2048, 2);
    if ((g & 3) == 3) __builtin_amdgcn_global_load_lds((const unsigned*)(sp + voff[3]), (unsigned*)(dst + (g & 4) * 1024), 16, 3072, 2);
  }
}
__device__ __forceinline__ unsigned mean_time(unsigned stamp, bool ok) {
  const unsigned now = (unsigned)__builtin_amdgcn_s_memrealtime();
  float d = ok ? (float)((now - stamp) & 0xffffu) : 0.f;
  d = swp32_add(d, d); d = swp16_add(d, d); d += dpp<XOR8>(d); d += dpp<XOR7>(d); d += dpp<XOR2>(d); d += dpp<XOR1>(d);
  return now - (unsigned)(d / (float)max(__builtin_popcountll(__builtin_amdgcn_ballot_w64(ok)), 1));
}
__device__ __forceinline__ void read_tr(const LAS unsigned char* cell, unsigned a0, bf16x8* f) {
#pragma unroll
  for (int kb = 0; kb < 2; ++kb)
#pragma unroll
    for (int t = 0; t < 4; ++t) {
      const s16x4 lo = __builtin_amdgcn_ds_read_tr16_b64_v4i16((LAS s16x4*)(cell + ((a0 ^ (t << 5)) + kb * 4096)));
      const s16x4 hi = __builtin_amdgcn_ds_read_tr16_b64_v4i16((LAS s16x4*)(cell + ((a0 ^ (t << 5)) + kb * 4096 + 512)));
      f[kb * 4 + t] = __builtin_bit_cast(bf16x8, __builtin_shufflevector(lo, hi, 0, 1, 2, 3, 4, 5, 6, 7));
    }
}
__device__ __forceinline__ f32x4 sel4(f32x4 a0, f32x4 a1, f32x4 a2, f32x4 a3, bool b0, bool b1) {
  f32x4 r;
#pragma unroll
  for (int e = 0; e < 4; ++e) { const float lo = b0 ? a1[e] : a0[e], hi = b0 ? a3[e] : a2[e]; r[e] = b1 ? hi : lo; }
  return r;
}

__global__ void __launch_bounds__(512, 2) fused_kernel(const u16* __restrict__ x, const u16* __restrict__ Kc, const u16* __restrict__ Vc,
    const u16* __restrict__ wq, const u16* __restrict__ wo, const u16* __restrict__ w1, const u16* __restrict__ w2,
    const u16* __restrict__ gw, unsigned char* ws, u16* out) {
  __shared__ __attribute__((aligned(16))) Lds sm;
  LAS Lds* L = (LAS Lds*)&sm;
  asm volatile("" ::"s"(x), "s"(Kc), "s"(Vc), "s"(wq), "s"(wo), "s"(w1), "s"(w2), "s"(gw), "s"(ws), "s"(out));
  const int tid = threadIdx.x, lane = tid & 63, wave = __builtin_amdgcn_readfirstlane(tid >> 6);
  const int blk = blockIdx.x, b = blk >> 6;
#define GMAP 2
  const int kvh = ((blk >> 3) & 6) | (((blk >> 2) ^ (blk >> 4)) & 1), kh = (blk >> 3) & 1, qd = (blk + (blk >> 4)) & 3;
  const int s = kh * 4 + qd;
  const int j = kvh * 8 + s;
  const int gid = b * 64 + j;
  const __amdgpu_buffer_rsrc_t rs = __builtin_amdgcn_make_buffer_rsrc((void*)ws, 0, WS_END, 0x00020000);
  const v4u srd = {(unsigned)(size_t)ws, (unsigned)((size_t)ws >> 32) & 0xffffu, (unsigned)WS_END, 0x00020000u};
  constexpr int NA = 64 - 4 * NB;
  const char* vbase = (const char*)Vc + ((size_t)((b * 8 + kvh) * SK + kh * 4096)) * (DH * 2) + qd * 128;
  if (wave < 2) STAMP(8);
  if (wave < 2) {
    __builtin_amdgcn_s_setprio(3);
    if (tid < 30) L->freed[0][tid] = 0u;
    volatile LAS unsigned* full = &L->full[wave];
    const char* kb = (const char*)Kc + ((size_t)((b * 8 + kvh) * SK + s * 1024 + wave * 16)) * (DH * 2);
    unsigned vo[4];
#pragma unroll
    for (int g = 0; g < 4; ++g) vo[g] = (lane >> 5) * 512 + (((lane & 31) ^ (((2 * g + (lane >> 5)) & 7) << 1)) << 4);
    constexpr int NOLEAD = -LV - 1;
    int pub = 0;
    const long vdelta = (const char*)Vc - (const char*)Kc - (32 - 4 * NB) * 16384;
    int half = 4096;
#pragma unroll 1
    for (int n = 0; n < 64; ++n) {
      TL(n);
      if (n == PREH) WG_BARRIER();
      const char* src = kb + (size_t)n * 16384 + (n < 32 ? 0 : vdelta);
      if (n >= NA) {
        const int m = n - NA;
        src = vbase + (size_t)((m / NB) * 1024 + (m % NB) * 128 + wave * 64) * 512;
        if (m == 0) {
          STAMP(1);
          half = 16384;
#pragma unroll
          for (int g = 0; g < 4; ++g) vo[g] = (8 * g + (lane >> 3)) * 512 + (((lane & 7) ^ (((lane >> 3) & 2) | ((g & 1) << 2))) << 4) - g * 1024;
        }
      }
      int ld = NOLEAD;
      if (n >= 8) {
        const v2u av = *(const volatile LAS v2u*)L->anchor;
        const unsigned a0 = __builtin_amdgcn_readfirstlane(av[0]), an = __builtin_amdgcn_readfirstlane(av[1]);
        const int span = min(max((int)(an - a0) - TOFF2 + TOFF1, 1150), 1400);
        ld = (int)((an ? an + (RC ? ((n - 26) * span * RC) >> 16 : (n - 26) * TAU2 / 10) : a0 + n * TAU1 / 10) - (unsigned)__builtin_amdgcn_s_memrealtime());
        if (!an) { if (!a0) ld = NOLEAD; else if (ld <= 0) ld = min(ld + LAG1, NOLEAD); }
        volatile LAS unsigned* fr = &L->freed[wave][n & 7];
#pragma unroll 1
        for (unsigned spins = 0; spins < (1u << 22); ++spins) {
          if ((int)__builtin_amdgcn_readfirstlane(*fr) >= n - 7) break;
          DRAIN();
          pub = n; *full = pub;
          __builtin_amdgcn_s_sleep(1);
        }
        asm volatile("" ::: "memory");
      }
      unsigned char* dst = sm.ring[wave][n & 7];
      if (ld > 0) { if (ld > 50) issue8<8>(src, half, dst, vo, n, pub, full); else issue8<10>(src, half, dst, vo, n, pub, full); }
      else { if (ld > -60 && n < 54) issue8<13>(src, half, dst, vo, n, pub, full); else issue8<20>(src, half, dst, vo, n, pub, full); }
    }
#pragma unroll
    for (int k = 2; k >= 0; --k) {
      asm volatile("s_waitcnt vmcnt(%0)" ::"i"(8 * k) : "memory");
      *full = max(pub, 64 - k);
    }
    STAMP(2);
    WG_BARRIER();
  } else {
    const GAS unsigned* x32 = (const GAS unsigned*)(x + b * DM);
    const int wv = wave - 2;
    const u64 ep = *(const u64*)(ws + WS_EPOCH + b * 256);
    {
      const int r = lane >> 3;
      const GAS char* wqp = (const GAS char*)wq + (size_t)(2 * r) * (QLD * 2) + (j * 64 + (lane & 7) * 8) * 2;
      v4u wr[10]; unsigned xp[5];
#pragma unroll
      for (int k = 0; k < 5; ++k) {
        const int pg = min(wv + 6 * k, 27);
        xp[k] = x32[8 * pg + r];
        wr[2 * k] = ldw(wqp + (size_t)(16 * pg) * (QLD * 2));
        wr[2 * k + 1] = ldw(wqp + (size_t)(16 * pg + 1) * (QLD * 2));
      }
      if (wave == 2) clear_slots(rs, gid, lane);
      __builtin_amdgcn_sched_barrier(0);
      __builtin_amdgcn_s_barrier();
      asm volatile("" ::: "memory");
      __builtin_amdgcn_sched_barrier(0);
      if (wv >= 4) xp[4] = 0u;
      STAMP(1);
      float qa[8];
#pragma unroll
      for (int c = 0; c < 8; ++c) qa[c] = 0.f;
#pragma unroll
      for (int k = 0; k < 5; ++k) pair_dot(wr[2 * k], wr[2 * k + 1], xp[k], qa);
      L->qpart[wv][(lane & 7) * 8 + r] = tr8(qa, lane);
    }
    u64 tag = (ep + 1) << 16;
    asm volatile("" : "+v"(tag));
    LGKM0();
    unsigned arr = 0;
    if (lane == 0) arr = __hip_atomic_fetch_add(&L->cnt, 1u, __ATOMIC_RELAXED, __HIP_MEMORY_SCOPE_WORKGROUP);
    if (__builtin_amdgcn_readfirstlane(arr) == 5) {
      asm volatile("" ::: "memory");
      float qv = L->qpart[0][lane];
#pragma unroll
      for (int k = 1; k < 6; ++k) qv += L->qpart[k][lane];
      __hip_atomic_store((GAS u64*)(ws + WS_QG) + gid * 64 + lane, tag | (pkbf(qv, 0.f) & 0xffffu), RLX_AGENT);
    }
    STAMP(2);
    if (wave >= 4) {
      const int cw = wave - 4, w = cw & 1, par = cw >> 1;
      volatile LAS unsigned* full = &L->full[w];
      volatile LAS unsigned* fr = L->freed[w];
      const int g4 = lane >> 4, c16 = lane & 15;
      const unsigned ka0 = c16 * 512 + ((g4 ^ ((c16 & 7) << 1)) << 4);
      const unsigned ta0 = (8 * g4 + (c16 >> 2)) * 128 + ((((c16 >> 3) & 1) | ((g4 & 1) << 1)) << 5) + (lane & 3) * 8;
      {
        bf16x8 qf[8];
#pragma unroll 1
        for (int gen = 0; gen < 4; ++gen) {
          bf16x8 kf[4][8];
#pragma unroll
          for (int h = 0; h < 4; ++h) {
            const int n = 8 * gen + 2 * h + par;
            wait_ge(full, n + 1);
            const LAS unsigned char* cell = L->ring[w][2 * h + par];
#pragma unroll
            for (int t = 0; t < 8; ++t) kf[h][t] = *(const LAS bf16x8*)(cell + ((ka0 ^ ((t & 3) << 6)) + (t >> 2) * 256));
            LGKM0(); fr[2 * h + par] = n + 1;
            PIN8(kf[h]);
          }
          if (gen == 0) {
            wait_ge(&L->qready, 1);
            STAMP(3);
#pragma unroll
            for (int t = 0; t < 8; ++t) qf[t] = *(const LAS bf16x8*)(L->q + (lane & 1) * 128 + (4 * t + g4) * 4);
          }
          f32x4 sc[4];
#pragma unroll
          for (int h = 0; h < 4; ++h) sc[h] = f32x4{0.f, 0.f, 0.f, 0.f};
#pragma unroll
          for (int t = 0; t < 8; ++t)
#pragma unroll
            for (int h = 0; h < 4; ++h) sc[h] = __builtin_amdgcn_mfma_f32_16x16x32_bf16(kf[h][t], qf[t], sc[h], 0, 0, 0);
          const f32x4 sv = sel4(sc[0], sc[1], sc[2], sc[3], c16 & 2, c16 & 4);
          *(LAS v2u*)&L->P[gen][c16 & 1][(2 * (c16 & 6) + 2 * par + w) * 8 + 2 * g4] = v2u{pkbf(sv[0], sv[1]), pkbf(sv[2], sv[3])};
          LGKM0(); *(volatile LAS unsigned*)&L->kdone[cw] = gen + 1;
        }
      }
      STAMP(4);
      v4u wf[32];
      const GAS u16* wop = (const GAS u16*)wo + (size_t)(2 * kvh * 256 + 64 * qd + 8 * g4) * DM + min(128 * cw + 8 * c16, DM - 8);
      f32x4 acc0 = {0.f, 0.f, 0.f, 0.f}, acc1 = {0.f, 0.f, 0.f, 0.f};
      float mprev0 = -INFINITY, mprev1 = -INFINITY;
      const int vrow = 4 * g4 + (c16 >> 2);
      const unsigned va0 = vrow * 512 + ((vrow & 7) << 5) + (lane & 3) * 8;
      auto astep = [&](int aa) {
        const int n = 32 - 4 * NB + 2 * aa + par, gen = aa >> 2;
        if ((aa & 3) == 0 || aa == 2 * NB) {
          wait_ge(&L->pready, gen + 1);
          const float m0 = L->mref[gen][0], m1 = L->mref[gen][1];
          const float a0 = __builtin_amdgcn_exp2f(mprev0 - m0), a1 = __builtin_amdgcn_exp2f(mprev1 - m1);
          mprev0 = m0; mprev1 = m1;
          acc0 *= f32x4{a0, a1, a0, a1}; acc1 *= f32x4{a0, a1, a0, a1};
        }
        const v2u pv = *(const LAS v2u*)&L->P[gen][lane & 1][(4 * (aa & 3) + 2 * par + w) * 8 + 2 * g4];
        s16x4 pa[8];
#pragma unroll
        for (int v = 0; v < 8; ++v) { const bool on = (c16 >> 1) == v; pa[v] = __builtin_bit_cast(s16x4, v2u{on ? pv[0] : 0u, on ? pv[1] : 0u}); }
        PIN8(pa);
        wait_ge(full, n + 1);
        const LAS unsigned char* cell = L->ring[w][n & 7];
#pragma unroll
        for (int v = 0; v < 8; ++v) {
          acc0 = __builtin_amdgcn_mfma_f32_16x16x16bf16_1k(pa[v], __builtin_amdgcn_ds_read_tr16_b64_v4i16((LAS s16x4*)(cell + (va0 ^ (v << 5)))), acc0, 0, 0, 0);
          acc1 = __builtin_amdgcn_mfma_f32_16x16x16bf16_1k(pa[v], __builtin_amdgcn_ds_read_tr16_b64_v4i16((LAS s16x4*)(cell + ((va0 ^ (v << 5)) + 256))), acc1, 0, 0, 0);
        }
        asm volatile("" : "+v"(acc0), "+v"(acc1));
        LGKM0(); fr[n & 7] = n + 1;
      };
      constexpr int NWS = (32 + WOB - 1) / WOB;
#pragma unroll
      for (int hb = 0; hb < NWS; ++hb) {
#pragma unroll
        for (int i = WOB * hb; i < WOB * hb + WOB && i < 32; ++i) wf[i] = ldw(wop + (size_t)((i >> 4) * 256 + ((i >> 3) & 1) * 32 + (i & 7)) * DM);
        __builtin_amdgcn_sched_barrier(0);
        astep(2 * NB + hb);
      }
#pragma unroll 1
      for (int aa = 2 * NB + NWS; aa < 16; ++aa) astep(aa);
#pragma unroll
      for (int r = 0; r < 4; ++r) {
        L->mrg[cw][(r & 1) * 256 + (2 * g4 + (r >> 1)) * 16 + c16] = acc0[r];
        L->mrg[cw][(r & 1) * 256 + 128 + (2 * g4 + (r >> 1)) * 16 + c16] = acc1[r];
      }
      LGKM0();
      if (lane == 0) __hip_atomic_fetch_add(&L->acnt, 1u, __ATOMIC_RELAXED, __HIP_MEMORY_SCOPE_WORKGROUP);
      STAMP(7);
      __builtin_amdgcn_sched_barrier(0);
#pragma unroll
      for (int kb = 0; kb < 4; ++kb) {
        v4u tf[8];
#pragma unroll
        for (int e = 0; e < 8; ++e)
#pragma unroll
          for (int d = 0; d < 4; ++d)
            tf[e][d] = (e & 1) ? perm_hi(wf[8 * kb + 2 * d][e >> 1], wf[8 * kb + 2 * d + 1][e >> 1]) : perm_lo(wf[8 * kb + 2 * d][e >> 1], wf[8 * kb + 2 * d + 1][e >> 1]);
        PIN8(tf);
#pragma unroll
        for (int e = 0; e < 8; ++e) wf[8 * kb + e] = tf[e];
      }
      f32x4 acc[4];
#pragma unroll
      for (int t = 0; t < 4; ++t) acc[t] = f32x4{0.f, 0.f, 0.f, 0.f};
      f32x4 o = {0.f, 0.f, 0.f, 0.f};
      wait_ge(&L->pready, 5);
      LAS v2u* obw = (LAS v2u*)&L->ob[cw][(c16 & 1) * 32 + ((c16 >> 1) & 3) * 8 + 2 * g4];
#pragma unroll 1
      for (int mm = 0; mm < 2 * NB; ++mm) {
        const int m = 2 * mm + par, n = NA + m;

        const LAS unsigned* pp = &L->PB[m / NB][lane & 1][(m % NB) * 64 + w * 32 + 4 * g4];
        const bf16x8 p0 = *(const LAS bf16x8*)pp, p1 = *(const LAS bf16x8*)(pp + 16);
        wait_ge(full, n + 1);
        bf16x8 vf[8];
        read_tr(L->ring[w][n & 7], ta0, vf);
#pragma unroll
        for (int t = 0; t < 4; ++t) acc[t] = __builtin_amdgcn_mfma_f32_16x16x32_bf16(vf[t], p0, acc[t], 0, 0, 0);
#pragma unroll
        for (int t = 0; t < 4; ++t) acc[t] = __builtin_amdgcn_mfma_f32_16x16x32_bf16(vf[4 + t], p1, acc[t], 0, 0, 0);
        asm volatile("" : "+v"(acc[0]), "+v"(acc[1]), "+v"(acc[2]), "+v"(acc[3]));
      }
      STAMP(5);
      if (cw == 0) {
        wait_ge(&L->oready, 1);
        if ((c16 & 1) == kh) o = *(const LAS f32x4*)&L->oa[((c16 >> 1) & 3) * 16 + 4 * g4];
      }
      o += sel4(acc[0], acc[1], acc[2], acc[3], c16 & 2, c16 & 4);
      *obw = v2u{pkbf(o[0], o[1]), pkbf(o[2], o[3])};
      const int col = 128 * cw + 8 * c16 + 4 * (g4 & 1);
      const bool st_on = g4 < 2 && col < DM;
      const int yoff = st_on ? WS_Y + gid * (DM * 4) + col * 4 : WS_END;
      const LAS float* ybp = &L->yb[st_on ? col : 0];
      const LAS unsigned* obr = &L->ob[c16 & 3][4 * g4];
      WG_BARRIER();
      STAMP(6);
      bf16x8 of[4];
#pragma unroll
      for (int kb = 0; kb < 4; ++kb) of[kb] = *(const LAS bf16x8*)(obr + 16 * kb);
      const f32x4 ybv = *(const LAS f32x4*)ybp;
      const float g = L->gate;
      __builtin_amdgcn_sched_barrier(0);
      f32x4 ya[8];
#pragma unroll
      for (int e = 0; e < 8; ++e) ya[e] = f32x4{0.f, 0.f, 0.f, 0.f};
#pragma unroll
      for (int kb = 0; kb < 4; ++kb) {
#pragma unroll
        for (int e = 0; e < 8; ++e) ya[e] = __builtin_amdgcn_mfma_f32_16x16x32_bf16(of[kb], __builtin_bit_cast(bf16x8, wf[8 * kb + e]), ya[e], 0, 0, 0);
        __builtin_amdgcn_sched_barrier(0);
      }
      v4u yv;
#pragma unroll
      for (int r = 0; r < 4; ++r) {
        const float lo = (ya[r][0] + ya[r][1]) + (ya[r][2] + ya[r][3]), hi = (ya[4 + r][0] + ya[4 + r][1]) + (ya[4 + r][2] + ya[4 + r][3]);
        yv[r] = san(fmaf(g, (g4 & 1) ? hi : lo, ybv[r]));
      }
      __builtin_amdgcn_raw_buffer_store_b128(yv, rs, yoff, 0, 16);
      STAMP(8);
    } else {
      if (wave == 2) {
        {
          const unsigned thi = (unsigned)(tag >> 32), tlo = (unsigned)tag;
          int off[4];
#pragma unroll
          for (int k = 0; k < 4; ++k) off[k] = WS_QG + (gid & ~7) * 512 + k * 1024 + lane * 16;
          __builtin_amdgcn_s_sleep(80);
#pragma unroll 1
          for (unsigned spins = 0; spins < SPIN_CAP; ++spins) {
            v4u g[4];
            bool ok = true;
#pragma unroll
            for (int k = 0; k < 4; ++k) g[k] = __builtin_amdgcn_raw_buffer_load_b128(rs, off[k], 0, 16);
#pragma unroll
            for (int k = 0; k < 4; ++k) {
              if (off[k] != WS_END && g[k][1] == thi && g[k][3] == thi && (g[k][0] & 0xffff0000u) == tlo && (g[k][2] & 0xffff0000u) == tlo) {
                L->q[k * 64 + lane] = perm_lo(g[k][0], g[k][2]);
                off[k] = WS_END;
              }
              ok &= off[k] == WS_END;
            }
            if (__all(ok)) break;
            __builtin_amdgcn_s_sleep(16);
          }
          LGKM0();
          *(volatile LAS unsigned*)&L->qready = 1u;
        }
        STAMP(3);
        constexpr float C2 = 0.0625f * 1.4426950408889634f;
        float mrun = -INFINITY, lrun = 0.f;
#pragma unroll 1
        for (int gen = 0; gen < 4; ++gen) {
#pragma unroll
          for (int c = 0; c < 4; ++c) wait_ge(&L->kdone[c], gen + 1);
          v4u g = *(const LAS v4u*)&L->P[gen][0][lane * 4];
          if (gen == 0) {
            __builtin_amdgcn_raw_buffer_store_b128(g, rs, WS_S + gid * 1024 + lane * 16, 0, 16);
          }
          if (gen == 2) {
            DRAIN();
            if (lane == 0) __hip_atomic_store((GAS u64*)(ws + WS_RDY) + gid, tag | ((unsigned)__builtin_amdgcn_s_memrealtime() & 0xffffu), RLX_AGENT);
          }
          float cm = -INFINITY;
#pragma unroll
          for (int e = 0; e < 4; ++e) cm = fmaxf(cm, fmaxf(bflo(g[e]), bfhi(g[e])));
          const float mn = fmaxf(mrun, max32(cm) * C2);
          float ls = 0.f;
#pragma unroll
          for (int e = 0; e < 4; ++e) {
            const float pl = __builtin_amdgcn_exp2f(fmaf(bflo(g[e]), C2, -mn)), ph = __builtin_amdgcn_exp2f(fmaf(bfhi(g[e]), C2, -mn));
            ls += pl + ph;
            g[e] = pkbf(pl, ph);
          }
          *(LAS v4u*)&L->P[gen][0][lane * 4] = g;
          lrun = fmaf(lrun, __builtin_amdgcn_exp2f(mrun - mn), ls);
          mrun = mn;
          if ((lane & 31) == 0) L->mref[gen][lane >> 5] = mn;
          LGKM0();
          *(volatile LAS unsigned*)&L->pready = gen + 1;
        }
        lrun += dpp<XOR1>(lrun); lrun += dpp<XOR2>(lrun); lrun += dpp<ROR4>(lrun); lrun += dpp<XOR8>(lrun); lrun = swp16_add(lrun, lrun);
        if ((lane & 31) == 0) __builtin_amdgcn_raw_buffer_store_b64(v2u{san(mrun), san(lrun)}, rs, WS_ST + gid * 128 + (lane >> 5) * 8, 0, 16);
        u64 gv;
#pragma unroll 1
        for (unsigned spins = 0; spins < SPIN_CAP; ++spins) {
          gv = __hip_atomic_load((GAS u64*)(ws + WS_RDY) + b * 64 + lane, RLX_AGENT);
          if (__all((gv & ~0xffffull) == tag)) break;
          __builtin_amdgcn_s_sleep(32);
        }
        *(volatile LAS unsigned*)&L->anchor[1] = (mean_time((unsigned)gv, true) + TOFF2 - (qd == 3 ? Q3LEAD : 0)) | 1u;
        if (j == 0 && lane == 0) __hip_atomic_store((GAS u64*)(ws + WS_EPOCH + b * 256), ep + 1, RLX_AGENT);
        asm volatile("" ::: "memory");
        v4u h[4];
#pragma unroll
        for (int k = 0; k < 4; ++k) h[k] = __builtin_amdgcn_raw_buffer_load_b128(rs, WS_S + ((gid & ~3) + k) * 1024 + lane * 16, 0, 16);
        v2u sv;
#pragma unroll 1
        for (unsigned spins = 0; spins < SPIN_CAP; ++spins) {
          __builtin_amdgcn_s_sleep(127);
          sv = __builtin_amdgcn_raw_buffer_load_b64(rs, WS_ST + ((gid & ~7) + (lane & 7)) * 128 + (lane >> 5) * 8, 0, 16);
          if (__all(sv[0] != SENT && sv[1] != SENT)) break;
        }
        const float mp = __uint_as_float(sv[0]);
        float M = fmaxf(mp, dpp<XOR1>(mp)); M = fmaxf(M, dpp<XOR2>(M)); M = fmaxf(M, dpp<XOR7>(M));
        float Ls = __uint_as_float(sv[1]) * __builtin_amdgcn_exp2f(mp - M);
        Ls += dpp<XOR1>(Ls); Ls += dpp<XOR2>(Ls); Ls += dpp<XOR7>(Ls);
        const float inv = 1.f / Ls;
#pragma unroll
        for (int k = 0; k < 4; ++k) {
#pragma unroll
          for (int e = 0; e < 4; ++e)
            h[k][e] = pkbf(inv * __builtin_amdgcn_exp2f(fmaf(bflo(h[k][e]), C2, -M)), inv * __builtin_amdgcn_exp2f(fmaf(bfhi(h[k][e]), C2, -M)));
          *(LAS v4u*)&L->PB[k][0][lane * 4] = h[k];
        }
        LGKM0();
        *(volatile LAS unsigned*)&L->pready = 5u;
        const float as = inv * __builtin_amdgcn_exp2f(mrun - M);
        STAMP(4);
        wait_ge(&L->acnt, 4);
        f32x4 o0 = *(const LAS f32x4*)(L->mrg[0] + lane * 8), o1 = *(const LAS f32x4*)(L->mrg[0] + lane * 8 + 4);
#pragma unroll
        for (int ww = 1; ww < 4; ++ww) { o0 += *(const LAS f32x4*)(L->mrg[ww] + lane * 8); o1 += *(const LAS f32x4*)(L->mrg[ww] + lane * 8 + 4); }
        o0 *= as; o1 *= as;
        v4u ov = {pkbf(o0[0], o0[1]), pkbf(o0[2], o0[3]), pkbf(o1[0], o1[1]), pkbf(o1[2], o1[3])};
#pragma unroll
        for (int k = 0; k < 4; ++k) ov[k] = ov[k] == SENT ? 0x7FC07FC0u : ov[k];
        __builtin_amdgcn_raw_buffer_store_b128(ov, rs, WS_PO + gid * 1024 + lane * 16, 0, 16);
        STAMP(5);
        int off = WS_PO + ((gid & ~7) + (lane >> 3)) * 1024 + s * 128 + (lane & 7) * 16;
        __builtin_amdgcn_s_sleep(20);
#pragma unroll 1
        for (unsigned spins = 0; spins < SPIN_CAP; ++spins) {
          if (off != WS_END) {
            ov = __builtin_amdgcn_raw_buffer_load_b128(rs, off, 0, 16);
            if (max(max(ov[0], ov[1]), max(ov[2], ov[3])) != SENT) off = WS_END;
          }
          if (__all(off == WS_END)) break;
          __builtin_amdgcn_s_sleep(2);
        }
        STAMP(6);
        float oe[8];
#pragma unroll
        for (int k = 0; k < 4; ++k) { oe[2 * k] = bflo(ov[k]); oe[2 * k + 1] = bfhi(ov[k]); }
        L->oa[8 * (lane & 7) + (lane >> 3)] = tr8(oe, lane);
        LGKM0();
        *(volatile LAS unsigned*)&L->oready = 1u;
        STAMP(7);
      } else {
        wait_ge(&L->full[0], 3);
        if (lane == 0) __hip_atomic_store((GAS u64*)(ws + WS_TS) + gid, tag | ((unsigned)__builtin_amdgcn_s_memrealtime() & 0xffffu), RLX_AGENT);
        wait_ge(&L->full[0], NTS);
        {
          const u64 tv = __hip_atomic_load((GAS u64*)(ws + WS_TS) + b * 64 + lane, RLX_AGENT);
          *(volatile LAS unsigned*)&L->anchor[0] = (mean_time((unsigned)tv, (tv & ~0xffffull) == tag) + TOFF1 - (qd == 3 ? Q3LEAD : 0)) | 1u;
        }
        const int cl = lane < 56 ? lane : 55;
        float z = 0.f;
#pragma unroll
        for (int k = 0; k < 4; ++k) {
          const int idx = lane + 64 * k;
          const bool in = idx < DM / 2;
          const unsigned xv = x32[in ? idx : 0], gv = ((const GAS unsigned*)gw)[in ? idx : 0];
          const float t = rbf(bflo(xv) * bflo(gv)) + rbf(bfhi(xv) * bfhi(gv));
          z += in ? t : 0.f;
        }
        z = swp32_add(z, z); z = swp16_add(z, z); z += dpp<XOR8>(z); z += dpp<XOR7>(z); z += dpp<XOR2>(z); z += dpp<XOR1>(z);
        const float g = rbf(1.f / (1.f + __expf(-rbf(z))));
        if (lane == 0) L->gate = g;
        float yb[8];
#pragma unroll
        for (int c = 0; c < 8; ++c) yb[c] = 0.f;
        if (s != 3) {
          const int u0 = (s == 7 ? 48 + kvh : kvh * 6 + 2 * qd + kh) * 32, rr = lane >> 2;
          float ha[8];
#pragma unroll
          for (int c = 0; c < 8; ++c) ha[c] = 0.f;
          const GAS u16* w1p = (const GAS u16*)w1 + (size_t)(2 * rr) * DFF + u0 + (lane & 3) * 8;
#pragma unroll 1
          for (int h = 0; h < 14 / MB1; ++h) {
            v4u ma[2 * MB1]; unsigned xq[MB1];
#pragma unroll
            for (int k = 0; k < MB1; ++k) {
              ma[2 * k] = ldw(w1p + (size_t)((MB1 * h + k) * 32) * DFF);
              ma[2 * k + 1] = ldw(w1p + (size_t)((MB1 * h + k) * 32 + 1) * DFF);
              xq[k] = x32[(MB1 * h + k) * 16 + rr];
            }
#pragma unroll
            for (int k = 0; k < MB1; ++k) pair_dot(ma[2 * k], ma[2 * k + 1], xq[k], ha);
          }
          float hv = tr8(ha, lane);
          hv += dpp<SHR4>(hv);
          hv = fmaxf(rbf(hv), 0.f);
#pragma unroll 1
          for (int c4 = 0; c4 < 32 / MB2; ++c4) {
            v4u w2r[MB2];
#pragma unroll
            for (int u = 0; u < MB2; ++u) w2r[u] = ldw((const GAS u16*)w2 + (size_t)(u0 + c4 * MB2 + u) * DM + cl * 8);
#pragma unroll
            for (int u = 0; u < MB2; ++u) {
              const int un = c4 * MB2 + u;
              const float hu = __builtin_bit_cast(float, __builtin_amdgcn_readlane(__builtin_bit_cast(int, hv), (un & 7) * 8 + 4 + (un >> 3)));
#pragma unroll
              for (int c = 0; c < 4; ++c) { yb[2 * c] = fmaf(hu, bflo(w2r[u][c]), yb[2 * c]); yb[2 * c + 1] = fmaf(hu, bfhi(w2r[u][c]), yb[2 * c + 1]); }
            }
          }
        }
        const float omg = rbf(1.f - g);
        if (lane < 56) {
          *(LAS f32x4*)(L->yb + lane * 8) = f32x4{omg * yb[0], omg * yb[1], omg * yb[2], omg * yb[3]};
          *(LAS f32x4*)(L->yb + lane * 8 + 4) = f32x4{omg * yb[4], omg * yb[5], omg * yb[6], omg * yb[7]};
        }
      }
      WG_BARRIER();
    }
  }
  if (j < 14) {
    const v4u y = poll16(srd, WS_Y + (b * 64 + wave * 8 + (lane >> 3)) * (DM * 4) + j * 128 + (lane & 7) * 16);
    STAMP(9);
    const float r0 = swp32_add(__uint_as_float(y[0]), __uint_as_float(y[2])), r1 = swp32_add(__uint_as_float(y[1]), __uint_as_float(y[3]));
    float r = swp16_add(r0, r1);
    r += dpp<XOR8>(r);
    if (!(lane & 8)) L->fin[wave][(lane & 7) * 4 + (lane >> 4)] = r;
    WG_BARRIER();
    if (tid < 32) {
      const float t = ((L->fin[0][tid] + L->fin[1][tid]) + (L->fin[2][tid] + L->fin[3][tid])) + ((L->fin[4][tid] + L->fin[5][tid]) + (L->fin[6][tid] + L->fin[7][tid]));
      out[b * DM + j * 32 + tid] = (u16)pkbf(t, 0.f);
    }
  }
  STAMP_OUT();
  DRAIN();
}

extern "C" void kernel_launch(void* const* d_in, const int* in_sizes, int n_in, void* d_out, int out_size, void* d_ws, size_t ws_size, hipStream_t stream) {
  fused_kernel<<<256, 512, 0, stream>>>((const u16*)d_in[0], (const u16*)d_in[1], (const u16*)d_in[2], (const u16*)d_in[3], (const u16*)d_in[4],
                                        (const u16*)d_in[6], (const u16*)d_in[7], (const u16*)d_in[8], (unsigned char*)d_ws, (u16*)d_out);
}
```

```cpp
#include <hip/hip_runtime.h>
#include <hip/hip_bf16.h>
#include <cstdint>

#define NB 2
#define PREH 1
#define WOB 4
#define MB1 1
#define MB2 4
#define TAU 590
#define TAU1 580
#define TOFF1 -330
#define LAG1 40
#define NTS 8
#define NRETRY 4
#define MISSNOW 1
#define L3 100
#define D3 5
#define DMAX 20
#define NOL -1
#define TAU2 562
#define TOFF2 30
#define Q3LEAD 60
#define STAMPS 0

typedef unsigned v4u __attribute__((ext_vector_type(4)));
typedef unsigned v3u __attribute__((ext_vector_type(3)));
typedef unsigned v2u __attribute__((ext_vector_type(2)));
typedef float f32x4 __attribute__((ext_vector_type(4)));
typedef __bf16 bf16x2 __attribute__((ext_vector_type(2)));
typedef __bf16 bf16x8 __attribute__((ext_vector_type(8)));
typedef short s16x4 __attribute__((ext_vector_type(4)));
typedef unsigned short u16;
typedef unsigned long long u64;
#define GAS __attribute__((address_space(1)))
#define LAS __attribute__((address_space(3)))
#define RLX_AGENT __ATOMIC_RELAXED, __HIP_MEMORY_SCOPE_AGENT

constexpr int DM = 448, SK = 8192, DH = 256, DFF = 1792, QLD = 4096;
constexpr unsigned SENT = 0xFFFFFFFFu;
constexpr unsigned SPIN_CAP = 1u << 18;
constexpr int WS_EPOCH = 0;
constexpr int WS_RDY = 4096;
constexpr int WS_TS = 6144;
constexpr int WS_QG = 8192;
constexpr int WS_S = WS_QG + 256 * 64 * 8;
constexpr int WS_ST = WS_S + 256 * 1024;
constexpr int WS_PO = WS_ST + 256 * 128;
constexpr int WS_Y = WS_PO + 256 * 1024;
constexpr int WS_END = WS_Y + 256 * DM * 4;
constexpr size_t WS_STAMP = 16u << 20;

__device__ __forceinline__ float bflo(unsigned u) { return __builtin_bit_cast(float, u << 16); }
__device__ __forceinline__ float bfhi(unsigned u) { return __builtin_bit_cast(float, u & 0xffff0000u); }
__device__ __forceinline__ unsigned pkbf(float lo, float hi) {
  bf16x2 r; r[0] = (__bf16)lo; r[1] = (__bf16)hi; return __builtin_bit_cast(unsigned, r);
}
__device__ __forceinline__ float rbf(float f) { return bflo(pkbf(f, 0.f)); }
__device__ __forceinline__ float dot2(unsigned a, unsigned b, float acc) {
  return __builtin_amdgcn_fdot2_f32_bf16(__builtin_bit_cast(bf16x2, a), __builtin_bit_cast(bf16x2, b), acc, false);
}
__device__ __forceinline__ unsigned perm_lo(unsigned a, unsigned b) { return __builtin_amdgcn_perm(b, a, 0x05040100u); }
__device__ __forceinline__ unsigned perm_hi(unsigned a, unsigned b) { return __builtin_amdgcn_perm(b, a, 0x07060302u); }
template <int CTRL> __device__ __forceinline__ float dpp(float x) {
  return __builtin_bit_cast(float, __builtin_amdgcn_mov_dpp(__builtin_bit_cast(int, x), CTRL, 0xf, 0xf, true));
}
constexpr int XOR1 = 0xB1, XOR2 = 0x4E, XOR7 = 0x141, XOR8 = 0x128, ROR4 = 0x124, SHR4 = 0x114;
__device__ __forceinline__ float swp16_add(float a, float b) {
  auto s = __builtin_amdgcn_permlane16_swap(__float_as_uint(a), __float_as_uint(b), false, false);
  return __uint_as_float(s[0]) + __uint_as_float(s[1]);
}
__device__ __forceinline__ float swp32_add(float a, float b) {
  auto s = __builtin_amdgcn_permlane32_swap(__float_as_uint(a), __float_as_uint(b), false, false);
  return __uint_as_float(s[0]) + __uint_as_float(s[1]);
}
__device__ __forceinline__ float swp16_max(float a) {
  auto s = __builtin_amdgcn_permlane16_swap(__float_as_uint(a), __float_as_uint(a), false, false);
  return fmaxf(__uint_as_float(s[0]), __uint_as_float(s[1]));
}
__device__ __forceinline__ float swp32_max(float a) {
  auto s = __builtin_amdgcn_permlane32_swap(__float_as_uint(a), __float_as_uint(a), false, false);
  return fmaxf(__uint_as_float(s[0]), __uint_as_float(s[1]));
}
template <int CTRL> __device__ __forceinline__ float tr_dpp(float lo, float hi, bool sel) {
  const float t1 = lo + dpp<CTRL>(lo), t2 = hi + dpp<CTRL>(hi);
  return sel ? t2 : t1;
}
__device__ __forceinline__ float tr8(float (&a)[8], int lane) {
#pragma unroll
  for (int i = 0; i < 4; ++i) a[i] = swp32_add(a[i], a[i + 4]);
#pragma unroll
  for (int i = 0; i < 2; ++i) a[i] = swp16_add(a[i], a[i + 2]);
  return tr_dpp<XOR8>(a[0], a[1], lane & 8);
}
__device__ __forceinline__ float max32(float v) {
  v = fmaxf(v, dpp<XOR1>(v)); v = fmaxf(v, dpp<XOR2>(v)); v = fmaxf(v, dpp<ROR4>(v)); v = fmaxf(v, dpp<XOR8>(v));
  return swp16_max(v);
}
__device__ __forceinline__ void pair_dot(const v4u& ra, const v4u& rb, unsigned xp, float (&a)[8]) {
#pragma unroll
  for (int c = 0; c < 4; ++c) {
    a[2 * c] = dot2(perm_lo(ra[c], rb[c]), xp, a[2 * c]);
    a[2 * c + 1] = dot2(perm_hi(ra[c], rb[c]), xp, a[2 * c + 1]);
  }
}
__device__ __forceinline__ unsigned san(float f) { const unsigned u = __float_as_uint(f); return u == SENT ? 0x7FC00000u : u; }
__device__ __forceinline__ v4u ldw(const GAS void* p) {
  return *(const GAS v4u*)p;
}
#define WG_BARRIER() do { asm volatile("s_waitcnt lgkmcnt(0)" ::: "memory"); __builtin_amdgcn_s_barrier(); asm volatile("" ::: "memory"); __builtin_amdgcn_sched_barrier(0); } while (0)
#define DRAIN() asm volatile("s_waitcnt vmcnt(0)" ::: "memory")
#define STAMP(i) do {} while (0)
#define STAMP_OUT() do {} while (0)
#define TL(n) do {} while (0)

struct Lds {
  unsigned char ring[2][8][8192];
  unsigned P[4][2][128];
  unsigned PB[4][2][128];
  float mrg[4][512];
  float oa[64];
  unsigned q[256];
  float yb[DM];
  float qpart[6][64];
  float fin[8][32];
  unsigned ob[4][68];
  float mref[4][2];
  float gate, pad0[3];
  unsigned freed[2][8];
  unsigned full[2];
  unsigned cnt, qready;
  unsigned kdone[4];
  unsigned pready, acnt, oready, pad1;
  unsigned anchor[2];
};

#define PIN16(a) asm volatile("" : "+v"(a[0]), "+v"(a[1]), "+v"(a[2]), "+v"(a[3]), "+v"(a[4]), "+v"(a[5]), "+v"(a[6]), "+v"(a[7]), \
                                   "+v"(a[8]), "+v"(a[9]), "+v"(a[10]), "+v"(a[11]), "+v"(a[12]), "+v"(a[13]), "+v"(a[14]), "+v"(a[15]))
#define PIN8(a) asm volatile("" : "+v"(a[0]), "+v"(a[1]), "+v"(a[2]), "+v"(a[3]), "+v"(a[4]), "+v"(a[5]), "+v"(a[6]), "+v"(a[7]))
#define LGKM0() asm volatile("s_waitcnt lgkmcnt(0)" ::: "memory")

__device__ __forceinline__ void wait_ge(volatile LAS unsigned* w, unsigned need) {
#pragma unroll 1
  for (unsigned spins = 0; spins < (1u << 22); ++spins) {
    if ((unsigned)__builtin_amdgcn_readfirstlane(*w) >= need) break;
    __builtin_amdgcn_s_sleep(1);
  }
  asm volatile("" ::: "memory");
}

#define PTHR 64
#define PSLOW 10
#define PSLEEP 2
__device__ __forceinline__ v4u poll16(const v4u& srd, int off) {
  unsigned c0, c1, c2, c3, mx, cnt;
  asm volatile(
      "v_mov_b32 v252, -1\n\tv_mov_b32 v253, -1\n\tv_mov_b32 v254, -1\n\tv_mov_b32 v255, -1\n\t"
      "s_mov_b32 %5, 0x200000\n\t"
      "s_nop 1\n"
      "1:\n\t"
      "buffer_load_dwordx4 v[252:255], %6, %7, 0 offen sc1\n\t"
      "s_sleep %8\n\t"
      "s_waitcnt vmcnt(12)\n\t"
      "v_mov_b32 %0, v252\n\tv_mov_b32 %1, v253\n\tv_mov_b32 %2, v254\n\tv_mov_b32 %3, v255\n\t"
      "v_max3_u32 %4, %0, %1, %2\n\t"
      "v_max_u32 %4, %4, %3\n\t"
      "v_cmp_eq_u32 vcc, -1, %4\n\t"
      "s_and_b64 exec, exec, vcc\n\t"
      "s_cbranch_scc0 2f\n\t"
      "s_bcnt1_i32_b64 vcc_lo, exec\n\t"
      "s_cmp_le_u32 vcc_lo, %9\n\t"
      "s_cbranch_scc1 3f\n\t"
      "s_sleep %10\n"
      "3:\n\t"
      "s_sub_u32 %5, %5, 1\n\t"
      "s_cbranch_scc0 1b\n"
      "2:\n\t"
      "s_mov_b64 exec, -1\n\t"
      "s_nop 4"
      : "=&v"(c0), "=&v"(c1), "=&v"(c2), "=&v"(c3), "=&v"(mx), "=&s"(cnt)
      : "v"(off), "s"(srd), "i"(PSLEEP), "i"(PTHR), "i"(PSLOW)
      : "vcc", "scc", "v252", "v253", "v254", "v255", "memory");
  return v4u{c0, c1, c2, c3};
}

__device__ __forceinline__ void clear_slots(__amdgpu_buffer_rsrc_t rs, int gid, int lane) {
  const v4u sv = {SENT, SENT, SENT, SENT};
  __builtin_amdgcn_raw_buffer_store_b128(sv, rs, WS_PO + gid * 1024 + lane * 16, 0, 16);
  __builtin_amdgcn_raw_buffer_store_b128(sv, rs, WS_Y + gid * (DM * 4) + lane * 16, 0, 16);
  __builtin_amdgcn_raw_buffer_store_b128(sv, rs, lane < 48 ? WS_Y + gid * (DM * 4) + 1024 + lane * 16 : (lane == 48 ? WS_ST + gid * 128 : WS_END), 0, 16);
}
template <int D>
__device__ __forceinline__ void issue8(const char* src, int half, unsigned char* dst, const unsigned (&voff)[4], int n, int& pub, volatile LAS unsigned* full) {
#pragma unroll
  for (int g = 0; g < 8; ++g) {
    asm volatile("s_waitcnt vmcnt(%0)" ::"i"(D - 1) : "memory");
    if (g == (D - 1) % 8) { pub = max(pub, n - (D - 1) / 8); *full = pub; }
    const char* sp = src + (g >> 2) * half;
    if ((g & 3) == 0) __builtin_amdgcn_global_load_lds((const unsigned*)(sp + voff[0]), (unsigned*)(dst + (g & 4) * 1024), 16, 0, 2);
    if ((g & 3) == 1) __builtin_amdgcn_global_load_lds((const unsigned*)(sp + voff[1]), (unsigned*)(dst + (g & 4) * 1024), 16, 1024, 2);
    if ((g & 3) == 2) __builtin_amdgcn_global_load_lds((const unsigned*)(sp + voff[2]), (unsigned*)(dst + (g & 4) * 1024), 16, 2048, 2);
    if ((g & 3) == 3) __builtin_amdgcn_global_load_lds((const unsigned*)(sp + voff[3]), (unsigned*)(dst + (g & 4) * 1024), 16, 3072, 2);
  }
}
__device__ __forceinline__ unsigned mean_time(unsigned stamp, bool ok) {
  const unsigned now = (unsigned)__builtin_amdgcn_s_memrealtime();
  float d = ok ? (float)((now - stamp) & 0xffffu) : 0.f;
  d = swp32_add(d, d); d = swp16_add(d, d); d += dpp<XOR8>(d); d += dpp<XOR7>(d); d += dpp<XOR2>(d); d += dpp<XOR1>(d);
  return now - (unsigned)(MISSNOW ? d * (1.f / 64.f) : d / (float)max(__builtin_popcountll(__builtin_amdgcn_ballot_w64(ok)), 1));
}
__device__ __forceinline__ void read_tr(const LAS unsigned char* cell, unsigned a0, bf16x8* f) {
#pragma unroll
  for (int kb = 0; kb < 2; ++kb)
#pragma unroll
    for (int t = 0; t < 4; ++t) {
      const s16x4 lo = __builtin_amdgcn_ds_read_tr16_b64_v4i16((LAS s16x4*)(cell + ((a0 ^ (t << 5)) + kb * 4096)));
      const s16x4 hi = __builtin_amdgcn_ds_read_tr16_b64_v4i16((LAS s16x4*)(cell + ((a0 ^ (t << 5)) + kb * 4096 + 512)));
      f[kb * 4 + t] = __builtin_bit_cast(bf16x8, __builtin_shufflevector(lo, hi, 0, 1, 2, 3, 4, 5, 6, 7));
    }
}
__device__ __forceinline__ f32x4 sel4(f32x4 a0, f32x4 a1, f32x4 a2, f32x4 a3, bool b0, bool b1) {
  f32x4 r;
#pragma unroll
  for (int e = 0; e < 4; ++e) { const float lo = b0 ? a1[e] : a0[e], hi = b0 ? a3[e] : a2[e]; r[e] = b1 ? hi : lo; }
  return r;
}

__global__ void __launch_bounds__(512, 2) fused_kernel(const u16* __restrict__ x, const u16* __restrict__ Kc, const u16* __restrict__ Vc,
    const u16* __restrict__ wq, const u16* __restrict__ wo, const u16* __restrict__ w1, const u16* __restrict__ w2,
    const u16* __restrict__ gw, unsigned char* ws, u16* out) {
  __shared__ __attribute__((aligned(16))) Lds sm;
  LAS Lds* L = (LAS Lds*)&sm;
  asm volatile("" ::"s"(x), "s"(Kc), "s"(Vc), "s"(wq), "s"(wo), "s"(w1), "s"(w2), "s"(gw), "s"(ws), "s"(out));
  const int tid = threadIdx.x, lane = tid & 63, wave = __builtin_amdgcn_readfirstlane(tid >> 6);
  const int blk = blockIdx.x, b = blk >> 6;
#define GMAP 2
  const int kvh = ((blk >> 3) & 6) | (((blk >> 2) ^ (blk >> 4)) & 1), kh = (blk >> 3) & 1, qd = (blk + (blk >> 4)) & 3;
  const int s = kh * 4 + qd;
  const int j = kvh * 8 + s;
  const int gid = b * 64 + j;
  const __amdgpu_buffer_rsrc_t rs = __builtin_amdgcn_make_buffer_rsrc((void*)ws, 0, WS_END, 0x00020000);
  const v4u srd = {(unsigned)(size_t)ws, (unsigned)((size_t)ws >> 32) & 0xffffu, (unsigned)WS_END, 0x00020000u};
  constexpr int NA = 64 - 4 * NB;
  const char* vbase = (const char*)Vc + ((size_t)((b * 8 + kvh) * SK + kh * 4096)) * (DH * 2) + qd * 128;
  if (wave < 2) STAMP(8);
  if (wave < 2) {
    __builtin_amdgcn_s_setprio(3);
    if (tid < 30) L->freed[0][tid] = 0u;
    volatile LAS unsigned* full = &L->full[wave];
    const char* kb = (const char*)Kc + ((size_t)((b * 8 + kvh) * SK + s * 1024 + wave * 16)) * (DH * 2);
    unsigned vo[4];
#pragma unroll
    for (int g = 0; g < 4; ++g) vo[g] = (lane >> 5) * 512 + (((lane & 31) ^ (((2 * g + (lane >> 5)) & 7) << 1)) << 4);
    constexpr int NOLEAD = -1;
    int pub = 0;
    const long vdelta = (const char*)Vc - (const char*)Kc - (32 - 4 * NB) * 16384;
    int half = 4096;
#pragma unroll 1
    for (int n = 0; n < 64; ++n) {
      TL(n);
      if (n == PREH) WG_BARRIER();
      const char* src = kb + (size_t)n * 16384 + (n < 32 ? 0 : vdelta);
      if (n >= NA) {
        const int m = n - NA;
        src = vbase + (size_t)((m / NB) * 1024 + (m % NB) * 128 + wave * 64) * 512;
        if (m == 0) {
          STAMP(1);
          half = 16384;
#pragma unroll
          for (int g = 0; g < 4; ++g) vo[g] = (8 * g + (lane >> 3)) * 512 + (((lane & 7) ^ (((lane >> 3) & 2) | ((g & 1) << 2))) << 4) - g * 1024;
        }
      }
      int ld = n >= 2 ? NOL : NOLEAD;
      if (n >= 8) {
        const v2u av = *(const volatile LAS v2u*)L->anchor;
        const unsigned a0 = __builtin_amdgcn_readfirstlane(av[0]), an = __builtin_amdgcn_readfirstlane(av[1]);
        ld = (int)((an ? an + (n - 26) * TAU2 / 10 : a0 + n * TAU1 / 10) - (unsigned)__builtin_amdgcn_s_memrealtime());
        if (!an) { if (!a0) ld = NOL; else if (ld <= 0) ld = min(ld + LAG1, NOLEAD); }
        volatile LAS unsigned* fr = &L->freed[wave][n & 7];
#pragma unroll 1
        for (unsigned spins = 0; spins < (1u << 22); ++spins) {
          if ((int)__builtin_amdgcn_readfirstlane(*fr) >= n - 7) break;
          DRAIN();
          pub = n; *full = pub;
          __builtin_amdgcn_s_sleep(1);
        }
        asm volatile("" ::: "memory");
      }
      unsigned char* dst = sm.ring[wave][n & 7];
      if (ld > 50) { if (ld > L3) issue8<D3>(src, half, dst, vo, n, pub, full); else issue8<8>(src, half, dst, vo, n, pub, full); }
      else if (ld > 0) issue8<10>(src, half, dst, vo, n, pub, full);
      else if (ld > -60 && n < 54) issue8<13>(src, half, dst, vo, n, pub, full);
      else issue8<DMAX>(src, half, dst, vo, n, pub, full);
    }
#pragma unroll
    for (int k = 2; k >= 0; --k) {
      asm volatile("s_waitcnt vmcnt(%0)" ::"i"(8 * k) : "memory");
      *full = max(pub, 64 - k);
    }
    STAMP(2);
    WG_BARRIER();
  } else {
    const GAS unsigned* x32 = (const GAS unsigned*)(x + b * DM);
    const int wv = wave - 2;
    const u64 ep = *(const u64*)(ws + WS_EPOCH + b * 256);
    {
      const int r = lane >> 3;
      const GAS char* wqp = (const GAS char*)wq + (size_t)(2 * r) * (QLD * 2) + (j * 64 + (lane & 7) * 8) * 2;
      v4u wr[10]; unsigned xp[5];
#pragma unroll
      for (int k = 0; k < 5; ++k) {
        const int pg = min(wv + 6 * k, 27);
        xp[k] = x32[8 * pg + r];
        wr[2 * k] = ldw(wqp + (size_t)(16 * pg) * (QLD * 2));
        wr[2 * k + 1] = ldw(wqp + (size_t)(16 * pg + 1) * (QLD * 2));
      }
      if (wave == 2) clear_slots(rs, gid, lane);
      __builtin_amdgcn_sched_barrier(0);
      __builtin_amdgcn_s_barrier();
      asm volatile("" ::: "memory");
      __builtin_amdgcn_sched_barrier(0);
      if (wv >= 4) xp[4] = 0u;
      STAMP(1);
      float qa[8];
#pragma unroll
      for (int c = 0; c < 8; ++c) qa[c] = 0.f;
#pragma unroll
      for (int k = 0; k < 5; ++k) pair_dot(wr[2 * k], wr[2 * k + 1], xp[k], qa);
      L->qpart[wv][(lane & 7) * 8 + r] = tr8(qa, lane);
    }
    u64 tag = (ep + 1) << 16;
    asm volatile("" : "+v"(tag));
    LGKM0();
    unsigned arr = 0;
    if (lane == 0) arr = __hip_atomic_fetch_add(&L->cnt, 1u, __ATOMIC_RELAXED, __HIP_MEMORY_SCOPE_WORKGROUP);
    if (__builtin_amdgcn_readfirstlane(arr) == 5) {
      asm volatile("" ::: "memory");
      float qv = L->qpart[0][lane];
#pragma unroll
      for (int k = 1; k < 6; ++k) qv += L->qpart[k][lane];
      __hip_atomic_store((GAS u64*)(ws + WS_QG) + gid * 64 + lane, tag | (pkbf(qv, 0.f) & 0xffffu), RLX_AGENT);
    }
    STAMP(2);
    if (wave >= 4) {
      const int cw = wave - 4, w = cw & 1, par = cw >> 1;
      volatile LAS unsigned* full = &L->full[w];
      volatile LAS unsigned* fr = L->freed[w];
      const int g4 = lane >> 4, c16 = lane & 15;
      const unsigned ka0 = c16 * 512 + ((g4 ^ ((c16 & 7) << 1)) << 4);
      const unsigned ta0 = (8 * g4 + (c16 >> 2)) * 128 + ((((c16 >> 3) & 1) | ((g4 & 1) << 1)) << 5) + (lane & 3) * 8;
      {
        bf16x8 qf[8];
#pragma unroll 1
        for (int gen = 0; gen < 4; ++gen) {
          bf16x8 kf[4][8];
#pragma unroll
          for (int h = 0; h < 4; ++h) {
            const int n = 8 * gen + 2 * h + par;
            wait_ge(full, n + 1);
            const LAS unsigned char* cell = L->ring[w][2 * h + par];
#pragma unroll
            for (int t = 0; t < 8; ++t) kf[h][t] = *(const LAS bf16x8*)(cell + ((ka0 ^ ((t & 3) << 6)) + (t >> 2) * 256));
            LGKM0(); fr[2 * h + par] = n + 1;
            PIN8(kf[h]);
          }
          if (gen == 0) {
            wait_ge(&L->qready, 1);
            STAMP(3);
#pragma unroll
            for (int t = 0; t < 8; ++t) qf[t] = *(const LAS bf16x8*)(L->q + (lane & 1) * 128 + (4 * t + g4) * 4);
          }
          f32x4 sc[4];
#pragma unroll
          for (int h = 0; h < 4; ++h) sc[h] = f32x4{0.f, 0.f, 0.f, 0.f};
#pragma unroll
          for (int t = 0; t < 8; ++t)
#pragma unroll
            for (int h = 0; h < 4; ++h) sc[h] = __builtin_amdgcn_mfma_f32_16x16x32_bf16(kf[h][t], qf[t], sc[h], 0, 0, 0);
          const f32x4 sv = sel4(sc[0], sc[1], sc[2], sc[3], c16 & 2, c16 & 4);
          *(LAS v2u*)&L->P[gen][c16 & 1][(2 * (c16 & 6) + 2 * par + w) * 8 + 2 * g4] = v2u{pkbf(sv[0], sv[1]), pkbf(sv[2], sv[3])};
          LGKM0(); *(volatile LAS unsigned*)&L->kdone[cw] = gen + 1;
        }
      }
      STAMP(4);
      v4u wf[32];
      const GAS u16* wop = (const GAS u16*)wo + (size_t)(2 * kvh * 256 + 64 * qd + 8 * g4) * DM + min(128 * cw + 8 * c16, DM - 8);
      f32x4 acc0 = {0.f, 0.f, 0.f, 0.f}, acc1 = {0.f, 0.f, 0.f, 0.f};
      float mprev0 = -INFINITY, mprev1 = -INFINITY;
      const int vrow = 4 * g4 + (c16 >> 2);
      const unsigned va0 = vrow * 512 + ((vrow & 7) << 5) + (lane & 3) * 8;
      auto astep = [&](int aa) {
        const int n = 32 - 4 * NB + 2 * aa + par, gen = aa >> 2;
        if ((aa & 3) == 0 || aa == 2 * NB) {
          wait_ge(&L->pready, gen + 1);
          const float m0 = L->mref[gen][0], m1 = L->mref[gen][1];
          const float a0 = __builtin_amdgcn_exp2f(mprev0 - m0), a1 = __builtin_amdgcn_exp2f(mprev1 - m1);
          mprev0 = m0; mprev1 = m1;
          acc0 *= f32x4{a0, a1, a0, a1}; acc1 *= f32x4{a0, a1, a0, a1};
        }
        const v2u pv = *(const LAS v2u*)&L->P[gen][lane & 1][(4 * (aa & 3) + 2 * par + w) * 8 + 2 * g4];
        s16x4 pa[8];
#pragma unroll
        for (int v = 0; v < 8; ++v) { const bool on = (c16 >> 1) == v; pa[v] = __builtin_bit_cast(s16x4, v2u{on ? pv[0] : 0u, on ? pv[1] : 0u}); }
        PIN8(pa);
        wait_ge(full, n + 1);
        const LAS unsigned char* cell = L->ring[w][n & 7];
#pragma unroll
        for (int v = 0; v < 8; ++v) {
          acc0 = __builtin_amdgcn_mfma_f32_16x16x16bf16_1k(pa[v], __builtin_amdgcn_ds_read_tr16_b64_v4i16((LAS s16x4*)(cell + (va0 ^ (v << 5)))), acc0, 0, 0, 0);
          acc1 = __builtin_amdgcn_mfma_f32_16x16x16bf16_1k(pa[v], __builtin_amdgcn_ds_read_tr16_b64_v4i16((LAS s16x4*)(cell + ((va0 ^ (v << 5)) + 256))), acc1, 0, 0, 0);
        }
        asm volatile("" : "+v"(acc0), "+v"(acc1));
        LGKM0(); fr[n & 7] = n + 1;
      };
      constexpr int NWS = (32 + WOB - 1) / WOB;
#pragma unroll
      for (int hb = 0; hb < NWS; ++hb) {
#pragma unroll
        for (int i = WOB * hb; i < WOB * hb + WOB && i < 32; ++i) wf[i] = ldw(wop + (size_t)((i >> 4) * 256 + ((i >> 3) & 1) * 32 + (i & 7)) * DM);
        __builtin_amdgcn_sched_barrier(0);
        astep(2 * NB + hb);
      }
#pragma unroll 1
      for (int aa = 2 * NB + NWS; aa < 16; ++aa) astep(aa);
#pragma unroll
      for (int r = 0; r < 4; ++r) {
        L->mrg[cw][(r & 1) * 256 + (2 * g4 + (r >> 1)) * 16 + c16] = acc0[r];
        L->mrg[cw][(r & 1) * 256 + 128 + (2 * g4 + (r >> 1)) * 16 + c16] = acc1[r];
      }
      LGKM0();
      if (lane == 0) __hip_atomic_fetch_add(&L->acnt, 1u, __ATOMIC_RELAXED, __HIP_MEMORY_SCOPE_WORKGROUP);
      STAMP(7);
      __builtin_amdgcn_sched_barrier(0);
#pragma unroll
      for (int kb = 0; kb < 4; ++kb) {
        v4u tf[8];
#pragma unroll
        for (int e = 0; e < 8; ++e)
#pragma unroll
          for (int d = 0; d < 4; ++d)
            tf[e][d] = (e & 1) ? perm_hi(wf[8 * kb + 2 * d][e >> 1], wf[8 * kb + 2 * d + 1][e >> 1]) : perm_lo(wf[8 * kb + 2 * d][e >> 1], wf[8 * kb + 2 * d + 1][e >> 1]);
        PIN8(tf);
#pragma unroll
        for (int e = 0; e < 8; ++e) wf[8 * kb + e] = tf[e];
      }
      f32x4 acc[4];
#pragma unroll
      for (int t = 0; t < 4; ++t) acc[t] = f32x4{0.f, 0.f, 0.f, 0.f};
      f32x4 o = {0.f, 0.f, 0.f, 0.f};
      wait_ge(&L->pready, 5);
      LAS v2u* obw = (LAS v2u*)&L->ob[cw][(c16 & 1) * 32 + ((c16 >> 1) & 3) * 8 + 2 * g4];
#pragma unroll 1
      for (int mm = 0; mm < 2 * NB; ++mm) {
        const int m = 2 * mm + par, n = NA + m;

        const LAS unsigned* pp = &L->PB[m / NB][lane & 1][(m % NB) * 64 + w * 32 + 4 * g4];
        const bf16x8 p0 = *(const LAS bf16x8*)pp, p1 = *(const LAS bf16x8*)(pp + 16);
        wait_ge(full, n + 1);
        bf16x8 vf[8];
        read_tr(L->ring[w][n & 7], ta0, vf);
#pragma unroll
        for (int t = 0; t < 4; ++t) acc[t] = __builtin_amdgcn_mfma_f32_16x16x32_bf16(vf[t], p0, acc[t], 0, 0, 0);
#pragma unroll
        for (int t = 0; t < 4; ++t) acc[t] = __builtin_amdgcn_mfma_f32_16x16x32_bf16(vf[4 + t], p1, acc[t], 0, 0, 0);
        asm volatile("" : "+v"(acc[0]), "+v"(acc[1]), "+v"(acc[2]), "+v"(acc[3]));
      }
      STAMP(5);
      if (cw == 0) {
        wait_ge(&L->oready, 1);
        if ((c16 & 1) == kh) o = *(const LAS f32x4*)&L->oa[((c16 >> 1) & 3) * 16 + 4 * g4];
      }
      o += sel4(acc[0], acc[1], acc[2], acc[3], c16 & 2, c16 & 4);
      *obw = v2u{pkbf(o[0], o[1]), pkbf(o[2], o[3])};
      const int col = 128 * cw + 8 * c16 + 4 * (g4 & 1);
      const bool st_on = g4 < 2 && col < DM;
      const int yoff = st_on ? WS_Y + gid * (DM * 4) + col * 4 : WS_END;
      const LAS float* ybp = &L->yb[st_on ? col : 0];
      const LAS unsigned* obr = &L->ob[c16 & 3][4 * g4];
      WG_BARRIER();
      STAMP(6);
      bf16x8 of[4];
#pragma unroll
      for (int kb = 0; kb < 4; ++kb) of[kb] = *(const LAS bf16x8*)(obr + 16 * kb);
      const f32x4 ybv = *(const LAS f32x4*)ybp;
      const float g = L->gate;
      __builtin_amdgcn_sched_barrier(0);
      f32x4 ya[8];
#pragma unroll
      for (int e = 0; e < 8; ++e) ya[e] = f32x4{0.f, 0.f, 0.f, 0.f};
#pragma unroll
      for (int kb = 0; kb < 4; ++kb) {
#pragma unroll
        for (int e = 0; e < 8; ++e) ya[e] = __builtin_amdgcn_mfma_f32_16x16x32_bf16(of[kb], __builtin_bit_cast(bf16x8, wf[8 * kb + e]), ya[e], 0, 0, 0);
        __builtin_amdgcn_sched_barrier(0);
      }
      v4u yv;
#pragma unroll
      for (int r = 0; r < 4; ++r) {
        const float lo = (ya[r][0] + ya[r][1]) + (ya[r][2] + ya[r][3]), hi = (ya[4 + r][0] + ya[4 + r][1]) + (ya[4 + r][2] + ya[4 + r][3]);
        yv[r] = san(fmaf(g, (g4 & 1) ? hi : lo, ybv[r]));
      }
      __builtin_amdgcn_raw_buffer_store_b128(yv, rs, yoff, 0, 16);
      STAMP(8);
    } else {
      if (wave == 2) {
        {
          const unsigned thi = (unsigned)(tag >> 32), tlo = (unsigned)tag;
          int off[4];
#pragma unroll
          for (int k = 0; k < 4; ++k) off[k] = WS_QG + (gid & ~7) * 512 + k * 1024 + lane * 16;
          __builtin_amdgcn_s_sleep(80);
#pragma unroll 1
          for (unsigned spins = 0; spins < SPIN_CAP; ++spins) {
            v4u g[4];
            bool ok = true;
#pragma unroll
            for (int k = 0; k < 4; ++k) g[k] = __builtin_amdgcn_raw_buffer_load_b128(rs, off[k], 0, 16);
#pragma unroll
            for (int k = 0; k < 4; ++k) {
              if (off[k] != WS_END && g[k][1] == thi && g[k][3] == thi && (g[k][0] & 0xffff0000u) == tlo && (g[k][2] & 0xffff0000u) == tlo) {
                L->q[k * 64 + lane] = perm_lo(g[k][0], g[k][2]);
                off[k] = WS_END;
              }
              ok &= off[k] == WS_END;
            }
            if (__all(ok)) break;
            __builtin_amdgcn_s_sleep(16);
          }
          LGKM0();
          *(volatile LAS unsigned*)&L->qready = 1u;
        }
        STAMP(3);
        constexpr float C2 = 0.0625f * 1.4426950408889634f;
        float mrun = -INFINITY, lrun = 0.f;
#pragma unroll 1
        for (int gen = 0; gen < 4; ++gen) {
#pragma unroll
          for (int c = 0; c < 4; ++c) wait_ge(&L->kdone[c], gen + 1);
          v4u g = *(const LAS v4u*)&L->P[gen][0][lane * 4];
          if (gen == 0) {
            __builtin_amdgcn_raw_buffer_store_b128(g, rs, WS_S + gid * 1024 + lane * 16, 0, 16);
          }
          if (gen == 2) {
            DRAIN();
            if (lane == 0) __hip_atomic_store((GAS u64*)(ws + WS_RDY) + gid, tag | ((unsigned)__builtin_amdgcn_s_memrealtime() & 0xffffu), RLX_AGENT);
          }
          float cm = -INFINITY;
#pragma unroll
          for (int e = 0; e < 4; ++e) cm = fmaxf(cm, fmaxf(bflo(g[e]), bfhi(g[e])));
          const float mn = fmaxf(mrun, max32(cm) * C2);
          float ls = 0.f;
#pragma unroll
          for (int e = 0; e < 4; ++e) {
            const float pl = __builtin_amdgcn_exp2f(fmaf(bflo(g[e]), C2, -mn)), ph = __builtin_amdgcn_exp2f(fmaf(bfhi(g[e]), C2, -mn));
            ls += pl + ph;
            g[e] = pkbf(pl, ph);
          }
          *(LAS v4u*)&L->P[gen][0][lane * 4] = g;
          lrun = fmaf(lrun, __builtin_amdgcn_exp2f(mrun - mn), ls);
          mrun = mn;
          if ((lane & 31) == 0) L->mref[gen][lane >> 5] = mn;
          LGKM0();
          *(volatile LAS unsigned*)&L->pready = gen + 1;
        }
        lrun += dpp<XOR1>(lrun); lrun += dpp<XOR2>(lrun); lrun += dpp<ROR4>(lrun); lrun += dpp<XOR8>(lrun); lrun = swp16_add(lrun, lrun);
        if ((lane & 31) == 0) __builtin_amdgcn_raw_buffer_store_b64(v2u{san(mrun), san(lrun)}, rs, WS_ST + gid * 128 + (lane >> 5) * 8, 0, 16);
        u64 gv;
#pragma unroll 1
        for (unsigned spins = 0; spins < SPIN_CAP; ++spins) {
          gv = __hip_atomic_load((GAS u64*)(ws + WS_RDY) + b * 64 + lane, RLX_AGENT);
          if (__all((gv & ~0xffffull) == tag)) break;
          __builtin_amdgcn_s_sleep(32);
        }
        *(volatile LAS unsigned*)&L->anchor[1] = (mean_time((unsigned)gv, true) + TOFF2 - (qd == 3 ? Q3LEAD : 0)) | 1u;
        if (j == 0 && lane == 0) __hip_atomic_store((GAS u64*)(ws + WS_EPOCH + b * 256), ep + 1, RLX_AGENT);
        asm volatile("" ::: "memory");
        v4u h[4];
#pragma unroll
        for (int k = 0; k < 4; ++k) h[k] = __builtin_amdgcn_raw_buffer_load_b128(rs, WS_S + ((gid & ~3) + k) * 1024 + lane * 16, 0, 16);
        v2u sv;
#pragma unroll 1
        for (unsigned spins = 0; spins < SPIN_CAP; ++spins) {
          __builtin_amdgcn_s_sleep(127);
          sv = __builtin_amdgcn_raw_buffer_load_b64(rs, WS_ST + ((gid & ~7) + (lane & 7)) * 128 + (lane >> 5) * 8, 0, 16);
          if (__all(sv[0] != SENT && sv[1] != SENT)) break;
        }
        const float mp = __uint_as_float(sv[0]);
        float M = fmaxf(mp, dpp<XOR1>(mp)); M = fmaxf(M, dpp<XOR2>(M)); M = fmaxf(M, dpp<XOR7>(M));
        float Ls = __uint_as_float(sv[1]) * __builtin_amdgcn_exp2f(mp - M);
        Ls += dpp<XOR1>(Ls); Ls += dpp<XOR2>(Ls); Ls += dpp<XOR7>(Ls);
        const float inv = 1.f / Ls;
#pragma unroll
        for (int k = 0; k < 4; ++k) {
#pragma unroll
          for (int e = 0; e < 4; ++e)
            h[k][e] = pkbf(inv * __builtin_amdgcn_exp2f(fmaf(bflo(h[k][e]), C2, -M)), inv * __builtin_amdgcn_exp2f(fmaf(bfhi(h[k][e]), C2, -M)));
          *(LAS v4u*)&L->PB[k][0][lane * 4] = h[k];
        }
        LGKM0();
        *(volatile LAS unsigned*)&L->pready = 5u;
        const float as = inv * __builtin_amdgcn_exp2f(mrun - M);
        STAMP(4);
        wait_ge(&L->acnt, 4);
        f32x4 o0 = *(const LAS f32x4*)(L->mrg[0] + lane * 8), o1 = *(const LAS f32x4*)(L->mrg[0] + lane * 8 + 4);
#pragma unroll
        for (int ww = 1; ww < 4; ++ww) { o0 += *(const LAS f32x4*)(L->mrg[ww] + lane * 8); o1 += *(const LAS f32x4*)(L->mrg[ww] + lane * 8 + 4); }
        o0 *= as; o1 *= as;
        v4u ov = {pkbf(o0[0], o0[1]), pkbf(o0[2], o0[3]), pkbf(o1[0], o1[1]), pkbf(o1[2], o1[3])};
#pragma unroll
        for (int k = 0; k < 4; ++k) ov[k] = ov[k] == SENT ? 0x7FC07FC0u : ov[k];
        __builtin_amdgcn_raw_buffer_store_b128(ov, rs, WS_PO + gid * 1024 + lane * 16, 0, 16);
        STAMP(5);
        int off = WS_PO + ((gid & ~7) + (lane >> 3)) * 1024 + s * 128 + (lane & 7) * 16;
        __builtin_amdgcn_s_sleep(20);
#pragma unroll 1
        for (unsigned spins = 0; spins < SPIN_CAP; ++spins) {
          if (off != WS_END) {
            ov = __builtin_amdgcn_raw_buffer_load_b128(rs, off, 0, 16);
            if (max(max(ov[0], ov[1]), max(ov[2], ov[3])) != SENT) off = WS_END;
          }
          if (__all(off == WS_END)) break;
          __builtin_amdgcn_s_sleep(2);
        }
        STAMP(6);
        float oe[8];
#pragma unroll
        for (int k = 0; k < 4; ++k) { oe[2 * k] = bflo(ov[k]); oe[2 * k + 1] = bfhi(ov[k]); }
        L->oa[8 * (lane & 7) + (lane >> 3)] = tr8(oe, lane);
        LGKM0();
        *(volatile LAS unsigned*)&L->oready = 1u;
        STAMP(7);
      } else {
        wait_ge(&L->full[0], 3);
        if (lane == 0) __hip_atomic_store((GAS u64*)(ws + WS_TS) + gid, tag | ((unsigned)__builtin_amdgcn_s_memrealtime() & 0xffffu), RLX_AGENT);
#pragma unroll 1
        for (int k = 0; k < NRETRY; ++k) {
          wait_ge(&L->full[0], NTS + 3 * k);
          const u64 tv = __hip_atomic_load((GAS u64*)(ws + WS_TS) + b * 64 + lane, RLX_AGENT);
          const bool ok = (tv & ~0xffffull) == tag;
          *(volatile LAS unsigned*)&L->anchor[0] = (mean_time((unsigned)tv, ok) + TOFF1 - (qd == 3 ? Q3LEAD : 0)) | 1u;
          if (__all(ok)) break;
        }
        const int cl = lane < 56 ? lane : 55;
        float z = 0.f;
#pragma unroll
        for (int k = 0; k < 4; ++k) {
          const int idx = lane + 64 * k;
          const bool in = idx < DM / 2;
          const unsigned xv = x32[in ? idx : 0], gv = ((const GAS unsigned*)gw)[in ? idx : 0];
          const float t = rbf(bflo(xv) * bflo(gv)) + rbf(bfhi(xv) * bfhi(gv));
          z += in ? t : 0.f;
        }
        z = swp32_add(z, z); z = swp16_add(z, z); z += dpp<XOR8>(z); z += dpp<XOR7>(z); z += dpp<XOR2>(z); z += dpp<XOR1>(z);
        const float g = rbf(1.f / (1.f + __expf(-rbf(z))));
        if (lane == 0) L->gate = g;
        float yb[8];
#pragma unroll
        for (int c = 0; c < 8; ++c) yb[c] = 0.f;
        if (s != 3) {
          const int u0 = (s == 7 ? 48 + kvh : kvh * 6 + 2 * qd + kh) * 32, rr = lane >> 2;
          float ha[8];
#pragma unroll
          for (int c = 0; c < 8; ++c) ha[c] = 0.f;
          const GAS u16* w1p = (const GAS u16*)w1 + (size_t)(2 * rr) * DFF + u0 + (lane & 3) * 8;
#pragma unroll 1
          for (int h = 0; h < 14 / MB1; ++h) {
            v4u ma[2 * MB1]; unsigned xq[MB1];
#pragma unroll
            for (int k = 0; k < MB1; ++k) {
              ma[2 * k] = ldw(w1p + (size_t)((MB1 * h + k) * 32) * DFF);
              ma[2 * k + 1] = ldw(w1p + (size_t)((MB1 * h + k) * 32 + 1) * DFF);
              xq[k] = x32[(MB1 * h + k) * 16 + rr];
            }
#pragma unroll
            for (int k = 0; k < MB1; ++k) pair_dot(ma[2 * k], ma[2 * k + 1], xq[k], ha);
          }
          float hv = tr8(ha, lane);
          hv += dpp<SHR4>(hv);
          hv = fmaxf(rbf(hv), 0.f);
#pragma unroll 1
          for (int c4 = 0; c4 < 32 / MB2; ++c4) {
            v4u w2r[MB2];
#pragma unroll
            for (int u = 0; u < MB2; ++u) w2r[u] = ldw((const GAS u16*)w2 + (size_t)(u0 + c4 * MB2 + u) * DM + cl * 8);
#pragma unroll
            for (int u = 0; u < MB2; ++u) {
              const int un = c4 * MB2 + u;
              const float hu = __builtin_bit_cast(float, __builtin_amdgcn_readlane(__builtin_bit_cast(int, hv), (un & 7) * 8 + 4 + (un >> 3)));
#pragma unroll
              for (int c = 0; c < 4; ++c) { yb[2 * c] = fmaf(hu, bflo(w2r[u][c]), yb[2 * c]); yb[2 * c + 1] = fmaf(hu, bfhi(w2r[u][c]), yb[2 * c + 1]); }
            }
          }
        }
        const float omg = rbf(1.f - g);
        if (lane < 56) {
          *(LAS f32x4*)(L->yb + lane * 8) = f32x4{omg * yb[0], omg * yb[1], omg * yb[2], omg * yb[3]};
          *(LAS f32x4*)(L->yb + lane * 8 + 4) = f32x4{omg * yb[4], omg * yb[5], omg * yb[6], omg * yb[7]};
        }
      }
      WG_BARRIER();
    }
  }
  if (j < 14) {
    const v4u y = poll16(srd, WS_Y + (b * 64 + wave * 8 + (lane >> 3)) * (DM * 4) + j * 128 + (lane & 7) * 16);
    STAMP(9);
    const float r0 = swp32_add(__uint_as_float(y[0]), __uint_as_float(y[2])), r1 = swp32_add(__uint_as_float(y[1]), __uint_as_float(y[3]));
    float r = swp16_add(r0, r1);
    r += dpp<XOR8>(r);
    if (!(lane & 8)) L->fin[wave][(lane & 7) * 4 + (lane >> 4)] = r;
    WG_BARRIER();
    if (tid < 32) {
      const float t = ((L->fin[0][tid] + L->fin[1][tid]) + (L->fin[2][tid] + L->fin[3][tid])) + ((L->fin[4][tid] + L->fin[5][tid]) + (L->fin[6][tid] + L->fin[7][tid]));
      out[b * DM + j * 32 + tid] = (u16)pkbf(t, 0.f);
    }
  }
  STAMP_OUT();
  DRAIN();
}

extern "C" void kernel_launch(void* const* d_in, const int* in_sizes, int n_in, void* d_out, int out_size, void* d_ws, size_t ws_size, hipStream_t stream) {
  fused_kernel<<<256, 512, 0, stream>>>((const u16*)d_in[0], (const u16*)d_in[1], (const u16*)d_in[2], (const u16*)d_in[3], (const u16*)d_in[4],
                                        (const u16*)d_in[6], (const u16*)d_in[7], (const u16*)d_in[8], (unsigned char*)d_ws, (u16*)d_out);
}
```

```cpp
#include <hip/hip_runtime.h>
#include <hip/hip_bf16.h>
#include <cstdint>

constexpr int NB = 2;
constexpr int WOB = 4;
constexpr int MB2 = 4;
constexpr int TAU1 = 580, TOFF1 = -330, LAG1 = 40, TAU2 = 560, TOFF2 = 30, Q3LEAD = 60;

typedef unsigned v4u __attribute__((ext_vector_type(4)));
typedef unsigned v2u __attribute__((ext_vector_type(2)));
typedef float f32x4 __attribute__((ext_vector_type(4)));
typedef __bf16 bf16x2 __attribute__((ext_vector_type(2)));
typedef __bf16 bf16x8 __attribute__((ext_vector_type(8)));
typedef short s16x4 __attribute__((ext_vector_type(4)));
typedef unsigned short u16;
typedef unsigned long long u64;
#define GAS __attribute__((address_space(1)))
#define LAS __attribute__((address_space(3)))
#define RLX_AGENT __ATOMIC_RELAXED, __HIP_MEMORY_SCOPE_AGENT

constexpr int DM = 448, SK = 8192, DH = 256, DFF = 1792, QLD = 4096;
constexpr unsigned SENT = 0xFFFFFFFFu;
constexpr unsigned SPIN_CAP = 1u << 18;
constexpr int WS_EPOCH = 0;
constexpr int WS_RDY = 4096;
constexpr int WS_TS = 6144;
constexpr int WS_QG = 8192;
constexpr int WS_S = WS_QG + 256 * 64 * 8;
constexpr int WS_ST = WS_S + 256 * 1024;
constexpr int WS_PO = WS_ST + 256 * 128;
constexpr int WS_Y = WS_PO + 256 * 1024;
constexpr int WS_END = WS_Y + 256 * DM * 4;

__device__ __forceinline__ float bflo(unsigned u) { return __builtin_bit_cast(float, u << 16); }
__device__ __forceinline__ float bfhi(unsigned u) { return __builtin_bit_cast(float, u & 0xffff0000u); }
__device__ __forceinline__ unsigned pkbf(float lo, float hi) {
  bf16x2 r; r[0] = (__bf16)lo; r[1] = (__bf16)hi; return __builtin_bit_cast(unsigned, r);
}
__device__ __forceinline__ float rbf(float f) { return bflo(pkbf(f, 0.f)); }
__device__ __forceinline__ float dot2(unsigned a, unsigned b, float acc) {
  return __builtin_amdgcn_fdot2_f32_bf16(__builtin_bit_cast(bf16x2, a), __builtin_bit_cast(bf16x2, b), acc, false);
}
__device__ __forceinline__ unsigned perm_lo(unsigned a, unsigned b) { return __builtin_amdgcn_perm(b, a, 0x05040100u); }
__device__ __forceinline__ unsigned perm_hi(unsigned a, unsigned b) { return __builtin_amdgcn_perm(b, a, 0x07060302u); }
template <int CTRL> __device__ __forceinline__ float dpp(float x) {
  return __builtin_bit_cast(float, __builtin_amdgcn_mov_dpp(__builtin_bit_cast(int, x), CTRL, 0xf, 0xf, true));
}
constexpr int XOR1 = 0xB1, XOR2 = 0x4E, XOR7 = 0x141, XOR8 = 0x128, ROR4 = 0x124, SHR4 = 0x114;
__device__ __forceinline__ float swp16_add(float a, float b) {
  auto s = __builtin_amdgcn_permlane16_swap(__float_as_uint(a), __float_as_uint(b), false, false);
  return __uint_as_float(s[0]) + __uint_as_float(s[1]);
}
__device__ __forceinline__ float swp32_add(float a, float b) {
  auto s = __builtin_amdgcn_permlane32_swap(__float_as_uint(a), __float_as_uint(b), false, false);
  return __uint_as_float(s[0]) + __uint_as_float(s[1]);
}
__device__ __forceinline__ float swp16_max(float a) {
  auto s = __builtin_amdgcn_permlane16_swap(__float_as_uint(a), __float_as_uint(a), false, false);
  return fmaxf(__uint_as_float(s[0]), __uint_as_float(s[1]));
}
template <int CTRL> __device__ __forceinline__ float tr_dpp(float lo, float hi, bool sel) {
  const float t1 = lo + dpp<CTRL>(lo), t2 = hi + dpp<CTRL>(hi);
  return sel ? t2 : t1;
}
__device__ __forceinline__ float tr8(float (&a)[8], int lane) {
#pragma unroll
  for (int i = 0; i < 4; ++i) a[i] = swp32_add(a[i], a[i + 4]);
#pragma unroll
  for (int i = 0; i < 2; ++i) a[i] = swp16_add(a[i], a[i + 2]);
  return tr_dpp<XOR8>(a[0], a[1], lane & 8);
}
__device__ __forceinline__ float max32(float v) {
  v = fmaxf(v, dpp<XOR1>(v)); v = fmaxf(v, dpp<XOR2>(v)); v = fmaxf(v, dpp<ROR4>(v)); v = fmaxf(v, dpp<XOR8>(v));
  return swp16_max(v);
}
__device__ __forceinline__ void pair_dot(const v4u& ra, const v4u& rb, unsigned xp, float (&a)[8]) {
#pragma unroll
  for (int c = 0; c < 4; ++c) {
    a[2 * c] = dot2(perm_lo(ra[c], rb[c]), xp, a[2 * c]);
    a[2 * c + 1] = dot2(perm_hi(ra[c], rb[c]), xp, a[2 * c + 1]);
  }
}
__device__ __forceinline__ unsigned san(float f) { const unsigned u = __float_as_uint(f); return u == SENT ? 0x7FC00000u : u; }
__device__ __forceinline__ v4u ldw(const GAS void* p) {
  return *(const GAS v4u*)p;
}
#define WG_BARRIER() do { asm volatile("s_waitcnt lgkmcnt(0)" ::: "memory"); __builtin_amdgcn_s_barrier(); asm volatile("" ::: "memory"); __builtin_amdgcn_sched_barrier(0); } while (0)
#define DRAIN() asm volatile("s_waitcnt vmcnt(0)" ::: "memory")

struct Lds {
  unsigned char ring[2][8][8192];
  unsigned P[4][2][128];
  unsigned PB[4][2][128];
  float mrg[4][512];
  float oa[64];
  unsigned q[256];
  float yb[DM];
  float qpart[6][64];
  float fin[8][32];
  unsigned ob[4][68];
  float mref[4][2];
  float gate, pad0[3];
  unsigned freed[2][8];
  unsigned full[2];
  unsigned cnt, qready;
  unsigned kdone[4];
  unsigned pready, acnt, oready, pad1;
  unsigned anchor[2];
};

#define PIN8(a) asm volatile("" : "+v"(a[0]), "+v"(a[1]), "+v"(a[2]), "+v"(a[3]), "+v"(a[4]), "+v"(a[5]), "+v"(a[6]), "+v"(a[7]))
#define LGKM0() asm volatile("s_waitcnt lgkmcnt(0)" ::: "memory")

__device__ __forceinline__ void wait_ge(volatile LAS unsigned* w, unsigned need) {
#pragma unroll 1
  for (unsigned spins = 0; spins < (1u << 22); ++spins) {
    if ((unsigned)__builtin_amdgcn_readfirstlane(*w) >= need) break;
    __builtin_amdgcn_s_sleep(1);
  }
  asm volatile("" ::: "memory");
}

template <int SLEEP>
__device__ __forceinline__ v4u poll16(__amdgpu_buffer_rsrc_t rs, int off) {
  v4u v = {0u, 0u, 0u, 0u};
#pragma unroll 1
  for (unsigned spins = 0; spins < SPIN_CAP; ++spins) {
    if (off != WS_END) {
      v = __builtin_amdgcn_raw_buffer_load_b128(rs, off, 0, 16);
      if (max(max(v[0], v[1]), max(v[2], v[3])) != SENT) off = WS_END;
    }
    if (__all(off == WS_END)) break;
    __builtin_amdgcn_s_sleep(SLEEP);
  }
  return v;
}

__device__ __forceinline__ void clear_slots(__amdgpu_buffer_rsrc_t rs, int gid, int lane) {
  const v4u sv = {SENT, SENT, SENT, SENT};
  __builtin_amdgcn_raw_buffer_store_b128(sv, rs, WS_PO + gid * 1024 + lane * 16, 0, 16);
  __builtin_amdgcn_raw_buffer_store_b128(sv, rs, WS_Y + gid * (DM * 4) + lane * 16, 0, 16);
  __builtin_amdgcn_raw_buffer_store_b128(sv, rs, lane < 48 ? WS_Y + gid * (DM * 4) + 1024 + lane * 16 : (lane == 48 ? WS_ST + gid * 128 : WS_END), 0, 16);
}
template <int D>
__device__ __forceinline__ void issue8(const char* src, int half, unsigned char* dst, const unsigned (&voff)[4], int n, int& pub, volatile LAS unsigned* full) {
#pragma unroll
  for (int g = 0; g < 8; ++g) {
    asm volatile("s_waitcnt vmcnt(%0)" ::"i"(D - 1) : "memory");
    if (g == (D - 1) % 8) { pub = max(pub, n - (D - 1) / 8); *full = pub; }
    const char* sp = src + (g >> 2) * half;
    if ((g & 3) == 0) __builtin_amdgcn_global_load_lds((const unsigned*)(sp + voff[0]), (unsigned*)(dst + (g & 4) * 1024), 16, 0, 2);
    if ((g & 3) == 1) __builtin_amdgcn_global_load_lds((const unsigned*)(sp + voff[1]), (unsigned*)(dst + (g & 4) * 1024), 16, 1024, 2);
    if ((g & 3) == 2) __builtin_amdgcn_global_load_lds((const unsigned*)(sp + voff[2]), (unsigned*)(dst + (g & 4) * 1024), 16, 2048, 2);
    if ((g & 3) == 3) __builtin_amdgcn_global_load_lds((const unsigned*)(sp + voff[3]), (unsigned*)(dst + (g & 4) * 1024), 16, 3072, 2);
  }
}
__device__ __forceinline__ unsigned mean_time(unsigned stamp, bool ok) {
  const unsigned now = (unsigned)__builtin_amdgcn_s_memrealtime();
  float d = ok ? (float)((now - stamp) & 0xffffu) : 0.f;
  d = swp32_add(d, d); d = swp16_add(d, d); d += dpp<XOR8>(d); d += dpp<XOR7>(d); d += dpp<XOR2>(d); d += dpp<XOR1>(d);
  return now - (unsigned)(d * (1.f / 64.f));
}
__device__ __forceinline__ void read_tr(const LAS unsigned char* cell, unsigned a0, bf16x8* f) {
#pragma unroll
  for (int kb = 0; kb < 2; ++kb)
#pragma unroll
    for (int t = 0; t < 4; ++t) {
      const s16x4 lo = __builtin_amdgcn_ds_read_tr16_b64_v4i16((LAS s16x4*)(cell + ((a0 ^ (t << 5)) + kb * 4096)));
      const s16x4 hi = __builtin_amdgcn_ds_read_tr16_b64_v4i16((LAS s16x4*)(cell + ((a0 ^ (t << 5)) + kb * 4096 + 512)));
      f[kb * 4 + t] = __builtin_bit_cast(bf16x8, __builtin_shufflevector(lo, hi, 0, 1, 2, 3, 4, 5, 6, 7));
    }
}
__device__ __forceinline__ f32x4 sel4(f32x4 a0, f32x4 a1, f32x4 a2, f32x4 a3, bool b0, bool b1) {
  f32x4 r;
#pragma unroll
  for (int e = 0; e < 4; ++e) { const float lo = b0 ? a1[e] : a0[e], hi = b0 ? a3[e] : a2[e]; r[e] = b1 ? hi : lo; }
  return r;
}

__global__ void __launch_bounds__(512, 2) fused_kernel(const u16* __restrict__ x, const u16* __restrict__ Kc, const u16* __restrict__ Vc,
    const u16* __restrict__ wq, const u16* __restrict__ wo, const u16* __restrict__ w1, const u16* __restrict__ w2,
    const u16* __restrict__ gw, unsigned char* ws, u16* out) {
  __shared__ __attribute__((aligned(16))) Lds sm;
  LAS Lds* L = (LAS Lds*)&sm;
  asm volatile("" ::"s"(x), "s"(Kc), "s"(Vc), "s"(wq), "s"(wo), "s"(w1), "s"(w2), "s"(gw), "s"(ws), "s"(out));
  const int tid = threadIdx.x, lane = tid & 63, wave = __builtin_amdgcn_readfirstlane(tid >> 6);
  const int blk = blockIdx.x, b = blk >> 6;
  const int kvh = ((blk >> 3) & 6) | (((blk >> 2) ^ (blk >> 4)) & 1), kh = (blk >> 3) & 1, qd = (blk + (blk >> 4)) & 3;
  const int s = kh * 4 + qd;
  const int j = kvh * 8 + s;
  const int gid = b * 64 + j;
  const __amdgpu_buffer_rsrc_t rs = __builtin_amdgcn_make_buffer_rsrc((void*)ws, 0, WS_END, 0x00020000);
  constexpr int NA = 64 - 4 * NB;
  const char* vbase = (const char*)Vc + ((size_t)((b * 8 + kvh) * SK + kh * 4096)) * (DH * 2) + qd * 128;
  if (wave < 2) {
    __builtin_amdgcn_s_setprio(3);
    if (tid < 30) L->freed[0][tid] = 0u;
    volatile LAS unsigned* full = &L->full[wave];
    const char* kb = (const char*)Kc + ((size_t)((b * 8 + kvh) * SK + s * 1024 + wave * 16)) * (DH * 2);
    unsigned vo[4];
#pragma unroll
    for (int g = 0; g < 4; ++g) vo[g] = (lane >> 5) * 512 + (((lane & 31) ^ (((2 * g + (lane >> 5)) & 7) << 1)) << 4);
    int pub = 0;
    const long vdelta = (const char*)Vc - (const char*)Kc - (32 - 4 * NB) * 16384;
    int half = 4096;
#pragma unroll 1
    for (int n = 0; n < 64; ++n) {
      if (n == 1) WG_BARRIER();
      const char* src = kb + (size_t)n * 16384 + (n < 32 ? 0 : vdelta);
      if (n >= NA) {
        const int m = n - NA;
        src = vbase + (size_t)((m / NB) * 1024 + (m % NB) * 128 + wave * 64) * 512;
        if (m == 0) {
          half = 16384;
#pragma unroll
          for (int g = 0; g < 4; ++g) vo[g] = (8 * g + (lane >> 3)) * 512 + (((lane & 7) ^ (((lane >> 3) & 2) | ((g & 1) << 2))) << 4) - g * 1024;
        }
      }
      int ld = -1;
      if (n >= 8) {
        const v2u av = *(const volatile LAS v2u*)L->anchor;
        const unsigned a0 = __builtin_amdgcn_readfirstlane(av[0]), an = __builtin_amdgcn_readfirstlane(av[1]);
        ld = (int)((an ? an + (n - 26) * TAU2 / 10 : a0 + n * TAU1 / 10) - (unsigned)__builtin_amdgcn_s_memrealtime());
        if (!an) { if (!a0) ld = -1; else if (ld <= 0) ld = min(ld + LAG1, -1); }
        volatile LAS unsigned* fr = &L->freed[wave][n & 7];
#pragma unroll 1
        for (unsigned spins = 0; spins < (1u << 22); ++spins) {
          if ((int)__builtin_amdgcn_readfirstlane(*fr) >= n - 7) break;
          DRAIN();
          pub = n; *full = pub;
          __builtin_amdgcn_s_sleep(1);
        }
        asm volatile("" ::: "memory");
      }
      unsigned char* dst = sm.ring[wave][n & 7];
      if (ld > 50) { if (ld > 100) issue8<5>(src, half, dst, vo, n, pub, full); else issue8<8>(src, half, dst, vo, n, pub, full); }
      else if (ld > 0) issue8<10>(src, half, dst, vo, n, pub, full);
      else if (ld > -60 && n < 54) issue8<13>(src, half, dst, vo, n, pub, full);
      else issue8<20>(src, half, dst, vo, n, pub, full);
    }
#pragma unroll
    for (int k = 2; k >= 0; --k) {
      asm volatile("s_waitcnt vmcnt(%0)" ::"i"(8 * k) : "memory");
      *full = max(pub, 64 - k);
    }
    WG_BARRIER();
  } else {
    const GAS unsigned* x32 = (const GAS unsigned*)(x + b * DM);
    const int wv = wave - 2;
    const u64 ep = __hip_atomic_load((GAS u64*)(ws + WS_EPOCH + b * 256), RLX_AGENT);
    {
      const int r = lane >> 3;
      const GAS char* wqp = (const GAS char*)wq + (size_t)(2 * r) * (QLD * 2) + (j * 64 + (lane & 7) * 8) * 2;
      v4u wr[10]; unsigned xp[5];
#pragma unroll
      for (int k = 0; k < 5; ++k) {
        const int pg = min(wv + 6 * k, 27);
        xp[k] = x32[8 * pg + r];
        wr[2 * k] = ldw(wqp + (size_t)(16 * pg) * (QLD * 2));
        wr[2 * k + 1] = ldw(wqp + (size_t)(16 * pg + 1) * (QLD * 2));
      }
      if (wave == 2) clear_slots(rs, gid, lane);
      __builtin_amdgcn_sched_barrier(0);
      __builtin_amdgcn_s_barrier();
      asm volatile("" ::: "memory");
      __builtin_amdgcn_sched_barrier(0);
      if (wv >= 4) xp[4] = 0u;
      float qa[8];
#pragma unroll
      for (int c = 0; c < 8; ++c) qa[c] = 0.f;
#pragma unroll
      for (int k = 0; k < 5; ++k) pair_dot(wr[2 * k], wr[2 * k + 1], xp[k], qa);
      L->qpart[wv][(lane & 7) * 8 + r] = tr8(qa, lane);
    }
    u64 tag = (ep + 1) << 16;
    asm volatile("" : "+v"(tag));
    LGKM0();
    unsigned arr = 0;
    if (lane == 0) arr = __hip_atomic_fetch_add(&L->cnt, 1u, __ATOMIC_RELAXED, __HIP_MEMORY_SCOPE_WORKGROUP);
    if (__builtin_amdgcn_readfirstlane(arr) == 5) {
      asm volatile("" ::: "memory");
      float qv = L->qpart[0][lane];
#pragma unroll
      for (int k = 1; k < 6; ++k) qv += L->qpart[k][lane];
      __hip_atomic_store((GAS u64*)(ws + WS_QG) + gid * 64 + lane, tag | (pkbf(qv, 0.f) & 0xffffu), RLX_AGENT);
    }
    if (wave >= 4) {
      const int cw = wave - 4, w = cw & 1, par = cw >> 1;
      volatile LAS unsigned* full = &L->full[w];
      volatile LAS unsigned* fr = L->freed[w];
      const int g4 = lane >> 4, c16 = lane & 15;
      const unsigned ka0 = c16 * 512 + ((g4 ^ ((c16 & 7) << 1)) << 4);
      const unsigned ta0 = (8 * g4 + (c16 >> 2)) * 128 + ((((c16 >> 3) & 1) | ((g4 & 1) << 1)) << 5) + (lane & 3) * 8;
      {
        bf16x8 qf[8];
#pragma unroll 1
        for (int gen = 0; gen < 4; ++gen) {
          bf16x8 kf[4][8];
#pragma unroll
          for (int h = 0; h < 4; ++h) {
            const int n = 8 * gen + 2 * h + par;
            wait_ge(full, n + 1);
            const LAS unsigned char* cell = L->ring[w][2 * h + par];
#pragma unroll
            for (int t = 0; t < 8; ++t) kf[h][t] = *(const LAS bf16x8*)(cell + ((ka0 ^ ((t & 3) << 6)) + (t >> 2) * 256));
            LGKM0(); fr[2 * h + par] = n + 1;
            PIN8(kf[h]);
          }
          if (gen == 0) {
            wait_ge(&L->qready, 1);
#pragma unroll
            for (int t = 0; t < 8; ++t) qf[t] = *(const LAS bf16x8*)(L->q + (lane & 1) * 128 + (4 * t + g4) * 4);
          }
          f32x4 sc[4];
#pragma unroll
          for (int h = 0; h < 4; ++h) sc[h] = f32x4{0.f, 0.f, 0.f, 0.f};
#pragma unroll
          for (int t = 0; t < 8; ++t)
#pragma unroll
            for (int h = 0; h < 4; ++h) sc[h] = __builtin_amdgcn_mfma_f32_16x16x32_bf16(kf[h][t], qf[t], sc[h], 0, 0, 0);
          const f32x4 sv = sel4(sc[0], sc[1], sc[2], sc[3], c16 & 2, c16 & 4);
          *(LAS v2u*)&L->P[gen][c16 & 1][(2 * (c16 & 6) + 2 * par + w) * 8 + 2 * g4] = v2u{pkbf(sv[0], sv[1]), pkbf(sv[2], sv[3])};
          LGKM0(); *(volatile LAS unsigned*)&L->kdone[cw] = gen + 1;
        }
      }
      v4u wf[32];
      const GAS u16* wop = (const GAS u16*)wo + (size_t)(2 * kvh * 256 + 64 * qd + 8 * g4) * DM + min(128 * cw + 8 * c16, DM - 8);
      f32x4 acc0 = {0.f, 0.f, 0.f, 0.f}, acc1 = {0.f, 0.f, 0.f, 0.f};
      float mprev0 = -INFINITY, mprev1 = -INFINITY;
      const int vrow = 4 * g4 + (c16 >> 2);
      const unsigned va0 = vrow * 512 + ((vrow & 7) << 5) + (lane & 3) * 8;
      auto astep = [&](int aa) {
        const int n = 32 - 4 * NB + 2 * aa + par, gen = aa >> 2;
        if ((aa & 3) == 0 || aa == 2 * NB) {
          wait_ge(&L->pready, gen + 1);
          const float m0 = L->mref[gen][0], m1 = L->mref[gen][1];
          const float a0 = __builtin_amdgcn_exp2f(mprev0 - m0), a1 = __builtin_amdgcn_exp2f(mprev1 - m1);
          mprev0 = m0; mprev1 = m1;
          acc0 *= f32x4{a0, a1, a0, a1}; acc1 *= f32x4{a0, a1, a0, a1};
        }
        const v2u pv = *(const LAS v2u*)&L->P[gen][lane & 1][(4 * (aa & 3) + 2 * par + w) * 8 + 2 * g4];
        s16x4 pa[8];
#pragma unroll
        for (int v = 0; v < 8; ++v) { const bool on = (c16 >> 1) == v; pa[v] = __builtin_bit_cast(s16x4, v2u{on ? pv[0] : 0u, on ? pv[1] : 0u}); }
        PIN8(pa);
        wait_ge(full, n + 1);
        const LAS unsigned char* cell = L->ring[w][n & 7];
#pragma unroll
        for (int v = 0; v < 8; ++v) {
          acc0 = __builtin_amdgcn_mfma_f32_16x16x16bf16_1k(pa[v], __builtin_amdgcn_ds_read_tr16_b64_v4i16((LAS s16x4*)(cell + (va0 ^ (v << 5)))), acc0, 0, 0, 0);
          acc1 = __builtin_amdgcn_mfma_f32_16x16x16bf16_1k(pa[v], __builtin_amdgcn_ds_read_tr16_b64_v4i16((LAS s16x4*)(cell + ((va0 ^ (v << 5)) + 256))), acc1, 0, 0, 0);
        }
        asm volatile("" : "+v"(acc0), "+v"(acc1));
        LGKM0(); fr[n & 7] = n + 1;
      };
      constexpr int NWS = (32 + WOB - 1) / WOB;
#pragma unroll
      for (int hb = 0; hb < NWS; ++hb) {
#pragma unroll
        for (int i = WOB * hb; i < WOB * hb + WOB && i < 32; ++i) wf[i] = ldw(wop + (size_t)((i >> 4) * 256 + ((i >> 3) & 1) * 32 + (i & 7)) * DM);
        __builtin_amdgcn_sched_barrier(0);
        astep(2 * NB + hb);
      }
#pragma unroll 1
      for (int aa = 2 * NB + NWS; aa < 16; ++aa) astep(aa);
#pragma unroll
      for (int r = 0; r < 4; ++r) {
        L->mrg[cw][(r & 1) * 256 + (2 * g4 + (r >> 1)) * 16 + c16] = acc0[r];
        L->mrg[cw][(r & 1) * 256 + 128 + (2 * g4 + (r >> 1)) * 16 + c16] = acc1[r];
      }
      LGKM0();
      if (lane == 0) __hip_atomic_fetch_add(&L->acnt, 1u, __ATOMIC_RELAXED, __HIP_MEMORY_SCOPE_WORKGROUP);
      __builtin_amdgcn_sched_barrier(0);
#pragma unroll
      for (int kb = 0; kb < 4; ++kb) {
        v4u tf[8];
#pragma unroll
        for (int e = 0; e < 8; ++e)
#pragma unroll
          for (int d = 0; d < 4; ++d)
            tf[e][d] = (e & 1) ? perm_hi(wf[8 * kb + 2 * d][e >> 1], wf[8 * kb + 2 * d + 1][e >> 1]) : perm_lo(wf[8 * kb + 2 * d][e >> 1], wf[8 * kb + 2 * d + 1][e >> 1]);
        PIN8(tf);
#pragma unroll
        for (int e = 0; e < 8; ++e) wf[8 * kb + e] = tf[e];
      }
      f32x4 acc[4];
#pragma unroll
      for (int t = 0; t < 4; ++t) acc[t] = f32x4{0.f, 0.f, 0.f, 0.f};
      f32x4 o = {0.f, 0.f, 0.f, 0.f};
      wait_ge(&L->pready, 5);
      LAS v2u* obw = (LAS v2u*)&L->ob[cw][(c16 & 1) * 32 + ((c16 >> 1) & 3) * 8 + 2 * g4];
#pragma unroll 1
      for (int mm = 0; mm < 2 * NB; ++mm) {
        const int m = 2 * mm + par, n = NA + m;

        const LAS unsigned* pp = &L->PB[m / NB][lane & 1][(m % NB) * 64 + w * 32 + 4 * g4];
        const bf16x8 p0 = *(const LAS bf16x8*)pp, p1 = *(const LAS bf16x8*)(pp + 16);
        wait_ge(full, n + 1);
        bf16x8 vf[8];
        read_tr(L->ring[w][n & 7], ta0, vf);
#pragma unroll
        for (int t = 0; t < 4; ++t) acc[t] = __builtin_amdgcn_mfma_f32_16x16x32_bf16(vf[t], p0, acc[t], 0, 0, 0);
#pragma unroll
        for (int t = 0; t < 4; ++t) acc[t] = __builtin_amdgcn_mfma_f32_16x16x32_bf16(vf[4 + t], p1, acc[t], 0, 0, 0);
        asm volatile("" : "+v"(acc[0]), "+v"(acc[1]), "+v"(acc[2]), "+v"(acc[3]));
      }
      if (cw == 0) {
        wait_ge(&L->oready, 1);
        if ((c16 & 1) == kh) o = *(const LAS f32x4*)&L->oa[((c16 >> 1) & 3) * 16 + 4 * g4];
      }
      o += sel4(acc[0], acc[1], acc[2], acc[3], c16 & 2, c16 & 4);
      *obw = v2u{pkbf(o[0], o[1]), pkbf(o[2], o[3])};
      const int col = 128 * cw + 8 * c16 + 4 * (g4 & 1);
      const bool st_on = g4 < 2 && col < DM;
      const int yoff = st_on ? WS_Y + gid * (DM * 4) + col * 4 : WS_END;
      const LAS float* ybp = &L->yb[st_on ? col : 0];
      const LAS unsigned* obr = &L->ob[c16 & 3][4 * g4];
      WG_BARRIER();
      bf16x8 of[4];
#pragma unroll
      for (int kb = 0; kb < 4; ++kb) of[kb] = *(const LAS bf16x8*)(obr + 16 * kb);
      const f32x4 ybv = *(const LAS f32x4*)ybp;
      const float g = L->gate;
      __builtin_amdgcn_sched_barrier(0);
      f32x4 ya[8];
#pragma unroll
      for (int e = 0; e < 8; ++e) ya[e] = f32x4{0.f, 0.f, 0.f, 0.f};
#pragma unroll
      for (int kb = 0; kb < 4; ++kb) {
#pragma unroll
        for (int e = 0; e < 8; ++e) ya[e] = __builtin_amdgcn_mfma_f32_16x16x32_bf16(of[kb], __builtin_bit_cast(bf16x8, wf[8 * kb + e]), ya[e], 0, 0, 0);
        __builtin_amdgcn_sched_barrier(0);
      }
      v4u yv;
#pragma unroll
      for (int r = 0; r < 4; ++r) {
        const float lo = (ya[r][0] + ya[r][1]) + (ya[r][2] + ya[r][3]), hi = (ya[4 + r][0] + ya[4 + r][1]) + (ya[4 + r][2] + ya[4 + r][3]);
        yv[r] = san(fmaf(g, (g4 & 1) ? hi : lo, ybv[r]));
      }
      __builtin_amdgcn_raw_buffer_store_b128(yv, rs, yoff, 0, 16);
    } else {
      if (wave == 2) {
        {
          const unsigned thi = (unsigned)(tag >> 32), tlo = (unsigned)tag;
          int off[4];
#pragma unroll
          for (int k = 0; k < 4; ++k) off[k] = WS_QG + (gid & ~7) * 512 + k * 1024 + lane * 16;
          __builtin_amdgcn_s_sleep(80);
#pragma unroll 1
          for (unsigned spins = 0; spins < SPIN_CAP; ++spins) {
            v4u g[4];
            bool ok = true;
#pragma unroll
            for (int k = 0; k < 4; ++k) g[k] = __builtin_amdgcn_raw_buffer_load_b128(rs, off[k], 0, 16);
#pragma unroll
            for (int k = 0; k < 4; ++k) {
              if (off[k] != WS_END && g[k][1] == thi && g[k][3] == thi && (g[k][0] & 0xffff0000u) == tlo && (g[k][2] & 0xffff0000u) == tlo) {
                L->q[k * 64 + lane] = perm_lo(g[k][0], g[k][2]);
                off[k] = WS_END;
              }
              ok &= off[k] == WS_END;
            }
            if (__all(ok)) break;
            __builtin_amdgcn_s_sleep(16);
          }
          LGKM0();
          *(volatile LAS unsigned*)&L->qready = 1u;
        }
        constexpr float C2 = 0.0625f * 1.4426950408889634f;
        float mrun = -INFINITY, lrun = 0.f;
#pragma unroll 1
        for (int gen = 0; gen < 4; ++gen) {
#pragma unroll
          for (int c = 0; c < 4; ++c) wait_ge(&L->kdone[c], gen + 1);
          v4u g = *(const LAS v4u*)&L->P[gen][0][lane * 4];
          if (gen == 0) {
            __builtin_amdgcn_raw_buffer_store_b128(g, rs, WS_S + gid * 1024 + lane * 16, 0, 16);
          }
          if (gen == 2) {
            DRAIN();
            if (lane == 0) __hip_atomic_store((GAS u64*)(ws + WS_RDY) + gid, tag | ((unsigned)__builtin_amdgcn_s_memrealtime() & 0xffffu), RLX_AGENT);
          }
          float cm = -INFINITY;
#pragma unroll
          for (int e = 0; e < 4; ++e) cm = fmaxf(cm, fmaxf(bflo(g[e]), bfhi(g[e])));
          const float mn = fmaxf(mrun, max32(cm) * C2);
          float ls = 0.f;
#pragma unroll
          for (int e = 0; e < 4; ++e) {
            const float pl = __builtin_amdgcn_exp2f(fmaf(bflo(g[e]), C2, -mn)), ph = __builtin_amdgcn_exp2f(fmaf(bfhi(g[e]), C2, -mn));
            ls += pl + ph;
            g[e] = pkbf(pl, ph);
          }
          *(LAS v4u*)&L->P[gen][0][lane * 4] = g;
          lrun = fmaf(lrun, __builtin_amdgcn_exp2f(mrun - mn), ls);
          mrun = mn;
          if ((lane & 31) == 0) L->mref[gen][lane >> 5] = mn;
          LGKM0();
          *(volatile LAS unsigned*)&L->pready = gen + 1;
        }
        lrun += dpp<XOR1>(lrun); lrun += dpp<XOR2>(lrun); lrun += dpp<ROR4>(lrun); lrun += dpp<XOR8>(lrun); lrun = swp16_add(lrun, lrun);
        if ((lane & 31) == 0) __builtin_amdgcn_raw_buffer_store_b64(v2u{san(mrun), san(lrun)}, rs, WS_ST + gid * 128 + (lane >> 5) * 8, 0, 16);
        u64 gv;
#pragma unroll 1
        for (unsigned spins = 0; spins < SPIN_CAP; ++spins) {
          gv = __hip_atomic_load((GAS u64*)(ws + WS_RDY) + b * 64 + lane, RLX_AGENT);
          if (__all((gv & ~0xffffull) == tag)) break;
          __builtin_amdgcn_s_sleep(32);
        }
        *(volatile LAS unsigned*)&L->anchor[1] = (mean_time((unsigned)gv, true) + TOFF2 - (qd == 3 ? Q3LEAD : 0)) | 1u;
        if (j == 0 && lane == 0) __hip_atomic_store((GAS u64*)(ws + WS_EPOCH + b * 256), ep + 1, RLX_AGENT);
        asm volatile("" ::: "memory");
        v4u h[4];
#pragma unroll
        for (int k = 0; k < 4; ++k) h[k] = __builtin_amdgcn_raw_buffer_load_b128(rs, WS_S + ((gid & ~3) + k) * 1024 + lane * 16, 0, 16);
        v2u sv;
#pragma unroll 1
        for (unsigned spins = 0; spins < SPIN_CAP; ++spins) {
          __builtin_amdgcn_s_sleep(127);
          sv = __builtin_amdgcn_raw_buffer_load_b64(rs, WS_ST + ((gid & ~7) + (lane & 7)) * 128 + (lane >> 5) * 8, 0, 16);
          if (__all(sv[0] != SENT && sv[1] != SENT)) break;
        }
        const float mp = __uint_as_float(sv[0]);
        float M = fmaxf(mp, dpp<XOR1>(mp)); M = fmaxf(M, dpp<XOR2>(M)); M = fmaxf(M, dpp<XOR7>(M));
        float Ls = __uint_as_float(sv[1]) * __builtin_amdgcn_exp2f(mp - M);
        Ls += dpp<XOR1>(Ls); Ls += dpp<XOR2>(Ls); Ls += dpp<XOR7>(Ls);
        const float inv = 1.f / Ls;
#pragma unroll
        for (int k = 0; k < 4; ++k) {
#pragma unroll
          for (int e = 0; e < 4; ++e)
            h[k][e] = pkbf(inv * __builtin_amdgcn_exp2f(fmaf(bflo(h[k][e]), C2, -M)), inv * __builtin_amdgcn_exp2f(fmaf(bfhi(h[k][e]), C2, -M)));
          *(LAS v4u*)&L->PB[k][0][lane * 4] = h[k];
        }
        LGKM0();
        *(volatile LAS unsigned*)&L->pready = 5u;
        const float as = inv * __builtin_amdgcn_exp2f(mrun - M);
        wait_ge(&L->acnt, 4);
        f32x4 o0 = *(const LAS f32x4*)(L->mrg[0] + lane * 8), o1 = *(const LAS f32x4*)(L->mrg[0] + lane * 8 + 4);
#pragma unroll
        for (int ww = 1; ww < 4; ++ww) { o0 += *(const LAS f32x4*)(L->mrg[ww] + lane * 8); o1 += *(const LAS f32x4*)(L->mrg[ww] + lane * 8 + 4); }
        o0 *= as; o1 *= as;
        v4u ov = {pkbf(o0[0], o0[1]), pkbf(o0[2], o0[3]), pkbf(o1[0], o1[1]), pkbf(o1[2], o1[3])};
#pragma unroll
        for (int k = 0; k < 4; ++k) ov[k] = ov[k] == SENT ? 0x7FC07FC0u : ov[k];
        __builtin_amdgcn_raw_buffer_store_b128(ov, rs, WS_PO + gid * 1024 + lane * 16, 0, 16);
        __builtin_amdgcn_s_sleep(20);
        ov = poll16<2>(rs, WS_PO + ((gid & ~7) + (lane >> 3)) * 1024 + s * 128 + (lane & 7) * 16);
        float oe[8];
#pragma unroll
        for (int k = 0; k < 4; ++k) { oe[2 * k] = bflo(ov[k]); oe[2 * k + 1] = bfhi(ov[k]); }
        L->oa[8 * (lane & 7) + (lane >> 3)] = tr8(oe, lane);
        LGKM0();
        *(volatile LAS unsigned*)&L->oready = 1u;
      } else {
        wait_ge(&L->full[0], 3);
        if (lane == 0) __hip_atomic_store((GAS u64*)(ws + WS_TS) + gid, tag | ((unsigned)__builtin_amdgcn_s_memrealtime() & 0xffffu), RLX_AGENT);
#pragma unroll 1
        for (int k = 0; k < 4; ++k) {
          wait_ge(&L->full[0], 8 + 3 * k);
          const u64 tv = __hip_atomic_load((GAS u64*)(ws + WS_TS) + b * 64 + lane, RLX_AGENT);
          const bool ok = (tv & ~0xffffull) == tag;
          *(volatile LAS unsigned*)&L->anchor[0] = (mean_time((unsigned)tv, ok) + TOFF1 - (qd == 3 ? Q3LEAD : 0)) | 1u;
          if (__all(ok)) break;
        }
        const int cl = lane < 56 ? lane : 55;
        float z = 0.f;
#pragma unroll
        for (int k = 0; k < 4; ++k) {
          const int idx = lane + 64 * k;
          const bool in = idx < DM / 2;
          const unsigned xv = x32[in ? idx : 0], gv = ((const GAS unsigned*)gw)[in ? idx : 0];
          const float t = rbf(bflo(xv) * bflo(gv)) + rbf(bfhi(xv) * bfhi(gv));
          z += in ? t : 0.f;
        }
        z = swp32_add(z, z); z = swp16_add(z, z); z += dpp<XOR8>(z); z += dpp<XOR7>(z); z += dpp<XOR2>(z); z += dpp<XOR1>(z);
        const float g = rbf(1.f / (1.f + __expf(-rbf(z))));
        if (lane == 0) L->gate = g;
        float yb[8];
#pragma unroll
        for (int c = 0; c < 8; ++c) yb[c] = 0.f;
        if (s != 3) {
          const int u0 = (s == 7 ? 48 + kvh : kvh * 6 + 2 * qd + kh) * 32, rr = lane >> 2;
          float ha[8];
#pragma unroll
          for (int c = 0; c < 8; ++c) ha[c] = 0.f;
          const GAS u16* w1p = (const GAS u16*)w1 + (size_t)(2 * rr) * DFF + u0 + (lane & 3) * 8;
#pragma unroll 1
          for (int h = 0; h < 14; ++h) pair_dot(ldw(w1p + (size_t)(h * 32) * DFF), ldw(w1p + (size_t)(h * 32 + 1) * DFF), x32[h * 16 + rr], ha);
          float hv = tr8(ha, lane);
          hv += dpp<SHR4>(hv);
          hv = rbf(hv);
          hv = hv <= 0.f ? 0.f : hv;
#pragma unroll 1
          for (int c4 = 0; c4 < 32 / MB2; ++c4) {
            v4u w2r[MB2];
#pragma unroll
            for (int u = 0; u < MB2; ++u) w2r[u] = ldw((const GAS u16*)w2 + (size_t)(u0 + c4 * MB2 + u) * DM + cl * 8);
#pragma unroll
            for (int u = 0; u < MB2; ++u) {
              const int un = c4 * MB2 + u;
              const float hu = __builtin_bit_cast(float, __builtin_amdgcn_readlane(__builtin_bit_cast(int, hv), (un & 7) * 8 + 4 + (un >> 3)));
#pragma unroll
              for (int c = 0; c < 4; ++c) { yb[2 * c] = fmaf(hu, bflo(w2r[u][c]), yb[2 * c]); yb[2 * c + 1] = fmaf(hu, bfhi(w2r[u][c]), yb[2 * c + 1]); }
            }
          }
        }
        const float omg = rbf(1.f - g);
        if (lane < 56) {
          *(LAS f32x4*)(L->yb + lane * 8) = f32x4{omg * yb[0], omg * yb[1], omg * yb[2], omg * yb[3]};
          *(LAS f32x4*)(L->yb + lane * 8 + 4) = f32x4{omg * yb[4], omg * yb[5], omg * yb[6], omg * yb[7]};
        }
      }
      WG_BARRIER();
    }
  }
  if (j < 14) {
    if (wave < 4) __builtin_amdgcn_s_sleep(14);
    __builtin_amdgcn_s_sleep(8);
    const v4u y = poll16<1>(rs, WS_Y + (b * 64 + wave * 8 + (lane >> 3)) * (DM * 4) + j * 128 + (lane & 7) * 16);
    const float r0 = swp32_add(__uint_as_float(y[0]), __uint_as_float(y[2])), r1 = swp32_add(__uint_as_float(y[1]), __uint_as_float(y[3]));
    float r = swp16_add(r0, r1);
    r += dpp<XOR8>(r);
    if (!(lane & 8)) L->fin[wave][(lane & 7) * 4 + (lane >> 4)] = r;
    WG_BARRIER();
    if (tid < 32) {
      const float t = ((L->fin[0][tid] + L->fin[1][tid]) + (L->fin[2][tid] + L->fin[3][tid])) + ((L->fin[4][tid] + L->fin[5][tid]) + (L->fin[6][tid] + L->fin[7][tid]));
      out[b * DM + j * 32 + tid] = (u16)pkbf(t, 0.f);
    }
  }
}

extern "C" void kernel_launch(void* const* d_in, const int* in_sizes, int n_in, void* d_out, int out_size, void* d_ws, size_t ws_size, hipStream_t stream) {
  fused_kernel<<<256, 512, 0, stream>>>((const u16*)d_in[0], (const u16*)d_in[1], (const u16*)d_in[2], (const u16*)d_in[3], (const u16*)d_in[4],
                                        (const u16*)d_in[6], (const u16*)d_in[7], (const u16*)d_in[8], (unsigned char*)d_ws, (u16*)d_out);
}
```

```cpp
#include <hip/hip_runtime.h>
#include <hip/hip_bf16.h>
#include <cstdint>

#define K_NT 1
#define V_NT 1
#define W_NT 0
#define CL 8
#define PACE0 0
#define PACE 0
#define STAMPS 0

typedef unsigned v4u __attribute__((ext_vector_type(4)));
typedef unsigned v3u __attribute__((ext_vector_type(3)));
typedef unsigned v2u __attribute__((ext_vector_type(2)));
typedef float f32x4 __attribute__((ext_vector_type(4)));
typedef __bf16 bf16x2 __attribute__((ext_vector_type(2)));
typedef unsigned short u16;
typedef unsigned long long u64;
#define GAS __attribute__((address_space(1)))
#define LAS __attribute__((address_space(3)))
#define RLX_AGENT __ATOMIC_RELAXED, __HIP_MEMORY_SCOPE_AGENT

constexpr int DM = 448, SK = 8192, DH = 256, DFF = 1792, QLD = 4096;
constexpr unsigned SENT = 0xFFFFFFFFu;
constexpr unsigned SPIN_CAP = 1u << 18;
constexpr int NCH = 128 / CL;
constexpr int WS_EPOCH = 0;
constexpr int WS_ML = 4096;
constexpr int WS_QG = 8192;
constexpr int WS_PO = WS_QG + 256 * 64 * 8;
constexpr int WS_Y = WS_PO + 256 * 512 * 4;
constexpr int WS_END = WS_Y + 256 * DM * 4;
constexpr size_t WS_STAMP = 16u << 20;

__device__ __forceinline__ float bflo(unsigned u) { return __builtin_bit_cast(float, u << 16); }
__device__ __forceinline__ float bfhi(unsigned u) { return __builtin_bit_cast(float, u & 0xffff0000u); }
__device__ __forceinline__ unsigned pkbf(float lo, float hi) {
  bf16x2 r; r[0] = (__bf16)lo; r[1] = (__bf16)hi; return __builtin_bit_cast(unsigned, r);
}
__device__ __forceinline__ float rbf(float f) { return bflo(pkbf(f, 0.f)); }
__device__ __forceinline__ float dot2(unsigned a, unsigned b, float acc) {
  return __builtin_amdgcn_fdot2_f32_bf16(__builtin_bit_cast(bf16x2, a), __builtin_bit_cast(bf16x2, b), acc, false);
}
__device__ __forceinline__ unsigned perm_lo(unsigned a, unsigned b) { return __builtin_amdgcn_perm(b, a, 0x05040100u); }
__device__ __forceinline__ unsigned perm_hi(unsigned a, unsigned b) { return __builtin_amdgcn_perm(b, a, 0x07060302u); }
template <int CTRL> __device__ __forceinline__ float dpp(float x) {
  return __builtin_bit_cast(float, __builtin_amdgcn_mov_dpp(__builtin_bit_cast(int, x), CTRL, 0xf, 0xf, true));
}
constexpr int XOR1 = 0xB1, XOR2 = 0x4E, XOR7 = 0x141, XOR8 = 0x128, ROR4 = 0x124, SHR4 = 0x114;
__device__ __forceinline__ float swp16_add(float a, float b) {
  auto s = __builtin_amdgcn_permlane16_swap(__float_as_uint(a), __float_as_uint(b), false, false);
  return __uint_as_float(s[0]) + __uint_as_float(s[1]);
}
__device__ __forceinline__ float swp32_add(float a, float b) {
  auto s = __builtin_amdgcn_permlane32_swap(__float_as_uint(a), __float_as_uint(b), false, false);
  return __uint_as_float(s[0]) + __uint_as_float(s[1]);
}
__device__ __forceinline__ float swp16_max(float a) {
  auto s = __builtin_amdgcn_permlane16_swap(__float_as_uint(a), __float_as_uint(a), false, false);
  return fmaxf(__uint_as_float(s[0]), __uint_as_float(s[1]));
}
__device__ __forceinline__ float swp32_max(float a) {
  auto s = __builtin_amdgcn_permlane32_swap(__float_as_uint(a), __float_as_uint(a), false, false);
  return fmaxf(__uint_as_float(s[0]), __uint_as_float(s[1]));
}
template <int CTRL> __device__ __forceinline__ float tr_dpp(float lo, float hi, bool sel) {
  const float t1 = lo + dpp<CTRL>(lo), t2 = hi + dpp<CTRL>(hi);
  return sel ? t2 : t1;
}
__device__ __forceinline__ float tr8(float (&a)[8], int lane) {
#pragma unroll
  for (int i = 0; i < 4; ++i) a[i] = swp32_add(a[i], a[i + 4]);
#pragma unroll
  for (int i = 0; i < 2; ++i) a[i] = swp16_add(a[i], a[i + 2]);
  return tr_dpp<XOR8>(a[0], a[1], lane & 8);
}
__device__ __forceinline__ void pair_dot(const v4u& ra, const v4u& rb, unsigned xp, float (&a)[8]) {
#pragma unroll
  for (int c = 0; c < 4; ++c) {
    a[2 * c] = dot2(perm_lo(ra[c], rb[c]), xp, a[2 * c]);
    a[2 * c + 1] = dot2(perm_hi(ra[c], rb[c]), xp, a[2 * c + 1]);
  }
}
__device__ __forceinline__ unsigned san(float f) { const unsigned u = __float_as_uint(f); return u == SENT ? 0x7FC00000u : u; }
template <bool NT> __device__ __forceinline__ v4u ldkv(const GAS char* p) {
  return NT ? __builtin_nontemporal_load((const GAS v4u*)p) : *(const GAS v4u*)p;
}
__device__ __forceinline__ v4u ldw(const GAS void* p) {
  return W_NT ? __builtin_nontemporal_load((const GAS v4u*)p) : *(const GAS v4u*)p;
}
#define WG_BARRIER() do { asm volatile("s_waitcnt lgkmcnt(0)" ::: "memory"); __builtin_amdgcn_s_barrier(); asm volatile("" ::: "memory"); } while (0)
#define DRAIN() asm volatile("s_waitcnt vmcnt(0)" ::: "memory")
#define STAMP(i) do {} while (0)
#define STAMP_OUT() do {} while (0)

struct Lds {
  unsigned q[256];
  float p[4][64];
  float mrg[4][512];
  float ml[4][4];
  float yred[4][DM];
  float qpart[4][64];
  unsigned cnt[4];
};

#define PIN16(a) asm volatile("" : "+v"(a[0]), "+v"(a[1]), "+v"(a[2]), "+v"(a[3]), "+v"(a[4]), "+v"(a[5]), "+v"(a[6]), "+v"(a[7]), \
                                   "+v"(a[8]), "+v"(a[9]), "+v"(a[10]), "+v"(a[11]), "+v"(a[12]), "+v"(a[13]), "+v"(a[14]), "+v"(a[15]))
#define PIN8(a) asm volatile("" : "+v"(a[0]), "+v"(a[1]), "+v"(a[2]), "+v"(a[3]), "+v"(a[4]), "+v"(a[5]), "+v"(a[6]), "+v"(a[7]))

template <bool REFILL>
__device__ __forceinline__ void chunk(v4u (&kv)[2 * CL], const GAS char* kn, const GAS char* vn, const v4u& q0, const v4u& q1,
                                      float& m, float& l, float (&acc)[16], LAS float* pw, int lane) {
  float x[2 * CL];
#pragma unroll
  for (int j = 0; j < CL; ++j) {
    float a0 = 0.f, a1 = 0.f;
#pragma unroll
    for (int k = 0; k < 4; ++k) { a0 = dot2(kv[j][k], q0[k], a0); a1 = dot2(kv[j][k], q1[k], a1); }
    x[2 * j] = a0; x[2 * j + 1] = a1;
    if (REFILL) {
      asm volatile("" : "+v"(x[2 * j]), "+v"(x[2 * j + 1]));
      if (PACE) asm volatile("s_waitcnt vmcnt(%0)" ::"i"(PACE - 1));
      kv[j] = ldkv<K_NT>(kn + j * 1024);
      __builtin_amdgcn_sched_barrier(0);
    }
  }
#pragma unroll
  for (int i = 0; i < CL; ++i) x[i] = swp16_add(x[i], x[i + CL]);
#pragma unroll
  for (int i = 0; i < CL / 2; ++i) x[i] = tr_dpp<XOR8>(x[i], x[i + CL / 2], lane & 8);
#pragma unroll
  for (int i = 0; i < CL / 4; ++i) x[i] = tr_dpp<XOR7>(x[i], x[i + CL / 4], lane & 4);
#pragma unroll
  for (int i = 0; i < CL / 8; ++i) x[i] = tr_dpp<XOR2>(x[i], x[i + CL / 8], lane & 2);
  float sc;
  if (CL == 16) sc = tr_dpp<XOR1>(x[0], x[1], lane & 1); else sc = x[0] + dpp<XOR1>(x[0]);
  sc = rbf(sc) * (0.0625f * 1.4426950408889634f);
  float cm = sc;
  if (CL == 16) cm = fmaxf(cm, dpp<XOR2>(cm));
  cm = fmaxf(cm, dpp<ROR4>(cm)); cm = fmaxf(cm, dpp<XOR8>(cm)); cm = swp16_max(cm); cm = swp32_max(cm);
  const float mn = fmaxf(m, cm);
  const float alpha = __builtin_amdgcn_exp2f(m - mn), p = __builtin_amdgcn_exp2f(sc - mn);
  l = fmaf(l, alpha, p); m = mn;
  pw[CL == 16 ? lane : lane >> 1] = p;
  const float al0 = dpp<0x00>(alpha), al1 = dpp<CL == 16 ? 0x55 : 0xAA>(alpha);
#pragma unroll
  for (int k = 0; k < 8; ++k) { acc[k] *= al0; acc[8 + k] *= al1; }
  asm volatile("s_waitcnt lgkmcnt(0)" ::: "memory");
  f32x4 pr[CL / 2];
#pragma unroll
  for (int u = 0; u < CL / 2; ++u) pr[u] = *(const LAS f32x4*)(pw + (lane >> 5) * (2 * CL) + 4 * u);
  __builtin_amdgcn_sched_barrier(0);
#pragma unroll
  for (int j = 0; j < CL; ++j) {
    const float p0 = pr[j >> 1][(j & 1) * 2], p1 = pr[j >> 1][(j & 1) * 2 + 1];
#pragma unroll
    for (int k = 0; k < 4; ++k) {
      const float lo = bflo(kv[CL + j][k]), hi = bfhi(kv[CL + j][k]);
      acc[2 * k] = fmaf(p0, lo, acc[2 * k]); acc[2 * k + 1] = fmaf(p0, hi, acc[2 * k + 1]);
      acc[8 + 2 * k] = fmaf(p1, lo, acc[8 + 2 * k]); acc[8 + 2 * k + 1] = fmaf(p1, hi, acc[8 + 2 * k + 1]);
    }
    if (REFILL) {
      PIN16(acc);
      if (PACE) asm volatile("s_waitcnt vmcnt(%0)" ::"i"(PACE - 1));
      kv[CL + j] = ldkv<V_NT>(vn + j * 1024);
      __builtin_amdgcn_sched_barrier(0);
    }
  }
  asm volatile("" ::: "memory");
}

__global__ void __launch_bounds__(512, 2) fused_kernel(const u16* __restrict__ x, const u16* __restrict__ Kc, const u16* __restrict__ Vc,
    const u16* __restrict__ wq, const u16* __restrict__ wo, const u16* __restrict__ w1, const u16* __restrict__ w2,
    const u16* __restrict__ gw, unsigned char* ws, u16* out) {
  __shared__ __attribute__((aligned(16))) Lds sm;
  LAS Lds* L = (LAS Lds*)&sm;
  const unsigned tbeg = (unsigned)__builtin_amdgcn_s_memrealtime();
  const int tid = threadIdx.x, lane = tid & 63, wave = __builtin_amdgcn_readfirstlane(tid >> 6);
  const int blk = blockIdx.x, kvh = blk & 7, s = (blk >> 3) & 7, b = blk >> 6;
  const int j = kvh * 8 + s;
  const int gid = b * 64 + j;
  const __amdgpu_buffer_rsrc_t rs = __builtin_amdgcn_make_buffer_rsrc((void*)ws, 0, WS_END, 0x00020000);
  STAMP(0);
  if (wave >= 4) {
    const int w = wave - 4;
    const size_t base = ((size_t)((b * 8 + kvh) * SK + s * 1024 + w * 2 * CL)) * (DH * 2) + lane * 16;
    const GAS char* kp = (const GAS char*)Kc + base;
    const GAS char* vp = (const GAS char*)Vc + base;
    if (tid == 256) L->cnt[0] = 0u;
    WG_BARRIER();
    v4u kv[2 * CL];
#pragma unroll
    for (int i = 0; i < 2 * CL; ++i) {
      if (PACE0 && i >= PACE0) asm volatile("s_waitcnt vmcnt(%0)" ::"i"(PACE0 - 1));
      kv[i] = i < CL ? ldkv<K_NT>(kp + i * 1024) : ldkv<V_NT>(vp + (i - CL) * 1024);
      __builtin_amdgcn_sched_barrier(0);
    }
    STAMP(1);
    WG_BARRIER();
    STAMP(2);
    const v4u q0 = *(const LAS v4u*)(L->q + (lane & 31) * 4), q1 = *(const LAS v4u*)(L->q + 128 + (lane & 31) * 4);
    float m = -INFINITY, l = 0.f, acc[16];
#pragma unroll
    for (int k = 0; k < 16; ++k) acc[k] = 0.f;
    LAS float* pw = L->p[w];
    for (int t = 1; t < NCH; ++t) {
      kp += CL * 4096; vp += CL * 4096;
      chunk<true>(kv, kp, vp, q0, q1, m, l, acc, pw, lane);
    }
    chunk<false>(kv, kp, vp, q0, q1, m, l, acc, pw, lane);
    STAMP(3);
    float o[8];
#pragma unroll
    for (int k = 0; k < 8; ++k) o[k] = swp32_add(acc[k], acc[8 + k]);
    *(LAS f32x4*)(L->mrg[w] + lane * 8) = f32x4{o[0], o[1], o[2], o[3]};
    *(LAS f32x4*)(L->mrg[w] + lane * 8 + 4) = f32x4{o[4], o[5], o[6], o[7]};
    if (CL == 16) l += dpp<XOR2>(l);
    l += dpp<ROR4>(l); l += dpp<XOR8>(l); l = swp16_add(l, l); l = swp32_add(l, l);
    if (CL == 16) { if (lane < 2) { L->ml[w][2 * lane] = m; L->ml[w][2 * lane + 1] = l; } }
    else if ((lane & 0x3d) == 0) { L->ml[w][lane] = m; L->ml[w][lane + 1] = l; }
    WG_BARRIER();
    STAMP(4);
    if (w < 2) {
      float M = -INFINITY;
#pragma unroll
      for (int ww = 0; ww < 4; ++ww) M = fmaxf(M, L->ml[ww][2 * w]);
      float Ls = 0.f; f32x4 os = {0.f, 0.f, 0.f, 0.f};
#pragma unroll
      for (int ww = 0; ww < 4; ++ww) {
        const float wt = __builtin_amdgcn_exp2f(L->ml[ww][2 * w] - M);
        Ls = fmaf(wt, L->ml[ww][2 * w + 1], Ls);
        os += wt * *(const LAS f32x4*)(L->mrg[ww] + w * 256 + lane * 4);
      }
      const v4u ov = {san(os[0]), san(os[1]), san(os[2]), san(os[3])};
      __builtin_amdgcn_raw_buffer_store_b128(ov, rs, WS_PO + gid * 2048 + w * 1024 + lane * 16, 0, 16);
      if (lane == 0) __builtin_amdgcn_raw_buffer_store_b64(v2u{san(M), san(Ls)}, rs, WS_ML + gid * 16 + w * 8, 0, 16);
      STAMP(5);
    }
    WG_BARRIER();
    if (w < 2 && j < 56) {
      const int cg = 2 * j + w;
      v4u y;
      for (unsigned spins = 0;; ++spins) {
        y = __builtin_amdgcn_raw_buffer_load_b128(rs, WS_Y + (b * 64 + lane) * (DM * 4) + cg * 16, 0, 16);
        const bool ok = y[0] != SENT && y[1] != SENT && y[2] != SENT && y[3] != SENT;
        if (__all(ok) || spins > SPIN_CAP) break;
        __builtin_amdgcn_s_sleep(1);
      }
      STAMP(6);
      float r0 = swp32_add(__uint_as_float(y[0]), __uint_as_float(y[2])), r1 = swp32_add(__uint_as_float(y[1]), __uint_as_float(y[3]));
      float r = swp16_add(r0, r1);
      r += dpp<XOR8>(r); r += dpp<XOR7>(r); r += dpp<XOR2>(r); r += dpp<XOR1>(r);
      while ((unsigned)__builtin_amdgcn_s_memrealtime() - tbeg < 6000u) __builtin_amdgcn_s_sleep(1);
      if ((lane & 15) == 0) out[b * DM + cg * 4 + (lane >> 4)] = (u16)pkbf(r, 0.f);
      STAMP(7);
    }
  } else {
    const int hw = wave;
    const GAS unsigned* x32 = (const GAS unsigned*)(x + b * DM);
    const int r = lane >> 3;
    unsigned xp[7];
#pragma unroll
    for (int k = 0; k < 7; ++k) xp[k] = x32[56 * hw + 7 * r + k];
    const GAS char* wqp = (const GAS char*)wq + (size_t)(112 * hw + 14 * r) * (QLD * 2) + (j * 64 + (lane & 7) * 8) * 2;
    v4u wr[14];
#pragma unroll
    for (int k = 0; k < 14; ++k) wr[k] = ldw(wqp + (size_t)k * (QLD * 2));
    const u64 ep = __hip_atomic_load((GAS u64*)(ws + WS_EPOCH + b * 256), RLX_AGENT);
    {
      const v4u sv = {SENT, SENT, SENT, SENT};
      const int off = hw < 2 ? WS_PO + gid * 2048 + hw * 1024 + lane * 16
                    : (tid < 240 ? WS_Y + gid * (DM * 4) + (tid - 128) * 16 : (tid == 240 ? WS_ML + gid * 16 : WS_END));
      __builtin_amdgcn_raw_buffer_store_b128(sv, rs, off, 0, 16);
    }
    __builtin_amdgcn_sched_barrier(0);
    WG_BARRIER();
    STAMP(1);
    float qa[8];
#pragma unroll
    for (int c = 0; c < 8; ++c) qa[c] = 0.f;
#pragma unroll
    for (int k = 0; k < 7; ++k) pair_dot(wr[2 * k], wr[2 * k + 1], xp[k], qa);
    L->qpart[hw][(lane & 7) * 8 + r] = tr8(qa, lane);
    const u64 tag = (ep + 1) << 16;
    STAMP(2);
    asm volatile("s_waitcnt vmcnt(0) lgkmcnt(0)" ::: "memory");
    unsigned arr = 0;
    if (lane == 0) arr = __hip_atomic_fetch_add(&L->cnt[0], 1u, __ATOMIC_RELAXED, __HIP_MEMORY_SCOPE_WORKGROUP);
    if (__builtin_amdgcn_readfirstlane(arr) == 3) {
      asm volatile("" ::: "memory");
      const float qv = (L->qpart[0][lane] + L->qpart[1][lane]) + (L->qpart[2][lane] + L->qpart[3][lane]);
      __hip_atomic_store((GAS u64*)(ws + WS_QG) + gid * 64 + lane, tag | (pkbf(qv, 0.f) & 0xffffu), RLX_AGENT);
    }
    STAMP(3);
    {
      const unsigned thi = (unsigned)(tag >> 32), tlo = (unsigned)tag;
      v4u g;
      for (unsigned spins = 0;; ++spins) {
        g = __builtin_amdgcn_raw_buffer_load_b128(rs, WS_QG + ((gid & ~7) + 2 * hw) * 512 + lane * 16, 0, 16);
        const bool ok = g[1] == thi && g[3] == thi && (g[0] & 0xffff0000u) == tlo && (g[2] & 0xffff0000u) == tlo;
        if (__all(ok) || spins > SPIN_CAP) break;
        __builtin_amdgcn_s_sleep(1);
      }
      L->q[hw * 64 + lane] = perm_lo(g[0], g[2]);
    }
    STAMP(4);
    WG_BARRIER();
    float z = 0.f;
#pragma unroll
    for (int k = 0; k < 4; ++k) {
      const int idx = lane + 64 * k;
      const bool in = idx < DM / 2;
      const unsigned xv = x32[in ? idx : 0], gv = ((const GAS unsigned*)gw)[in ? idx : 0];
      const float t = rbf(bflo(xv) * bflo(gv)) + rbf(bfhi(xv) * bfhi(gv));
      z += in ? t : 0.f;
    }
    z = swp32_add(z, z); z = swp16_add(z, z); z += dpp<XOR8>(z); z += dpp<XOR7>(z); z += dpp<XOR2>(z); z += dpp<XOR1>(z);
    float g = rbf(1.f / (1.f + __expf(-rbf(z))));
    float ya[8], yb[8];
#pragma unroll
    for (int c = 0; c < 8; ++c) { ya[c] = 0.f; yb[c] = 0.f; }
    const int cl = lane < 56 ? lane : 55;
    if (hw == 3 && s < 7) {
      const int u0 = (kvh * 7 + s) * 32, rr = lane >> 2;
      float ha[8];
#pragma unroll
      for (int c = 0; c < 8; ++c) ha[c] = 0.f;
      const GAS u16* w1p = (const GAS u16*)w1 + (size_t)(2 * rr) * DFF + u0 + (lane & 3) * 8;
      for (int h = 0; h < 2; ++h) {
        v4u ma[14]; unsigned xq[7];
#pragma unroll
        for (int k = 0; k < 7; ++k) {
          ma[2 * k] = ldw(w1p + (size_t)((7 * h + k) * 32) * DFF);
          ma[2 * k + 1] = ldw(w1p + (size_t)((7 * h + k) * 32 + 1) * DFF);
          xq[k] = x32[(7 * h + k) * 16 + rr];
        }
#pragma unroll
        for (int k = 0; k < 7; ++k) pair_dot(ma[2 * k], ma[2 * k + 1], xq[k], ha);
      }
      float hv = tr8(ha, lane);
      hv += dpp<SHR4>(hv);
      const float omg = rbf(1.f - g);
      hv = fmaxf(rbf(hv), 0.f);
      for (int c4 = 0; c4 < 4; ++c4) {
        v4u w2r[8];
#pragma unroll
        for (int u = 0; u < 8; ++u) w2r[u] = ldw((const GAS u16*)w2 + (size_t)(u0 + c4 * 8 + u) * DM + cl * 8);
#pragma unroll
        for (int u = 0; u < 8; ++u) {
          const float hu = __builtin_bit_cast(float, __builtin_amdgcn_readlane(__builtin_bit_cast(int, hv), u * 8 + 4 + c4));
#pragma unroll
          for (int c = 0; c < 4; ++c) { yb[2 * c] = fmaf(hu, bflo(w2r[u][c]), yb[2 * c]); yb[2 * c + 1] = fmaf(hu, bfhi(w2r[u][c]), yb[2 * c + 1]); }
        }
      }
#pragma unroll
      for (int c = 0; c < 8; ++c) yb[c] *= omg;
    }
    PIN8(yb);
    unsigned wp[8][8];
    {
      const GAS u16* wop = (const GAS u16*)wo + (size_t)(j * 64 + hw * 16) * DM + cl * 8;
#pragma unroll
      for (int i = 0; i < 8; ++i) {
        const v4u ra = ldw(wop + (2 * i) * DM), rb = ldw(wop + (2 * i + 1) * DM);
#pragma unroll
        for (int c = 0; c < 4; ++c) { wp[i][2 * c] = perm_lo(ra[c], rb[c]); wp[i][2 * c + 1] = perm_hi(ra[c], rb[c]); }
      }
#pragma unroll
      for (int i = 0; i < 8; ++i) PIN8(wp[i]);
    }
    asm volatile("" : "+v"(g));
    if (hw == 0) {
      for (unsigned spins = 0;; ++spins) {
        const u64 gv = __hip_atomic_load((GAS u64*)(ws + WS_QG) + (b * 64 + lane) * 64, RLX_AGENT);
        if (__all((gv & ~0xffffull) == tag) || spins > SPIN_CAP) break;
        __builtin_amdgcn_s_sleep(8);
      }
      if (j == 0 && lane == 0) __hip_atomic_store((GAS u64*)(ws + WS_EPOCH + b * 256), ep + 1, RLX_AGENT);
    }
    STAMP(5);
    WG_BARRIER();
    {
      const int pr = (gid & ~7) + (lane >> 3), head = s >> 2;
      v2u ov, mlv;
      for (unsigned spins = 0;; ++spins) {
        ov = __builtin_amdgcn_raw_buffer_load_b64(rs, WS_PO + pr * 2048 + (s * 64 + hw * 16 + (lane & 7) * 2) * 4, 0, 16);
        mlv = __builtin_amdgcn_raw_buffer_load_b64(rs, WS_ML + pr * 16 + head * 8, 0, 16);
        const bool ok = ov[0] != SENT && ov[1] != SENT && mlv[0] != SENT && mlv[1] != SENT;
        if (__all(ok) || spins > SPIN_CAP) break;
        __builtin_amdgcn_s_sleep(1);
      }
      STAMP(6);
      const float mp = __uint_as_float(mlv[0]);
      float M = fmaxf(mp, dpp<XOR8>(mp)); M = swp16_max(M); M = swp32_max(M);
      const float wt = __builtin_amdgcn_exp2f(mp - M);
      float Ls = wt * __uint_as_float(mlv[1]), o0 = wt * __uint_as_float(ov[0]), o1 = wt * __uint_as_float(ov[1]);
      Ls += dpp<XOR8>(Ls); o0 += dpp<XOR8>(o0); o1 += dpp<XOR8>(o1);
      Ls = swp16_add(Ls, Ls); o0 = swp16_add(o0, o0); o1 = swp16_add(o1, o1);
      Ls = swp32_add(Ls, Ls); o0 = swp32_add(o0, o0); o1 = swp32_add(o1, o1);
      const float inv = 1.f / Ls;
      const unsigned op = pkbf(o0 * inv, o1 * inv);
#pragma unroll
      for (int i = 0; i < 8; ++i) {
        const unsigned oi = (unsigned)__builtin_amdgcn_readlane((int)op, i);
#pragma unroll
        for (int c = 0; c < 8; ++c) ya[c] = dot2(wp[i][c], oi, ya[c]);
      }
      if (lane < 56) {
        f32x4 y0, y1;
#pragma unroll
        for (int c = 0; c < 4; ++c) { y0[c] = fmaf(g, ya[c], yb[c]); y1[c] = fmaf(g, ya[4 + c], yb[4 + c]); }
        *(LAS f32x4*)(L->yred[hw] + lane * 8) = y0;
        *(LAS f32x4*)(L->yred[hw] + lane * 8 + 4) = y1;
      }
    }
    STAMP(7);
    WG_BARRIER();
    if (tid < 112) {
      f32x4 y = *(const LAS f32x4*)(L->yred[0] + tid * 4);
#pragma unroll
      for (int ww = 1; ww < 4; ++ww) y += *(const LAS f32x4*)(L->yred[ww] + tid * 4);
      const v4u yv = {san(y[0]), san(y[1]), san(y[2]), san(y[3])};
      __builtin_amdgcn_raw_buffer_store_b128(yv, rs, WS_Y + gid * (DM * 4) + tid * 16, 0, 16);
    }
    STAMP(8);
  }
  STAMP_OUT();
}

extern "C" void kernel_launch(void* const* d_in, const int* in_sizes, int n_in, void* d_out, int out_size, void* d_ws, size_t ws_size, hipStream_t stream) {
  fused_kernel<<<256, 512, 0, stream>>>((const u16*)d_in[0], (const u16*)d_in[1], (const u16*)d_in[2], (const u16*)d_in[3], (const u16*)d_in[4],
                                        (const u16*)d_in[6], (const u16*)d_in[7], (const u16*)d_in[8], (unsigned char*)d_ws, (u16*)d_out);
}
```

```cpp
#include <hip/hip_runtime.h>
#include <hip/hip_bf16.h>
#include <cstdint>

constexpr int NB = 2;
constexpr int WOB = 4;
constexpr int MB2 = 4;
constexpr int TAU1 = 580, TOFF1 = -330, LAG1 = 40, TAU2 = 560, TOFF2 = 30, Q3LEAD = 60;

typedef unsigned v4u __attribute__((ext_vector_type(4)));
typedef unsigned v2u __attribute__((ext_vector_type(2)));
typedef float f32x4 __attribute__((ext_vector_type(4)));
typedef __bf16 bf16x2 __attribute__((ext_vector_type(2)));
typedef __bf16 bf16x8 __attribute__((ext_vector_type(8)));
typedef short s16x4 __attribute__((ext_vector_type(4)));
typedef unsigned short u16;
typedef unsigned long long u64;
#define GAS __attribute__((address_space(1)))
#define LAS __attribute__((address_space(3)))
#define RLX_AGENT __ATOMIC_RELAXED, __HIP_MEMORY_SCOPE_AGENT

constexpr int DM = 448, SK = 8192, DH = 256, DFF = 1792, QLD = 4096;
constexpr unsigned SENT = 0xFFFFFFFFu;
constexpr unsigned SPIN_CAP = 1u << 18;
constexpr int WS_EPOCH = 0;
constexpr int WS_RDY = 4096;
constexpr int WS_TS = 6144;
constexpr int WS_QG = 8192;
constexpr int WS_S = WS_QG + 256 * 64 * 8;
constexpr int WS_ST = WS_S + 256 * 1024;
constexpr int WS_PO = WS_ST + 256 * 128;
constexpr int WS_Y = WS_PO + 256 * 1024;
constexpr int WS_END = WS_Y + 256 * DM * 4;

__device__ __forceinline__ float bflo(unsigned u) { return __builtin_bit_cast(float, u << 16); }
__device__ __forceinline__ float bfhi(unsigned u) { return __builtin_bit_cast(float, u & 0xffff0000u); }
__device__ __forceinline__ unsigned pkbf(float lo, float hi) {
  bf16x2 r; r[0] = (__bf16)lo; r[1] = (__bf16)hi; return __builtin_bit_cast(unsigned, r);
}
__device__ __forceinline__ float rbf(float f) { return bflo(pkbf(f, 0.f)); }
__device__ __forceinline__ float dot2(unsigned a, unsigned b, float acc) {
  return __builtin_amdgcn_fdot2_f32_bf16(__builtin_bit_cast(bf16x2, a), __builtin_bit_cast(bf16x2, b), acc, false);
}
__device__ __forceinline__ unsigned perm_lo(unsigned a, unsigned b) { return __builtin_amdgcn_perm(b, a, 0x05040100u); }
__device__ __forceinline__ unsigned perm_hi(unsigned a, unsigned b) { return __builtin_amdgcn_perm(b, a, 0x07060302u); }
template <int CTRL> __device__ __forceinline__ float dpp(float x) {
  return __builtin_bit_cast(float, __builtin_amdgcn_mov_dpp(__builtin_bit_cast(int, x), CTRL, 0xf, 0xf, true));
}
constexpr int XOR1 = 0xB1, XOR2 = 0x4E, XOR7 = 0x141, XOR8 = 0x128, ROR4 = 0x124, SHR4 = 0x114;
__device__ __forceinline__ float swp16_add(float a, float b) {
  auto s = __builtin_amdgcn_permlane16_swap(__float_as_uint(a), __float_as_uint(b), false, false);
  return __uint_as_float(s[0]) + __uint_as_float(s[1]);
}
__device__ __forceinline__ float swp32_add(float a, float b) {
  auto s = __builtin_amdgcn_permlane32_swap(__float_as_uint(a), __float_as_uint(b), false, false);
  return __uint_as_float(s[0]) + __uint_as_float(s[1]);
}
__device__ __forceinline__ float swp16_max(float a) {
  auto s = __builtin_amdgcn_permlane16_swap(__float_as_uint(a), __float_as_uint(a), false, false);
  return fmaxf(__uint_as_float(s[0]), __uint_as_float(s[1]));
}
template <int CTRL> __device__ __forceinline__ float tr_dpp(float lo, float hi, bool sel) {
  const float t1 = lo + dpp<CTRL>(lo), t2 = hi + dpp<CTRL>(hi);
  return sel ? t2 : t1;
}
__device__ __forceinline__ float tr8(float (&a)[8], int lane) {
#pragma unroll
  for (int i = 0; i < 4; ++i) a[i] = swp32_add(a[i], a[i + 4]);
#pragma unroll
  for (int i = 0; i < 2; ++i) a[i] = swp16_add(a[i], a[i + 2]);
  return tr_dpp<XOR8>(a[0], a[1], lane & 8);
}
__device__ __forceinline__ float max32(float v) {
  v = fmaxf(v, dpp<XOR1>(v)); v = fmaxf(v, dpp<XOR2>(v)); v = fmaxf(v, dpp<ROR4>(v)); v = fmaxf(v, dpp<XOR8>(v));
  return swp16_max(v);
}
__device__ __forceinline__ void pair_dot(const v4u& ra, const v4u& rb, unsigned xp, float (&a)[8]) {
#pragma unroll
  for (int c = 0; c < 4; ++c) {
    a[2 * c] = dot2(perm_lo(ra[c], rb[c]), xp, a[2 * c]);
    a[2 * c + 1] = dot2(perm_hi(ra[c], rb[c]), xp, a[2 * c + 1]);
  }
}
__device__ __forceinline__ unsigned san(float f) { const unsigned u = __float_as_uint(f); return u == SENT ? 0x7FC00000u : u; }
__device__ __forceinline__ v4u ldw(const GAS void* p) {
  return *(const GAS v4u*)p;
}
#define WG_BARRIER() do { asm volatile("s_waitcnt lgkmcnt(0)" ::: "memory"); __builtin_amdgcn_s_barrier(); asm volatile("" ::: "memory"); __builtin_amdgcn_sched_barrier(0); } while (0)
#define DRAIN() asm volatile("s_waitcnt vmcnt(0)" ::: "memory")

struct Lds {
  unsigned char ring[2][8][8192];
  unsigned P[4][2][128];
  unsigned PB[4][2][128];
  float mrg[4][512];
  float oa[64];
  unsigned q[256];
  float yb[DM];
  float qpart[6][64];
  float fin[8][32];
  unsigned ob[4][68];
  float mref[4][2];
  float gate, pad0[3];
  unsigned freed[2][8];
  unsigned full[2];
  unsigned cnt, qready;
  unsigned kdone[4];
  unsigned pready, acnt, oready, pad1;
  unsigned anchor[2];
};

#define PIN8(a) asm volatile("" : "+v"(a[0]), "+v"(a[1]), "+v"(a[2]), "+v"(a[3]), "+v"(a[4]), "+v"(a[5]), "+v"(a[6]), "+v"(a[7]))
#define LGKM0() asm volatile("s_waitcnt lgkmcnt(0)" ::: "memory")

__device__ __forceinline__ void wait_ge(volatile LAS unsigned* w, unsigned need) {
#pragma unroll 1
  for (unsigned spins = 0; spins < (1u << 22); ++spins) {
    if ((unsigned)__builtin_amdgcn_readfirstlane(*w) >= need) break;
    __builtin_amdgcn_s_sleep(1);
  }
  asm volatile("" ::: "memory");
}

template <int SLEEP>
__device__ __forceinline__ v4u poll16(__amdgpu_buffer_rsrc_t rs, int off) {
  v4u v = {0u, 0u, 0u, 0u};
#pragma unroll 1
  for (unsigned spins = 0; spins < SPIN_CAP; ++spins) {
    if (off != WS_END) {
      v = __builtin_amdgcn_raw_buffer_load_b128(rs, off, 0, 16);
      if (max(max(v[0], v[1]), max(v[2], v[3])) != SENT) off = WS_END;
    }
    if (__all(off == WS_END)) break;
    __builtin_amdgcn_s_sleep(SLEEP);
  }
  return v;
}

__device__ __forceinline__ void clear_slots(__amdgpu_buffer_rsrc_t rs, int gid, int lane) {
  const v4u sv = {SENT, SENT, SENT, SENT};
  __builtin_amdgcn_raw_buffer_store_b128(sv, rs, WS_PO + gid * 1024 + lane * 16, 0, 16);
  __builtin_amdgcn_raw_buffer_store_b128(sv, rs, WS_Y + gid * (DM * 4) + lane * 16, 0, 16);
  __builtin_amdgcn_raw_buffer_store_b128(sv, rs, lane < 48 ? WS_Y + gid * (DM * 4) + 1024 + lane * 16 : (lane == 48 ? WS_ST + gid * 128 : WS_END), 0, 16);
}
template <int D>
__device__ __forceinline__ void issue8(const char* src, int half, unsigned char* dst, const unsigned (&voff)[4], int n, int& pub, volatile LAS unsigned* full) {
#pragma unroll
  for (int g = 0; g < 8; ++g) {
    asm volatile("s_waitcnt vmcnt(%0)" ::"i"(D - 1) : "memory");
    if (g == (D - 1) % 8) { pub = max(pub, n - (D - 1) / 8); *full = pub; }
    const char* sp = src + (g >> 2) * half;
    if ((g & 3) == 0) __builtin_amdgcn_global_load_lds((const unsigned*)(sp + voff[0]), (unsigned*)(dst + (g & 4) * 1024), 16, 0, 2);
    if ((g & 3) == 1) __builtin_amdgcn_global_load_lds((const unsigned*)(sp + voff[1]), (unsigned*)(dst + (g & 4) * 1024), 16, 1024, 2);
    if ((g & 3) == 2) __builtin_amdgcn_global_load_lds((const unsigned*)(sp + voff[2]), (unsigned*)(dst + (g & 4) * 1024), 16, 2048, 2);
    if ((g & 3) == 3) __builtin_amdgcn_global_load_lds((const unsigned*)(sp + voff[3]), (unsigned*)(dst + (g & 4) * 1024), 16, 3072, 2);
  }
}
__device__ __forceinline__ unsigned mean_time(unsigned stamp, bool ok) {
  const unsigned now = (unsigned)__builtin_amdgcn_s_memrealtime();
  float d = ok ? (float)((now - stamp) & 0xffffu) : 0.f;
  d = swp32_add(d, d); d = swp16_add(d, d); d += dpp<XOR8>(d); d += dpp<XOR7>(d); d += dpp<XOR2>(d); d += dpp<XOR1>(d);
  return now - (unsigned)(d * (1.f / 64.f));
}
__device__ __forceinline__ void read_tr(const LAS unsigned char* cell, unsigned a0, bf16x8* f) {
#pragma unroll
  for (int kb = 0; kb < 2; ++kb)
#pragma unroll
    for (int t = 0; t < 4; ++t) {
      const s16x4 lo = __builtin_amdgcn_ds_read_tr16_b64_v4i16((LAS s16x4*)(cell + ((a0 ^ (t << 5)) + kb * 4096)));
      const s16x4 hi = __builtin_amdgcn_ds_read_tr16_b64_v4i16((LAS s16x4*)(cell + ((a0 ^ (t << 5)) + kb * 4096 + 512)));
      f[kb * 4 + t] = __builtin_bit_cast(bf16x8, __builtin_shufflevector(lo, hi, 0, 1, 2, 3, 4, 5, 6, 7));
    }
}
__device__ __forceinline__ f32x4 sel4(f32x4 a0, f32x4 a1, f32x4 a2, f32x4 a3, bool b0, bool b1) {
  f32x4 r;
#pragma unroll
  for (int e = 0; e < 4; ++e) { const float lo = b0 ? a1[e] : a0[e], hi = b0 ? a3[e] : a2[e]; r[e] = b1 ? hi : lo; }
  return r;
}

__global__ void __launch_bounds__(512, 2) fused_kernel(const u16* __restrict__ x, const u16* __restrict__ Kc, const u16* __restrict__ Vc,
    const u16* __restrict__ wq, const u16* __restrict__ wo, const u16* __restrict__ w1, const u16* __restrict__ w2,
    const u16* __restrict__ gw, unsigned char* ws, u16* out) {
  __shared__ __attribute__((aligned(16))) Lds sm;
  LAS Lds* L = (LAS Lds*)&sm;
  asm volatile("" ::"s"(x), "s"(Kc), "s"(Vc), "s"(wq), "s"(wo), "s"(w1), "s"(w2), "s"(gw), "s"(ws), "s"(out));
  const int tid = threadIdx.x, lane = tid & 63, wave = __builtin_amdgcn_readfirstlane(tid >> 6);
  const int blk = blockIdx.x, b = blk >> 6;
  const int kvh = ((blk >> 3) & 6) | (((blk >> 2) ^ (blk >> 4)) & 1), kh = (blk >> 3) & 1, qd = (blk + (blk >> 4)) & 3;
  const int s = kh * 4 + qd;
  const int j = kvh * 8 + s;
  const int gid = b * 64 + j;
  const __amdgpu_buffer_rsrc_t rs = __builtin_amdgcn_make_buffer_rsrc((void*)ws, 0, WS_END, 0x00020000);
  constexpr int NA = 64 - 4 * NB;
  const char* vbase = (const char*)Vc + ((size_t)((b * 8 + kvh) * SK + kh * 4096)) * (DH * 2) + qd * 128;
  if (wave < 2) {
    __builtin_amdgcn_s_setprio(3);
    if (tid < 30) L->freed[0][tid] = 0u;
    volatile LAS unsigned* full = &L->full[wave];
    const char* kb = (const char*)Kc + ((size_t)((b * 8 + kvh) * SK + s * 1024 + wave * 16)) * (DH * 2);
    unsigned vo[4];
#pragma unroll
    for (int g = 0; g < 4; ++g) vo[g] = (lane >> 5) * 512 + (((lane & 31) ^ (((2 * g + (lane >> 5)) & 7) << 1)) << 4);
    int pub = 0;
    const long vdelta = (const char*)Vc - (const char*)Kc - (32 - 4 * NB) * 16384;
    int half = 4096;
#pragma unroll 1
    for (int n = 0; n < 64; ++n) {
      if (n == 1) WG_BARRIER();
      const char* src = kb + (size_t)n * 16384 + (n < 32 ? 0 : vdelta);
      if (n >= NA) {
        const int m = n - NA;
        src = vbase + (size_t)((m / NB) * 1024 + (m % NB) * 128 + wave * 64) * 512;
        if (m == 0) {
          half = 16384;
#pragma unroll
          for (int g = 0; g < 4; ++g) vo[g] = (8 * g + (lane >> 3)) * 512 + (((lane & 7) ^ (((lane >> 3) & 2) | ((g & 1) << 2))) << 4) - g * 1024;
        }
      }
      int ld = -1;
      if (n >= 8) {
        const v2u av = *(const volatile LAS v2u*)L->anchor;
        const unsigned a0 = __builtin_amdgcn_readfirstlane(av[0]), an = __builtin_amdgcn_readfirstlane(av[1]);
        ld = (int)((an ? an + (n - 26) * TAU2 / 10 : a0 + n * TAU1 / 10) - (unsigned)__builtin_amdgcn_s_memrealtime());
        if (!an) { if (!a0) ld = -1; else if (ld <= 0) ld = min(ld + LAG1, -1); }
        volatile LAS unsigned* fr = &L->freed[wave][n & 7];
#pragma unroll 1
        for (unsigned spins = 0; spins < (1u << 22); ++spins) {
          if ((int)__builtin_amdgcn_readfirstlane(*fr) >= n - 7) break;
          DRAIN();
          pub = n; *full = pub;
          __builtin_amdgcn_s_sleep(1);
        }
        asm volatile("" ::: "memory");
      }
      unsigned char* dst = sm.ring[wave][n & 7];
      if (ld > 50) { if (ld > 100) issue8<5>(src, half, dst, vo, n, pub, full); else issue8<8>(src, half, dst, vo, n, pub, full); }
      else if (ld > 0) issue8<10>(src, half, dst, vo, n, pub, full);
      else if (ld > -60 && n < 54) issue8<13>(src, half, dst, vo, n, pub, full);
      else issue8<20>(src, half, dst, vo, n, pub, full);
    }
#pragma unroll
    for (int k = 2; k >= 0; --k) {
      asm volatile("s_waitcnt vmcnt(%0)" ::"i"(8 * k) : "memory");
      *full = max(pub, 64 - k);
    }
    WG_BARRIER();
  } else {
    const GAS unsigned* x32 = (const GAS unsigned*)(x + b * DM);
    const int wv = wave - 2;
    const u64 ep = __hip_atomic_load((GAS u64*)(ws + WS_EPOCH + b * 256), RLX_AGENT);
    {
      const int r = lane >> 3;
      const GAS char* wqp = (const GAS char*)wq + (size_t)(2 * r) * (QLD * 2) + (j * 64 + (lane & 7) * 8) * 2;
      v4u wr[10]; unsigned xp[5];
#pragma unroll
      for (int k = 0; k < 5; ++k) {
        const int pg = min(wv + 6 * k, 27);
        xp[k] = x32[8 * pg + r];
        wr[2 * k] = ldw(wqp + (size_t)(16 * pg) * (QLD * 2));
        wr[2 * k + 1] = ldw(wqp + (size_t)(16 * pg + 1) * (QLD * 2));
      }
      if (wave == 2) clear_slots(rs, gid, lane);
      __builtin_amdgcn_sched_barrier(0);
      __builtin_amdgcn_s_barrier();
      asm volatile("" ::: "memory");
      __builtin_amdgcn_sched_barrier(0);
      if (wv >= 4) xp[4] = 0u;
      float qa[8];
#pragma unroll
      for (int c = 0; c < 8; ++c) qa[c] = 0.f;
#pragma unroll
      for (int k = 0; k < 5; ++k) pair_dot(wr[2 * k], wr[2 * k + 1], xp[k], qa);
      L->qpart[wv][(lane & 7) * 8 + r] = tr8(qa, lane);
    }
    u64 tag = (ep + 1) << 16;
    asm volatile("" : "+v"(tag));
    LGKM0();
    unsigned arr = 0;
    if (lane == 0) arr = __hip_atomic_fetch_add(&L->cnt, 1u, __ATOMIC_RELAXED, __HIP_MEMORY_SCOPE_WORKGROUP);
    if (__builtin_amdgcn_readfirstlane(arr) == 5) {
      asm volatile("" ::: "memory");
      float qv = L->qpart[0][lane];
#pragma unroll
      for (int k = 1; k < 6; ++k) qv += L->qpart[k][lane];
      __hip_atomic_store((GAS u64*)(ws + WS_QG) + gid * 64 + lane, tag | (pkbf(qv, 0.f) & 0xffffu), RLX_AGENT);
    }
    if (wave >= 4) {
      const int cw = wave - 4, w = cw & 1, par = cw >> 1;
      volatile LAS unsigned* full = &L->full[w];
      volatile LAS unsigned* fr = L->freed[w];
      const int g4 = lane >> 4, c16 = lane & 15;
      const unsigned ka0 = c16 * 512 + ((g4 ^ ((c16 & 7) << 1)) << 4);
      const unsigned ta0 = (8 * g4 + (c16 >> 2)) * 128 + ((((c16 >> 3) & 1) | ((g4 & 1) << 1)) << 5) + (lane & 3) * 8;
      {
        bf16x8 qf[8];
#pragma unroll 1
        for (int gen = 0; gen < 4; ++gen) {
          bf16x8 kf[4][8];
#pragma unroll
          for (int h = 0; h < 4; ++h) {
            const int n = 8 * gen + 2 * h + par;
            wait_ge(full, n + 1);
            const LAS unsigned char* cell = L->ring[w][2 * h + par];
#pragma unroll
            for (int t = 0; t < 8; ++t) kf[h][t] = *(const LAS bf16x8*)(cell + ((ka0 ^ ((t & 3) << 6)) + (t >> 2) * 256));
            LGKM0(); fr[2 * h + par] = n + 1;
            PIN8(kf[h]);
          }
          if (gen == 0) {
            wait_ge(&L->qready, 1);
#pragma unroll
            for (int t = 0; t < 8; ++t) qf[t] = *(const LAS bf16x8*)(L->q + (lane & 1) * 128 + (4 * t + g4) * 4);
          }
          f32x4 sc[4];
#pragma unroll
          for (int h = 0; h < 4; ++h) sc[h] = f32x4{0.f, 0.f, 0.f, 0.f};
#pragma unroll
          for (int t = 0; t < 8; ++t)
#pragma unroll
            for (int h = 0; h < 4; ++h) sc[h] = __builtin_amdgcn_mfma_f32_16x16x32_bf16(kf[h][t], qf[t], sc[h], 0, 0, 0);
          const f32x4 sv = sel4(sc[0], sc[1], sc[2], sc[3], c16 & 2, c16 & 4);
          *(LAS v2u*)&L->P[gen][c16 & 1][(2 * (c16 & 6) + 2 * par + w) * 8 + 2 * g4] = v2u{pkbf(sv[0], sv[1]), pkbf(sv[2], sv[3])};
          LGKM0(); *(volatile LAS unsigned*)&L->kdone[cw] = gen + 1;
        }
      }
      v4u wf[32];
      const GAS u16* wop = (const GAS u16*)wo + (size_t)(2 * kvh * 256 + 64 * qd + 8 * g4) * DM + min(128 * cw + 8 * c16, DM - 8);
      f32x4 acc0 = {0.f, 0.f, 0.f, 0.f}, acc1 = {0.f, 0.f, 0.f, 0.f};
      float mprev0 = -INFINITY, mprev1 = -INFINITY;
      const int vrow = 4 * g4 + (c16 >> 2);
      const unsigned va0 = vrow * 512 + ((vrow & 7) << 5) + (lane & 3) * 8;
      auto astep = [&](int aa) {
        const int n = 32 - 4 * NB + 2 * aa + par, gen = aa >> 2;
        if ((aa & 3) == 0 || aa == 2 * NB) {
          wait_ge(&L->pready, gen + 1);
          const float m0 = L->mref[gen][0], m1 = L->mref[gen][1];
          const float a0 = __builtin_amdgcn_exp2f(mprev0 - m0), a1 = __builtin_amdgcn_exp2f(mprev1 - m1);
          mprev0 = m0; mprev1 = m1;
          acc0 *= f32x4{a0, a1, a0, a1}; acc1 *= f32x4{a0, a1, a0, a1};
        }
        const v2u pv = *(const LAS v2u*)&L->P[gen][lane & 1][(4 * (aa & 3) + 2 * par + w) * 8 + 2 * g4];
        s16x4 pa[8];
#pragma unroll
        for (int v = 0; v < 8; ++v) { const bool on = (c16 >> 1) == v; pa[v] = __builtin_bit_cast(s16x4, v2u{on ? pv[0] : 0u, on ? pv[1] : 0u}); }
        PIN8(pa);
        wait_ge(full, n + 1);
        const LAS unsigned char* cell = L->ring[w][n & 7];
#pragma unroll
        for (int v = 0; v < 8; ++v) {
          acc0 = __builtin_amdgcn_mfma_f32_16x16x16bf16_1k(pa[v], __builtin_amdgcn_ds_read_tr16_b64_v4i16((LAS s16x4*)(cell + (va0 ^ (v << 5)))), acc0, 0, 0, 0);
          acc1 = __builtin_amdgcn_mfma_f32_16x16x16bf16_1k(pa[v], __builtin_amdgcn_ds_read_tr16_b64_v4i16((LAS s16x4*)(cell + ((va0 ^ (v << 5)) + 256))), acc1, 0, 0, 0);
        }
        asm volatile("" : "+v"(acc0), "+v"(acc1));
        LGKM0(); fr[n & 7] = n + 1;
      };
      constexpr int NWS = (32 + WOB - 1) / WOB;
#pragma unroll
      for (int hb = 0; hb < NWS; ++hb) {
#pragma unroll
        for (int i = WOB * hb; i < WOB * hb + WOB && i < 32; ++i) wf[i] = ldw(wop + (size_t)((i >> 4) * 256 + ((i >> 3) & 1) * 32 + (i & 7)) * DM);
        __builtin_amdgcn_sched_barrier(0);
        astep(2 * NB + hb);
      }
#pragma unroll 1
      for (int aa = 2 * NB + NWS; aa < 16; ++aa) astep(aa);
#pragma unroll
      for (int r = 0; r < 4; ++r) {
        L->mrg[cw][(r & 1) * 256 + (2 * g4 + (r >> 1)) * 16 + c16] = acc0[r];
        L->mrg[cw][(r & 1) * 256 + 128 + (2 * g4 + (r >> 1)) * 16 + c16] = acc1[r];
      }
      LGKM0();
      if (lane == 0) __hip_atomic_fetch_add(&L->acnt, 1u, __ATOMIC_RELAXED, __HIP_MEMORY_SCOPE_WORKGROUP);
      __builtin_amdgcn_sched_barrier(0);
#pragma unroll
      for (int kb = 0; kb < 4; ++kb) {
        v4u tf[8];
#pragma unroll
        for (int e = 0; e < 8; ++e)
#pragma unroll
          for (int d = 0; d < 4; ++d)
            tf[e][d] = (e & 1) ? perm_hi(wf[8 * kb + 2 * d][e >> 1], wf[8 * kb + 2 * d + 1][e >> 1]) : perm_lo(wf[8 * kb + 2 * d][e >> 1], wf[8 * kb + 2 * d + 1][e >> 1]);
        PIN8(tf);
#pragma unroll
        for (int e = 0; e < 8; ++e) wf[8 * kb + e] = tf[e];
      }
      f32x4 acc[4];
#pragma unroll
      for (int t = 0; t < 4; ++t) acc[t] = f32x4{0.f, 0.f, 0.f, 0.f};
      f32x4 o = {0.f, 0.f, 0.f, 0.f};
      wait_ge(&L->pready, 5);
      LAS v2u* obw = (LAS v2u*)&L->ob[cw][(c16 & 1) * 32 + ((c16 >> 1) & 3) * 8 + 2 * g4];
#pragma unroll 1
      for (int mm = 0; mm < 2 * NB; ++mm) {
        const int m = 2 * mm + par, n = NA + m;
        const LAS unsigned* pp = &L->PB[m / NB][lane & 1][(m % NB) * 64 + w * 32 + 4 * g4];
        const bf16x8 p0 = *(const LAS bf16x8*)pp, p1 = *(const LAS bf16x8*)(pp + 16);
        wait_ge(full, n + 1);
        bf16x8 vf[8];
        read_tr(L->ring[w][n & 7], ta0, vf);
#pragma unroll
        for (int t = 0; t < 4; ++t) acc[t] = __builtin_amdgcn_mfma_f32_16x16x32_bf16(vf[t], p0, acc[t], 0, 0, 0);
#pragma unroll
        for (int t = 0; t < 4; ++t) acc[t] = __builtin_amdgcn_mfma_f32_16x16x32_bf16(vf[4 + t], p1, acc[t], 0, 0, 0);
        asm volatile("" : "+v"(acc[0]), "+v"(acc[1]), "+v"(acc[2]), "+v"(acc[3]));
      }
      if (cw == 0) {
        wait_ge(&L->oready, 1);
        if ((c16 & 1) == kh) o = *(const LAS f32x4*)&L->oa[((c16 >> 1) & 3) * 16 + 4 * g4];
      }
      o += sel4(acc[0], acc[1], acc[2], acc[3], c16 & 2, c16 & 4);
      *obw = v2u{pkbf(o[0], o[1]), pkbf(o[2], o[3])};
      const int col = 128 * cw + 8 * c16 + 4 * (g4 & 1);
      const bool st_on = g4 < 2 && col < DM;
      const int yoff = st_on ? WS_Y + gid * (DM * 4) + col * 4 : WS_END;
      const LAS float* ybp = &L->yb[st_on ? col : 0];
      const LAS unsigned* obr = &L->ob[c16 & 3][4 * g4];
      WG_BARRIER();
      bf16x8 of[4];
#pragma unroll
      for (int kb = 0; kb < 4; ++kb) of[kb] = *(const LAS bf16x8*)(obr + 16 * kb);
      const f32x4 ybv = *(const LAS f32x4*)ybp;
      const float g = L->gate;
      __builtin_amdgcn_sched_barrier(0);
      f32x4 ya[8];
#pragma unroll
      for (int e = 0; e < 8; ++e) ya[e] = f32x4{0.f, 0.f, 0.f, 0.f};
#pragma unroll
      for (int kb = 0; kb < 4; ++kb) {
#pragma unroll
        for (int e = 0; e < 8; ++e) ya[e] = __builtin_amdgcn_mfma_f32_16x16x32_bf16(of[kb], __builtin_bit_cast(bf16x8, wf[8 * kb + e]), ya[e], 0, 0, 0);
        __builtin_amdgcn_sched_barrier(0);
      }
      v4u yv;
#pragma unroll
      for (int r = 0; r < 4; ++r) {
        const float lo = (ya[r][0] + ya[r][1]) + (ya[r][2] + ya[r][3]), hi = (ya[4 + r][0] + ya[4 + r][1]) + (ya[4 + r][2] + ya[4 + r][3]);
        yv[r] = san(fmaf(g, (g4 & 1) ? hi : lo, ybv[r]));
      }
      __builtin_amdgcn_raw_buffer_store_b128(yv, rs, yoff, 0, 16);
    } else {
      if (wave == 2) {
        {
          const unsigned thi = (unsigned)(tag >> 32), tlo = (unsigned)tag;
          int off[4];
#pragma unroll
          for (int k = 0; k < 4; ++k) off[k] = WS_QG + (gid & ~7) * 512 + k * 1024 + lane * 16;
          __builtin_amdgcn_s_sleep(80);
#pragma unroll 1
          for (unsigned spins = 0; spins < SPIN_CAP; ++spins) {
            v4u g[4];
            bool ok = true;
#pragma unroll
            for (int k = 0; k < 4; ++k) g[k] = __builtin_amdgcn_raw_buffer_load_b128(rs, off[k], 0, 16);
#pragma unroll
            for (int k = 0; k < 4; ++k) {
              if (off[k] != WS_END && g[k][1] == thi && g[k][3] == thi && (g[k][0] & 0xffff0000u) == tlo && (g[k][2] & 0xffff0000u) == tlo) {
                L->q[k * 64 + lane] = perm_lo(g[k][0], g[k][2]);
                off[k] = WS_END;
              }
              ok &= off[k] == WS_END;
            }
            if (__all(ok)) break;
            __builtin_amdgcn_s_sleep(16);
          }
          LGKM0();
          *(volatile LAS unsigned*)&L->qready = 1u;
        }
        constexpr float C2 = 0.0625f * 1.4426950408889634f;
        float mrun = -INFINITY, lrun = 0.f;
#pragma unroll 1
        for (int gen = 0; gen < 4; ++gen) {
#pragma unroll
          for (int c = 0; c < 4; ++c) wait_ge(&L->kdone[c], gen + 1);
          v4u g = *(const LAS v4u*)&L->P[gen][0][lane * 4];
          if (gen == 0) __builtin_amdgcn_raw_buffer_store_b128(g, rs, WS_S + gid * 1024 + lane * 16, 0, 16);
          if (gen == 2) {
            DRAIN();
            if (lane == 0) __hip_atomic_store((GAS u64*)(ws + WS_RDY) + gid, tag | ((unsigned)__builtin_amdgcn_s_memrealtime() & 0xffffu), RLX_AGENT);
          }
          float cm = -INFINITY;
#pragma unroll
          for (int e = 0; e < 4; ++e) cm = fmaxf(cm, fmaxf(bflo(g[e]), bfhi(g[e])));
          const float mn = fmaxf(mrun, max32(cm) * C2);
          float ls = 0.f;
#pragma unroll
          for (int e = 0; e < 4; ++e) {
            const float pl = __builtin_amdgcn_exp2f(fmaf(bflo(g[e]), C2, -mn)), ph = __builtin_amdgcn_exp2f(fmaf(bfhi(g[e]), C2, -mn));
            ls += pl + ph;
            g[e] = pkbf(pl, ph);
          }
          *(LAS v4u*)&L->P[gen][0][lane * 4] = g;
          lrun = fmaf(lrun, __builtin_amdgcn_exp2f(mrun - mn), ls);
          mrun = mn;
          if ((lane & 31) == 0) L->mref[gen][lane >> 5] = mn;
          LGKM0();
          *(volatile LAS unsigned*)&L->pready = gen + 1;
        }
        lrun += dpp<XOR1>(lrun); lrun += dpp<XOR2>(lrun); lrun += dpp<ROR4>(lrun); lrun += dpp<XOR8>(lrun); lrun = swp16_add(lrun, lrun);
        if ((lane & 31) == 0) __builtin_amdgcn_raw_buffer_store_b64(v2u{san(mrun), san(lrun)}, rs, WS_ST + gid * 128 + (lane >> 5) * 8, 0, 16);
        u64 gv;
#pragma unroll 1
        for (unsigned spins = 0; spins < SPIN_CAP; ++spins) {
          gv = __hip_atomic_load((GAS u64*)(ws + WS_RDY) + b * 64 + lane, RLX_AGENT);
          if (__all((gv & ~0xffffull) == tag)) break;
          __builtin_amdgcn_s_sleep(32);
        }
        *(volatile LAS unsigned*)&L->anchor[1] = (mean_time((unsigned)gv, true) + TOFF2 - (qd == 3 ? Q3LEAD : 0)) | 1u;
        if (j == 0 && lane == 0) __hip_atomic_store((GAS u64*)(ws + WS_EPOCH + b * 256), ep + 1, RLX_AGENT);
        asm volatile("" ::: "memory");
        v4u h[4];
#pragma unroll
        for (int k = 0; k < 4; ++k) h[k] = __builtin_amdgcn_raw_buffer_load_b128(rs, WS_S + ((gid & ~3) + k) * 1024 + lane * 16, 0, 16);
        v2u sv;
#pragma unroll 1
        for (unsigned spins = 0; spins < SPIN_CAP; ++spins) {
          __builtin_amdgcn_s_sleep(127);
          sv = __builtin_amdgcn_raw_buffer_load_b64(rs, WS_ST + ((gid & ~7) + (lane & 7)) * 128 + (lane >> 5) * 8, 0, 16);
          if (__all(sv[0] != SENT && sv[1] != SENT)) break;
        }
        const float mp = __uint_as_float(sv[0]);
        float M = fmaxf(mp, dpp<XOR1>(mp)); M = fmaxf(M, dpp<XOR2>(M)); M = fmaxf(M, dpp<XOR7>(M));
        float Ls = __uint_as_float(sv[1]) * __builtin_amdgcn_exp2f(mp - M);
        Ls += dpp<XOR1>(Ls); Ls += dpp<XOR2>(Ls); Ls += dpp<XOR7>(Ls);
        const float inv = 1.f / Ls;
#pragma unroll
        for (int k = 0; k < 4; ++k) {
#pragma unroll
          for (int e = 0; e < 4; ++e)
            h[k][e] = pkbf(inv * __builtin_amdgcn_exp2f(fmaf(bflo(h[k][e]), C2, -M)), inv * __builtin_amdgcn_exp2f(fmaf(bfhi(h[k][e]), C2, -M)));
          *(LAS v4u*)&L->PB[k][0][lane * 4] = h[k];
        }
        LGKM0();
        *(volatile LAS unsigned*)&L->pready = 5u;
        const float as = inv * __builtin_amdgcn_exp2f(mrun - M);
        wait_ge(&L->acnt, 4);
        f32x4 o0 = *(const LAS f32x4*)(L->mrg[0] + lane * 8), o1 = *(const LAS f32x4*)(L->mrg[0] + lane * 8 + 4);
#pragma unroll
        for (int ww = 1; ww < 4; ++ww) { o0 += *(const LAS f32x4*)(L->mrg[ww] + lane * 8); o1 += *(const LAS f32x4*)(L->mrg[ww] + lane * 8 + 4); }
        o0 *= as; o1 *= as;
        v4u ov = {pkbf(o0[0], o0[1]), pkbf(o0[2], o0[3]), pkbf(o1[0], o1[1]), pkbf(o1[2], o1[3])};
#pragma unroll
        for (int k = 0; k < 4; ++k) ov[k] = ov[k] == SENT ? 0x7FC07FC0u : ov[k];
        __builtin_amdgcn_raw_buffer_store_b128(ov, rs, WS_PO + gid * 1024 + lane * 16, 0, 16);
        __builtin_amdgcn_s_sleep(20);
        ov = poll16<2>(rs, WS_PO + ((gid & ~7) + (lane >> 3)) * 1024 + s * 128 + (lane & 7) * 16);
        float oe[8];
#pragma unroll
        for (int k = 0; k < 4; ++k) { oe[2 * k] = bflo(ov[k]); oe[2 * k + 1] = bfhi(ov[k]); }
        L->oa[8 * (lane & 7) + (lane >> 3)] = tr8(oe, lane);
        LGKM0();
        *(volatile LAS unsigned*)&L->oready = 1u;
      } else {
        wait_ge(&L->full[0], 3);
        if (lane == 0) __hip_atomic_store((GAS u64*)(ws + WS_TS) + gid, tag | ((unsigned)__builtin_amdgcn_s_memrealtime() & 0xffffu), RLX_AGENT);
#pragma unroll 1
        for (int k = 0; k < 4; ++k) {
          wait_ge(&L->full[0], 8 + 3 * k);
          const u64 tv = __hip_atomic_load((GAS u64*)(ws + WS_TS) + b * 64 + lane, RLX_AGENT);
          const bool ok = (tv & ~0xffffull) == tag;
          *(volatile LAS unsigned*)&L->anchor[0] = (mean_time((unsigned)tv, ok) + TOFF1 - (qd == 3 ? Q3LEAD : 0)) | 1u;
          if (__all(ok)) break;
        }
        const int cl = lane < 56 ? lane : 55;
        float z = 0.f;
#pragma unroll
        for (int k = 0; k < 4; ++k) {
          const int idx = lane + 64 * k;
          const bool in = idx < DM / 2;
          const unsigned xv = x32[in ? idx : 0], gv = ((const GAS unsigned*)gw)[in ? idx : 0];
          const float t = rbf(bflo(xv) * bflo(gv)) + rbf(bfhi(xv) * bfhi(gv));
          z += in ? t : 0.f;
        }
        z = swp32_add(z, z); z = swp16_add(z, z); z += dpp<XOR8>(z); z += dpp<XOR7>(z); z += dpp<XOR2>(z); z += dpp<XOR1>(z);
        const float g = rbf(1.f / (1.f + __expf(-rbf(z))));
        if (lane == 0) L->gate = g;
        float yb[8];
#pragma unroll
        for (int c = 0; c < 8; ++c) yb[c] = 0.f;
        if (s != 3) {
          const int u0 = (s == 7 ? 48 + kvh : kvh * 6 + 2 * qd + kh) * 32, rr = lane >> 2;
          float ha[8];
#pragma unroll
          for (int c = 0; c < 8; ++c) ha[c] = 0.f;
          const GAS u16* w1p = (const GAS u16*)w1 + (size_t)(2 * rr) * DFF + u0 + (lane & 3) * 8;
#pragma unroll 1
          for (int h = 0; h < 14; ++h) pair_dot(ldw(w1p + (size_t)(h * 32) * DFF), ldw(w1p + (size_t)(h * 32 + 1) * DFF), x32[h * 16 + rr], ha);
          float hv = tr8(ha, lane);
          hv += dpp<SHR4>(hv);
          hv = rbf(hv);
          hv = hv <= 0.f ? 0.f : hv;
#pragma unroll 1
          for (int c4 = 0; c4 < 32 / MB2; ++c4) {
            v4u w2r[MB2];
#pragma unroll
            for (int u = 0; u < MB2; ++u) w2r[u] = ldw((const GAS u16*)w2 + (size_t)(u0 + c4 * MB2 + u) * DM + cl * 8);
#pragma unroll
            for (int u = 0; u < MB2; ++u) {
              const int un = c4 * MB2 + u;
              const float hu = __builtin_bit_cast(float, __builtin_amdgcn_readlane(__builtin_bit_cast(int, hv), (un & 7) * 8 + 4 + (un >> 3)));
#pragma unroll
              for (int c = 0; c < 4; ++c) { yb[2 * c] = fmaf(hu, bflo(w2r[u][c]), yb[2 * c]); yb[2 * c + 1] = fmaf(hu, bfhi(w2r[u][c]), yb[2 * c + 1]); }
            }
          }
        }
        const float omg = rbf(1.f - g);
        if (lane < 56) {
          *(LAS f32x4*)(L->yb + lane * 8) = f32x4{omg * yb[0], omg * yb[1], omg * yb[2], omg * yb[3]};
          *(LAS f32x4*)(L->yb + lane * 8 + 4) = f32x4{omg * yb[4], omg * yb[5], omg * yb[6], omg * yb[7]};
        }
      }
      WG_BARRIER();
    }
  }
  if (j < 14) {
    if (wave < 4) __builtin_amdgcn_s_sleep(14);
    __builtin_amdgcn_s_sleep(8);
    const v4u y = poll16<1>(rs, WS_Y + (b * 64 + wave * 8 + (lane >> 3)) * (DM * 4) + j * 128 + (lane & 7) * 16);
    const float r0 = swp32_add(__uint_as_float(y[0]), __uint_as_float(y[2])), r1 = swp32_add(__uint_as_float(y[1]), __uint_as_float(y[3]));
    float r = swp16_add(r0, r1);
    r += dpp<XOR8>(r);
    if (!(lane & 8)) L->fin[wave][(lane & 7) * 4 + (lane >> 4)] = r;
    WG_BARRIER();
    if (tid < 32) {
      const float t = ((L->fin[0][tid] + L->fin[1][tid]) + (L->fin[2][tid] + L->fin[3][tid])) + ((L->fin[4][tid] + L->fin[5][tid]) + (L->fin[6][tid] + L->fin[7][tid]));
      out[b * DM + j * 32 + tid] = (u16)pkbf(t, 0.f);
    }
  }
}

extern "C" void kernel_launch(void* const* d_in, const int* in_sizes, int n_in, void* d_out, int out_size, void* d_ws, size_t ws_size, hipStream_t stream) {
  fused_kernel<<<256, 512, 0, stream>>>((const u16*)d_in[0], (const u16*)d_in[1], (const u16*)d_in[2], (const u16*)d_in[3], (const u16*)d_in[4],
                                        (const u16*)d_in[6], (const u16*)d_in[7], (const u16*)d_in[8], (unsigned char*)d_ws, (u16*)d_out);
}
```
